```python
import torch

def _softmax(x, axis=-1):
    return torch.softmax(x, dim=axis)

def _gelu(x):
    return torch.nn.functional.gelu(x, approximate='tanh')

def _silu(x):
    return torch.nn.functional.silu(x)

def _sigmoid(x):
    return torch.sigmoid(x)

def _layernorm(x, axis=-1, eps=1e-05):
    m = x.mean(dim=axis, keepdim=True)
    v = x.var(dim=axis, keepdim=True, unbiased=False)
    return (x - m) / torch.sqrt(v + eps)

def _scatter_add(tgt, idx, src, axis=0):
    return tgt.clone().index_add_(axis, idx, src)

def _segment_sum(src, seg, n):
    out = torch.zeros((n,) + src.shape[1:], dtype=src.dtype)
    return out.index_add_(0, seg, src)

def _pad(x, pw):
    flat = tuple((v for pair in reversed(pw) for v in pair))
    return torch.nn.functional.pad(x, flat)
kernel_name = 'sequential_preLN__memory_aug_recurrent_mem_tr__glu_gated_swiglu__prompt'

def setup_inputs():

    _w = lambda a: (lambda t: t.to(torch.bfloat16) if t.is_floating_point() else t)(torch.as_tensor(a))

    def _A_setup():
        B, S, Dm = (1, 2048, 640)
        H, Dh = (32, 128)
        M, seg = (8, 32)
        seg = min(seg, S)
        S = S // seg * seg
        return {'x': _w((lambda *s: torch.randn(*s, dtype=torch.bfloat16) * 0.05)(B, S, Dm)), 'mem0': _w((lambda *s: torch.randn(*s, dtype=torch.bfloat16) * 0.2)(B, M, Dm)), 'Wq': _w((lambda *s: torch.randn(*s, dtype=torch.bfloat16) * 1.6)(Dm, H, Dh) * Dm ** (-0.5)), 'Wk': _w((lambda *s: torch.randn(*s, dtype=torch.bfloat16) * 1.6)(Dm, H, Dh) * Dm ** (-0.5)), 'Wv': _w((lambda *s: torch.randn(*s, dtype=torch.bfloat16) * 0.2)(Dm, H, Dh) * Dm ** (-0.5)), 'Wo': _w((lambda *s: torch.randn(*s, dtype=torch.bfloat16) * 0.2)(H, Dh, Dm) * (H * Dh) ** (-0.5)), 'seg_len': seg}
    _A_in = _A_setup()

    def _B_setup():
        B, S, D, F = (1, 2048, 640, 1728)
        return {'x': _w((lambda *s: torch.randn(*s, dtype=torch.bfloat16) * 0.05)(B, S, D)), 'w1': _w((lambda *s: torch.randn(*s, dtype=torch.bfloat16) * 0.2)(D, F) * (2.0 / D) ** 0.5), 'w3': _w((lambda *s: torch.randn(*s, dtype=torch.bfloat16) * 0.2)(D, F) * (2.0 / D) ** 0.5), 'w2': _w((lambda *s: torch.randn(*s, dtype=torch.bfloat16) * 0.2)(F, D) * (2.0 / F) ** 0.5)}
    _B_in = _B_setup()
    return {'A_x': _A_in['x'], 'A_mem0': _A_in['mem0'], 'A_Wq': _A_in['Wq'], 'A_Wk': _A_in['Wk'], 'A_Wv': _A_in['Wv'], 'A_Wo': _A_in['Wo'], 'A_seg_len': _A_in['seg_len'], 'B_x': _B_in['x'], 'B_w1': _B_in['w1'], 'B_w3': _B_in['w3'], 'B_w2': _B_in['w2']}

def reference(A_x, A_mem0, A_Wq, A_Wk, A_Wv, A_Wo, A_seg_len, B_x, B_w1, B_w3, B_w2):

    def _A_ref(x, mem0, Wq, Wk, Wv, Wo, seg_len):
        B, S, Dm = x.shape
        H, Dh = (Wq.shape[1], Wq.shape[2])
        M = mem0.shape[1]
        scale = Dh ** (-0.5)
        n_seg = S // seg_len
        L = M + seg_len
        idx = torch.arange(L)
        mask = (idx[:, None] < M) | (idx[:, None] >= idx[None, :])
        mem = mem0
        outs = []
        for s in range(n_seg):
            xs = x[:, s * seg_len:(s + 1) * seg_len, :].clone()
            h = torch.concatenate([mem, xs], axis=1)
            q = torch.einsum('btd,dhe->bhte', h, Wq)
            k = torch.einsum('btd,dhe->bhte', h, Wk)
            v = torch.einsum('btd,dhe->bhte', h, Wv)
            scores = torch.einsum('bhqe,bhke->bhqk', q, k) * scale
            scores = torch.where(mask[None, None], scores, -1000000000.0)
            p = _softmax(scores, axis=-1)
            o = torch.einsum('bhqk,bhke->bhqe', p, v)
            o = torch.einsum('bhte,hed->btd', o, Wo)
            mem = o[:, :M, :].clone()
            outs.append(o[:, M:, :])
        return torch.concatenate(outs, axis=1)

    def _B_ref(x, w1, w3, w2):
        gate = torch.einsum('bsd,df->bsf', x, w1)
        up = torch.einsum('bsd,df->bsf', x, w3)
        h = _silu(gate) * up
        out = torch.einsum('bsf,fd->bsd', h, w2)
        return out
    x = A_x
    h = x + _A_ref(_layernorm(x, axis=-1), A_mem0, A_Wq, A_Wk, A_Wv, A_Wo, A_seg_len)
    h2 = h + _B_ref(_layernorm(h, axis=-1), B_w1, B_w3, B_w2)
    return h2
```

```cpp
#include <hip/hip_runtime.h>
#include <hip/hip_bf16.h>
typedef unsigned short u16;
using bf16x8 = __attribute__((ext_vector_type(8))) short;
using f32x4 = __attribute__((ext_vector_type(4))) float;
__device__ __forceinline__ float bf2f(u16 x) { return __uint_as_float(((unsigned)x) << 16); }
__device__ __forceinline__ u16 f2bf(float x) { unsigned u = __float_as_uint(x); u += 0x7fffu + ((u >> 16) & 1u); return (u16)(u >> 16); }

constexpr int S = 2048, D = 640, H = 32, DH = 128, HD = 4096, MM = 8, SEG = 32, NSEG = 64, F = 1728;

__global__ void ln_kernel(const u16* in, u16* out) {
    int row = blockIdx.x, t = threadIdx.x;
    float v[10]; float s = 0;
    for (int i = 0; i < 10; ++i) { v[i] = bf2f(in[row * D + t + 64 * i]); s += v[i]; }
    for (int m = 32; m; m >>= 1) s += __shfl_xor(s, m);
    float mean = s / D; float q = 0;
    for (int i = 0; i < 10; ++i) { float d = v[i] - mean; q += d * d; }
    for (int m = 32; m; m >>= 1) q += __shfl_xor(q, m);
    float r = 1.0f / sqrtf(q / D + 1e-5f);
    for (int i = 0; i < 10; ++i) out[row * D + t + 64 * i] = f2bf((v[i] - mean) * r);
}
__global__ void __launch_bounds__(256) gemm_kernel(const u16* A, int lda, const u16* B, int ldb, u16* C, int ldc, const u16* R, int M, int K) {
    __shared__ u16 sa[64][40], sb[64][40];
    int bm = blockIdx.y * 64, bn = blockIdx.x * 64, t = threadIdx.x, w = t >> 6, l = t & 63;
    f32x4 acc[4] = {};
    for (int k0 = 0; k0 < K; k0 += 32) {
        for (int i = t; i < 64 * 32; i += 256) { int r = i >> 5, c = i & 31; sa[r][c] = (bm + r < M) ? A[(size_t)(bm + r) * lda + k0 + c] : (u16)0; }
        for (int i = t; i < 64 * 32; i += 256) { int k = i >> 6, n = i & 63; sb[n][k] = B[(size_t)(k0 + k) * ldb + bn + n]; }
        __syncthreads();
        bf16x8 a; for (int j = 0; j < 8; ++j) a[j] = sa[w * 16 + (l & 15)][8 * (l >> 4) + j];
        for (int n = 0; n < 4; ++n) { bf16x8 b; for (int j = 0; j < 8; ++j) b[j] = sb[n * 16 + (l & 15)][8 * (l >> 4) + j];
            acc[n] = __builtin_amdgcn_mfma_f32_16x16x32_bf16(a, b, acc[n], 0, 0, 0); }
        __syncthreads();
    }
    for (int n = 0; n < 4; ++n) for (int j = 0; j < 4; ++j) {
        int r = bm + w * 16 + (l >> 4) * 4 + j, c = bn + n * 16 + (l & 15);
        if (r < M) { float v = acc[n][j]; if (R) v = bf2f(f2bf(v)) + bf2f(R[(size_t)r * ldc + c]); C[(size_t)r * ldc + c] = f2bf(v); }
    }
}
__global__ void __launch_bounds__(256) attn_kernel(const u16* Qm, const u16* Km, const u16* Vm, const u16* Qx, const u16* Kx, const u16* Vx, u16* Om, u16* Ox, int seg0, int memmode) {
    int h = blockIdx.x, seg = seg0 + blockIdx.y, t = threadIdx.x;
    __shared__ float q[32][129], k[40][129], v[40][129], p[32][41];
    int nq = memmode ? 8 : 32;
    for (int i = t; i < nq * 128; i += 256) { int r = i >> 7, e = i & 127; q[r][e] = bf2f(memmode ? Qm[(size_t)(seg * 8 + r) * HD + h * 128 + e] : Qx[(size_t)(seg * 32 + r) * HD + h * 128 + e]); }
    for (int i = t; i < 40 * 128; i += 256) { int r = i >> 7, e = i & 127;
        k[r][e] = bf2f(r < 8 ? Km[(size_t)(seg * 8 + r) * HD + h * 128 + e] : Kx[(size_t)(seg * 32 + r - 8) * HD + h * 128 + e]);
        v[r][e] = bf2f(r < 8 ? Vm[(size_t)(seg * 8 + r) * HD + h * 128 + e] : Vx[(size_t)(seg * 32 + r - 8) * HD + h * 128 + e]); }
    __syncthreads();
    for (int i = t; i < nq * 40; i += 256) { int r = i / 40, c = i % 40; float s = 0; for (int e = 0; e < 128; ++e) s += q[r][e] * k[c][e];
        s *= 0.08838834764831845f; if (!memmode && c > r + 8) s = -1e9f; p[r][c] = s; }
    __syncthreads();
    if (t < nq) { float m = -3e38f; for (int c = 0; c < 40; ++c) m = fmaxf(m, p[t][c]); float s = 0; for (int c = 0; c < 40; ++c) { float e = __expf(p[t][c] - m); p[t][c] = e; s += e; } float r = 1.0f / s; for (int c = 0; c < 40; ++c) p[t][c] = bf2f(f2bf(p[t][c] * r)); }
    __syncthreads();
    for (int i = t; i < nq * 128; i += 256) { int r = i >> 7, e = i & 127; float s = 0; for (int c = 0; c < 40; ++c) s += p[r][c] * v[c][e];
        if (memmode) Om[(size_t)r * HD + h * 128 + e] = f2bf(s); else Ox[(size_t)(seg * 32 + r) * HD + h * 128 + e] = f2bf(s); }
}
__global__ void swiglu_kernel(const u16* g, const u16* u, u16* o, int n) {
    int i = blockIdx.x * 256 + threadIdx.x; if (i < n) { float a = bf2f(g[i]); float s = a / (1.0f + __expf(-a)); o[i] = f2bf(bf2f(f2bf(s)) * bf2f(u[i])); }
}

extern "C" void kernel_launch(void* const* d_in, const int* in_sizes, int n_in, void* d_out, int out_size, void* d_ws, size_t ws_size, hipStream_t stream) {
    const u16* x = (const u16*)d_in[0]; const u16* mem0 = (const u16*)d_in[1];
    const u16* Wq = (const u16*)d_in[2]; const u16* Wk = (const u16*)d_in[3]; const u16* Wv = (const u16*)d_in[4]; const u16* Wo = (const u16*)d_in[5];
    const u16* w1 = (const u16*)d_in[8]; const u16* w3 = (const u16*)d_in[9]; const u16* w2 = (const u16*)d_in[10];
    u16* out = (u16*)d_out;
    u16* p = (u16*)d_ws;
    u16* xn = p; p += S * D;
    u16* Qx = p; p += (size_t)S * HD; u16* Kx = p; p += (size_t)S * HD; u16* Vx = p; p += (size_t)S * HD; u16* Ox = p; p += (size_t)S * HD;
    u16* Qm = p; p += 512 * HD; u16* Km = p; p += 512 * HD; u16* Vm = p; p += 512 * HD; u16* Om = p; p += 8 * HD;
    u16* mem = p; p += 65 * 8 * D; u16* hbuf = p; p += S * D; u16* hn = p; p += S * D;
    u16* g = p; p += S * F; u16* u = p; p += S * F; u16* act = p; p += S * F;
    ln_kernel<<<S, 64, 0, stream>>>(x, xn);
    gemm_kernel<<<dim3(HD / 64, S / 64), 256, 0, stream>>>(xn, D, Wq, HD, Qx, HD, nullptr, S, D);
    gemm_kernel<<<dim3(HD / 64, S / 64), 256, 0, stream>>>(xn, D, Wk, HD, Kx, HD, nullptr, S, D);
    gemm_kernel<<<dim3(HD / 64, S / 64), 256, 0, stream>>>(xn, D, Wv, HD, Vx, HD, nullptr, S, D);
    for (int s = 0; s < NSEG; ++s) {
        const u16* m = s == 0 ? mem0 : mem + (size_t)s * 8 * D;
        gemm_kernel<<<dim3(HD / 64, 1), 256, 0, stream>>>(m, D, Wk, HD, Km + (size_t)s * 8 * HD, HD, nullptr, 8, D);
        gemm_kernel<<<dim3(HD / 64, 1), 256, 0, stream>>>(m, D, Wv, HD, Vm + (size_t)s * 8 * HD, HD, nullptr, 8, D);
        if (s == NSEG - 1) break;
        gemm_kernel<<<dim3(HD / 64, 1), 256, 0, stream>>>(m, D, Wq, HD, Qm + (size_t)s * 8 * HD, HD, nullptr, 8, D);
        attn_kernel<<<dim3(H, 1), 256, 0, stream>>>(Qm, Km, Vm, Qx, Kx, Vx, Om, Ox, s, 1);
        gemm_kernel<<<dim3(D / 64, 1), 256, 0, stream>>>(Om, HD, Wo, D, mem + (size_t)(s + 1) * 8 * D, D, nullptr, 8, HD);
    }
    attn_kernel<<<dim3(H, NSEG), 256, 0, stream>>>(Qm, Km, Vm, Qx, Kx, Vx, Om, Ox, 0, 0);
    gemm_kernel<<<dim3(D / 64, S / 64), 256, 0, stream>>>(Ox, HD, Wo, D, hbuf, D, x, S, HD);
    ln_kernel<<<S, 64, 0, stream>>>(hbuf, hn);
    gemm_kernel<<<dim3(F / 64, S / 64), 256, 0, stream>>>(hn, D, w1, F, g, F, nullptr, S, D);
    gemm_kernel<<<dim3(F / 64, S / 64), 256, 0, stream>>>(hn, D, w3, F, u, F, nullptr, S, D);
    swiglu_kernel<<<(S * F + 255) / 256, 256, 0, stream>>>(g, u, act, S * F);
    gemm_kernel<<<dim3(D / 64, S / 64), 256, 0, stream>>>(act, F, w2, D, out, D, hbuf, S, F);
}
```

```cpp
#include <hip/hip_runtime.h>
#include <hip/hip_bf16.h>
#include <stdint.h>
#include <stdlib.h>

typedef unsigned short u16;
typedef unsigned v4u __attribute__((ext_vector_type(4)));
typedef unsigned v8u __attribute__((ext_vector_type(8)));
typedef unsigned v16u __attribute__((ext_vector_type(16)));
typedef float f32x2 __attribute__((ext_vector_type(2)));
typedef __bf16 bf16x2 __attribute__((ext_vector_type(2)));
#define DI __device__ __forceinline__
typedef __amdgpu_buffer_rsrc_t rsrc_t;

constexpr int S = 2048, D = 640, HD = 4096, F = 1728;

constexpr unsigned WS_CTL = 0;
constexpr unsigned FL_KV = 4096, FL_M = FL_KV + 16384, FL_END = FL_M + 8192;
constexpr unsigned NB_FO = 0x190000, NB_FH = NB_FO + 16384, NB_FA = NB_FH + 49152, NB_FEND = NB_FA + 32768;
constexpr unsigned NB_OF = 8u << 20, NB_HF = 10u << 20, NB_AF = 26u << 20;
constexpr unsigned WS_XNF = 1u << 20;
constexpr unsigned WS_HF = 4u << 20;
constexpr unsigned WS_AF = 8u << 20;
constexpr unsigned WS_KF = 16u << 20;
constexpr unsigned WS_VF = 33u << 20;
constexpr unsigned WS_KM = 50u << 20;
constexpr unsigned WS_VM = 54u << 20;
constexpr unsigned WS_OF = 58u << 20;
constexpr unsigned WS_DBG = 116u << 20;
constexpr unsigned WS_COMM = 128u << 20;
constexpr unsigned COMM_BYTES = 28u << 20;
constexpr unsigned WS_BYTES = 256u << 20;
constexpr unsigned OOB = 0x7ffffff0u;

DI unsigned pk(float a, float b) { f32x2 v = {a, b}; return __builtin_bit_cast(unsigned, __builtin_convertvector(v, bf16x2)); }
DI rsrc_t mkrsrc(const void* p, unsigned bytes) { return __builtin_amdgcn_make_buffer_rsrc((void*)p, 0, bytes, 0x00020000); }
DI void st16c(rsrc_t r, unsigned off, unsigned soff, v4u v) { __builtin_amdgcn_raw_buffer_store_b128(v, r, off, soff, 16); }
DI unsigned spread(unsigned L) { return ((L >> 13) << 14) | (L & 8191u); }
DI int lane_id() { int x; asm volatile("v_mbcnt_lo_u32_b32 %0, -1, 0\n\tv_mbcnt_hi_u32_b32 %0, -1, %0" : "=v"(x)); return x; }

DI unsigned comm_off(const char* ws) { return WS_COMM + ((0u - (unsigned)(uintptr_t)ws) & ((4u << 20) - 1)); }
struct Params {
    const u16 *x, *mem0, *Wq, *Wk, *Wv, *Wo, *w1, *w3, *w2;
    u16* out; char* ws; int k1, k2, c1, pad;
};

#define NHC 54
#define PROBES 12
constexpr unsigned WS_EPO = 0, WS_INI = 1024, TAGLO = 0x7f4a7c16u, TAGHI = 0x9e3779b9u;
typedef unsigned v2u __attribute__((ext_vector_type(2)));
DI void post_start(rsrc_t rw, v2u e, int k, unsigned bit) { __builtin_amdgcn_raw_buffer_store_b64((v2u){e[0] + TAGLO + bit, e[1] + TAGHI}, rw, WS_INI + k * 16, 0, 16); }
DI unsigned read_side(const char* ws) {
    const rsrc_t rw = mkrsrc(ws, WS_BYTES);
    const unsigned a = __builtin_amdgcn_raw_buffer_load_b32(rw, WS_EPO, 0, 16), b = __builtin_amdgcn_raw_buffer_load_b32(rw, WS_INI + 32 * 16, 0, 16);
    return __builtin_amdgcn_readfirstlane((b - a - TAGLO) & 1u);
}
DI unsigned hfoff(int piece) { return (piece / 7) * 16384 + (piece % 7) * 1024; }
DI v2u read_epoch(rsrc_t rw) { return __builtin_amdgcn_raw_buffer_load_b64(rw, WS_EPO, 0, 16); }

DI void row_prep(const Params& p, int q, int w, int l) {
    const rsrc_t rw = mkrsrc(p.ws, WS_BYTES);
    const int T = w * 32 + q;
    v4u v[20];
#pragma unroll
    for (int u = 0; u < 20; ++u) v[u] = *(const v4u*)(p.x + (size_t)(T * 16 + (l & 15)) * D + 32 * u + 8 * (l >> 4));
    const v2u e = read_epoch(rw);
    { const unsigned o = (q * 256 + w * 64 + l) * 16;
      if (FL_KV + o < FL_END) st16c(rw, FL_KV + o, 0, (v4u){0, 0, 0, 0});
      if (NB_FO + o < NB_FEND) st16c(rw, comm_off(p.ws) + NB_FO + o, 0, (v4u){0, 0, 0, 0}); }
    float s = 0.f;
#pragma unroll
    for (int u = 0; u < 20; ++u)
#pragma unroll
        for (int j = 0; j < 4; ++j) s += __uint_as_float(v[u][j] << 16) + __uint_as_float(v[u][j] & 0xffff0000u);
    s += __shfl_xor(s, 16); s += __shfl_xor(s, 32);
    const float mean = s * (1.0f / D); float sq = 0.f;
#pragma unroll
    for (int u = 0; u < 20; ++u)
#pragma unroll
        for (int j = 0; j < 4; ++j) { const float a = __uint_as_float(v[u][j] << 16) - mean, b = __uint_as_float(v[u][j] & 0xffff0000u) - mean; sq += a * a + b * b; }
    sq += __shfl_xor(sq, 16); sq += __shfl_xor(sq, 32);
    const float r = 1.0f / sqrtf(sq * (1.0f / D) + 1e-5f);
#pragma unroll
    for (int u = 0; u < 20; ++u) {
        v4u o;
#pragma unroll
        for (int j = 0; j < 4; ++j) o[j] = pk((__uint_as_float(v[u][j] << 16) - mean) * r, (__uint_as_float(v[u][j] & 0xffff0000u) - mean) * r);
        st16c(rw, WS_XNF + (T * 20 + u) * 1024 + l * 16, 0, o);
    }
    asm volatile("s_waitcnt vmcnt(0)" ::: "memory");
    __builtin_amdgcn_s_barrier();
    if (w == 0 && l == 0) post_start(rw, e, q, 0);
}

constexpr int L_MEM = 32768, L_W = 43008;
DI unsigned vsrc(int w, int l) { return (l >> 3) * 8192 + ((l & 7) ^ (2 * (((l >> 4) & 1) | ((w & 1) << 1)))) * 16; }
DI void fill_and_warm(const Params& p, int k, int xi, int w, int l) {
    const rsrc_t rw = mkrsrc(p.ws, WS_BYTES);
    const v2u e = read_epoch(rw);
    const unsigned cb = comm_off(p.ws);
    constexpr unsigned RING = 2 * 0xa0000, TOT = RING + (1u << 20);
    const bool prober = k == 63;
    if (!prober) for (unsigned o = (k * 256 + w * 64 + l) * 16; o < TOT; o += 63 * 4096) st16c(rw, cb + (o < RING ? o : (4u << 20) + (o - RING)), 0, (v4u){~0u, ~0u, ~0u, ~0u});
    const int pw = (k >> 2) * 4 + w;
    auto warm = [&](const u16* W, bool rows, int i0, int i1) {
        const rsrc_t rs = mkrsrc(W, D * HD * 2);
        for (int i = i0; i < i1; ++i) {
            const int n = pw + 64 * i;
            const unsigned so = rows ? (n >> 1) * 8192 + xi * 2048 + (n & 1) * 1024 : xi * 1310720 + n * 1024;
            asm volatile("s_mov_b32 m0, %2\n\ts_nop 0\n\tbuffer_load_dwordx4 %0, %1, %3 offen lds\n\ts_waitcnt vmcnt(30)" :: "v"(l * 16), "s"(rs), "s"(w * 1024), "s"(so) : "memory");
        }
    };
    auto stage_v = [&]() {
        const rsrc_t rs = mkrsrc(p.Wv, D * HD * 2);
        for (int i = 0; i < 20; ++i)
            asm volatile("s_mov_b32 m0, %2\n\ts_nop 0\n\tbuffer_load_dwordx4 %0, %1, %3 offen lds\n\ts_waitcnt vmcnt(30)" :: "v"(vsrc(w, l)), "s"(rs), "s"(L_W + (i * 4 + w) * 1024), "s"((i * 4 + w) * 65536 + xi * 2048 + (k >> 2) * 128) : "memory");
    };
    if (!prober) { warm(p.Wk, true, 0, 20); stage_v(); }
    __builtin_amdgcn_s_barrier();
    if (w == 1 && l == 0) post_start(rw, e, 64 + k, 0);
    if (!prober) { warm(p.Wq, true, 0, 20); warm(p.Wo, false, 0, 20); }
    else if (w == 0) {
        unsigned best[2] = {~0u, ~0u};
        for (int it = 0; it < PROBES; ++it) {
            const int sd = it & 1;
            unsigned long long t0, t1;
            asm volatile("s_memtime %0\n\ts_waitcnt lgkmcnt(0)" : "=s"(t0) :: "memory");
            if (l < 8) st16c(rw, cb + 0x180000 + (sd << 13) + (it >> 1) * 256 + l * 16, 0, (v4u){0, 0, 0, 0});
            asm volatile("s_waitcnt vmcnt(0)\n\ts_memtime %0\n\ts_waitcnt lgkmcnt(0)" : "=s"(t1) :: "memory");
            best[sd] = min(best[sd], (unsigned)(t1 - t0));
        }
        if (l == 0) post_start(rw, e, 32, best[1] < best[0] ? 1u : 0u);
    }
    if (prober) { __builtin_amdgcn_s_barrier(); stage_v(); }
    asm volatile("s_waitcnt vmcnt(0)" ::: "memory");
}

constexpr int L_WS = 28672, L_QX = L_W + 8192, L_OX = L_QX + 2048, L_KX = L_OX + 2048, L_END = L_W + 4 * L_WS;
#define CLOB_V "memory", "scc", "vcc", "v20", "v21", "v22", "v23", "v24", "v25", "v26", "v27", "v28", "v29", "v30", "v31", "v32", "v33", "v34", "v35", "v36", "v37", "v38", "v39", "v40", "v41", "v42", "v43", "v44", "v45", "v46", "v47", "v48", "v49", "v50", "v51", "v52", "v53", "v54", "v55", "v56", "v57", "v58", "v59", "v60", "v61", "v62", "v63", "v64", "v65", "v66", "v67", "v68", "v69", "v70", "v71", "v72", "v73", "v74", "v75", "v76", "v77", "v78", "v79", "v80", "v81", "v82", "v83", "v84", "v85", "v86", "v87", "v88", "v89", "v90", "v91", "v92", "v93", "v94", "v95", "v96", "v97", "v98", "v99", "v100", "v101", "v102", "v103", "v104", "v105", "v106", "v107", "v108", "v109", "v110", "v111", "v112", "v113", "v114", "v115", "v116", "v117", "v118", "v119", "v120", "v121", "v122", "v123", "v124", "v125", "v126", "v127", "a0", "a1", "a2", "a3", "a4", "a5", "a6", "a7", "a8", "a9", "a10", "a11", "a12", "a13", "a14", "a15", "a16", "a17", "a18", "a19", "a20", "a21", "a22", "a23", "a24", "a25", "a26", "a27", "a28", "a29", "a30", "a31", "a32", "a33", "a34", "a35", "a36", "a37", "a38", "a39", "a40", "a41", "a42", "a43", "a44", "a45", "a46", "a47", "a48", "a49", "a50", "a51", "a52", "a53", "a54", "a55", "a56", "a57", "a58", "a59", "a60", "a61", "a62", "a63", "a64", "a65", "a66", "a67", "a68", "a69", "a70", "a71", "a72", "a73", "a74", "a75", "a76", "a77", "a78", "a79", "a80", "a81", "a82", "a83", "a84", "a85", "a86", "a87", "a88", "a89", "a90", "a91", "a92", "a93", "a94", "a95", "a96", "a97", "a98", "a99", "a100", "a101", "a102", "a103", "a104", "a105", "a106", "a107", "a108", "a109", "a110", "a111", "a112", "a113", "a114", "a115", "a116", "a117", "a118", "a119", "a120", "a121", "a122", "a123", "a124", "a125", "a126", "a127", "a128", "a129", "a130", "a131", "a132", "a133", "a134", "a135", "a136", "a137", "a138", "a139", "a140", "a141", "a142", "a143", "a144", "a145", "a146", "a147", "a148", "a149", "a150", "a151", "a152", "a153", "a154", "a155", "a156", "a157", "a158", "a159", "a160", "a161", "a162", "a163", "a164", "a165", "a166", "a167", "a168", "a169", "a170", "a171", "a172", "a173", "a174", "a175", "a176", "a177", "a178", "a179", "a180", "a181", "a182", "a183", "a184", "a185", "a186", "a187", "a188", "a189", "a190", "a191", "a192", "a193", "a194", "a195", "a196", "a197", "a198", "a199", "a200", "a201", "a202", "a203", "a204", "a205", "a206", "a207", "a208", "a209", "a210", "a211", "a212", "a213", "a214", "a215", "a216", "a217", "a218", "a219", "a220", "a221", "a222", "a223", "a224", "a225", "a226", "a227", "a228", "a229", "a230", "a231", "a232", "a233", "a234", "a235", "a236", "a237", "a238", "a239", "a240", "a241", "a242", "a243", "a244", "a245", "a246", "a247", "a248", "a249", "a250", "a251", "a252", "a253", "a254", "a255", "s88", "s89", "s90", "s91", "s92", "s93", "s94", "s95", "s96", "s97", "s98", "s99", "s100", "s101"
#define CLOB_R "memory", "scc", "vcc", "v24", "v25", "v26", "v27", "v28", "v29", "v30", "v31", "v32", "v33", "v34", "v35", "v36", "v37", "v38", "v39", "v40", "v41", "v42", "v43", "v44", "v45", "v46", "v47", "v48", "v49", "v50", "v51", "v52", "v53", "v54", "v55", "v56", "v57", "v58", "v59", "v60", "v61", "v62", "v63", "v64", "v65", "v66", "v67", "v68", "v69", "v70", "v71", "v72", "v73", "v74", "v75", "v76", "v77", "v78", "v79", "v80", "v81", "v82", "v83", "v84", "v85", "v86", "v87", "v88", "v89", "v90", "v91", "v92", "v93", "v94", "v95", "v96", "v97", "v98", "v99", "v100", "v101", "v102", "v103", "v104", "v105", "v106", "v107", "v108", "v109", "v110", "v111", "v112", "v113", "v114", "v115", "v116", "v117", "v118", "v119", "v120", "v121", "v122", "v123", "v124", "v125", "v126", "v127", "v128", "v129", "v130", "v131", "v132", "v133", "v134", "v135", "v136", "v137", "v138", "v139", "v140", "v141", "v142", "v143", "v144", "v145", "v146", "v147", "v148", "v149", "v150", "v151", "v152", "v153", "v154", "v155", "v156", "v157", "v158", "v159", "v160", "v161", "v162", "v163", "v164", "v165", "v166", "v167", "v168", "v169", "v170", "v171", "v172", "v173", "v174", "v175", "v176", "v177", "v178", "v179", "v180", "v181", "v182", "v183", "v184", "v185", "v186", "v187", "v188", "v189", "v190", "v191", "v192", "v193", "v194", "v195", "v196", "v197", "v198", "v199", "v200", "v201", "v202", "v203", "v204", "v205", "v206", "v207", "v208", "v209", "v210", "v211", "v212", "v213", "v214", "v215", "v216", "v217", "v218", "v219", "v220", "v221", "v222", "v223", "v224", "v225", "v226", "v227", "v228", "v229", "v230", "v231", "v232", "v233", "v234", "v235", "v236", "v237", "v238", "v239", "v240", "v241", "v242", "v243", "v244", "v245", "v246", "v247", "v248", "v249", "v250", "v251", "v252", "v253", "v254", "v255", "a0", "a1", "a2", "a3", "a4", "a5", "a6", "a7", "a8", "a9", "a10", "a11", "a12", "a13", "a14", "a15", "a16", "a17", "a18", "a19", "a20", "a21", "a22", "a23", "a24", "a25", "a26", "a27", "a28", "a29", "a30", "a31", "a32", "a33", "a34", "a35", "a36", "a37", "a38", "a39", "a40", "a41", "a42", "a43", "a44", "a45", "a46", "a47", "a48", "a49", "a50", "a51", "a52", "a53", "a54", "a55", "a56", "a57", "a58", "a59", "a60", "a61", "a62", "a63", "a64", "a65", "a66", "a67", "a68", "a69", "a70", "a71", "a72", "a73", "a74", "a75", "a76", "a77", "a78", "a79", "a80", "a81", "a82", "a83", "a84", "a85", "a86", "a87", "a88", "a89", "a90", "a91", "a92", "a93", "a94", "a95", "a96", "a97", "a98", "a99", "a100", "a101", "a102", "a103", "a104", "a105", "a106", "a107", "a108", "a109", "a110", "a111", "a112", "a113", "a114", "a115", "a116", "a117", "a118", "a119", "a120", "a121", "a122", "a123", "a124", "a125", "a126", "a127", "a128", "a129", "a130", "a131", "a132", "a133", "a134", "a135", "a136", "a137", "a138", "a139", "a140", "a141", "a142", "a143", "a144", "a145", "a146", "a147", "a148", "a149", "a150", "a151", "a152", "a153", "a154", "a155", "a156", "a157", "a158", "a159", "a160", "a161", "a162", "a163", "a164", "a165", "a166", "a167", "a168", "a169", "a170", "a171", "a172", "a173", "a174", "a175", "a176", "a177", "a178", "a179", "a180", "a181", "a182", "a183", "a184", "a185", "a186", "a187", "a188", "a189", "a190", "a191", "a192", "a193", "a194", "a195", "a196", "a197", "a198", "a199", "a200", "a201", "a202", "a203", "a204", "a205", "a206", "a207", "a208", "a209", "a210", "a211", "a212", "a213", "a214", "a215", "a216", "a217", "a218", "a219", "a220", "a221", "a222", "a223", "a224", "a225", "a226", "a227", "a228", "a229", "a230", "a231", "a232", "a233", "a234", "a235", "a236", "a237", "a238", "a239", "a240", "a241", "a242", "a243", "a244", "a245", "a246", "a247", "a248", "a249", "a250", "a251", "a252", "a253", "a254", "a255", "s88", "s89", "s90", "s91", "s92", "s93", "s94", "s95", "s96", "s97", "s98", "s99", "s100", "s101"
#define CLOB_Q "memory", "scc", "vcc", "v32", "v33", "v34", "v35", "v36", "v37", "v38", "v39", "v40", "v41", "v42", "v43", "v44", "v45", "v46", "v47", "v48", "v49", "v50", "v51", "v52", "v53", "v54", "v55", "v56", "v57", "v58", "v59", "v60", "v61", "v62", "v63", "v64", "v65", "v66", "v67", "v68", "v69", "v70", "v71", "v72", "v73", "v74", "v75", "v76", "v77", "v78", "v79", "v80", "v81", "v82", "v83", "v84", "v85", "v86", "v87", "v88", "v89", "v90", "v91", "v92", "v93", "v94", "v95", "v96", "v97", "v98", "v99", "v100", "v101", "v102", "v103", "v104", "v105", "v106", "v107", "v108", "v109", "v110", "v111", "v112", "v113", "v114", "v115", "v116", "v117", "v118", "v119", "v120", "v121", "v122", "v123", "v124", "v125", "v126", "v127", "v128", "v129", "v130", "v131", "v132", "v133", "v134", "v135", "v136", "v137", "v138", "v139", "v140", "v141", "v142", "v143", "v144", "v145", "v146", "v147", "v148", "v149", "v150", "v151", "v152", "v153", "v154", "v155", "v156", "v157", "v158", "v159", "v160", "v161", "v162", "v163", "v164", "v165", "v166", "v167", "v168", "v169", "v170", "v171", "v172", "v173", "v174", "v175", "v176", "v177", "v178", "v179", "v180", "v181", "v182", "v183", "v184", "v185", "v186", "v187", "v188", "v189", "v190", "v191", "v192", "v193", "v194", "v195", "v196", "v197", "v198", "v199", "v200", "v201", "v202", "v203", "v204", "v205", "v206", "v207", "v208", "v209", "v210", "v211", "v212", "v213", "v214", "v215", "v216", "v217", "v218", "v219", "v220", "v221", "v222", "v223", "v224", "v225", "v226", "v227", "v228", "v229", "v230", "v231", "v232", "v233", "v234", "v235", "v236", "v237", "v238", "v239", "v240", "v241", "v242", "v243", "v244", "v245", "v246", "v247", "v248", "v249", "v250", "v251", "v252", "v253", "v254", "v255", "a0", "a1", "a2", "a3", "a4", "a5", "a6", "a7", "a8", "a9", "a10", "a11", "a12", "a13", "a14", "a15", "a16", "a17", "a18", "a19", "a20", "a21", "a22", "a23", "a24", "a25", "a26", "a27", "a28", "a29", "a30", "a31", "a32", "a33", "a34", "a35", "a36", "a37", "a38", "a39", "a40", "a41", "a42", "a43", "a44", "a45", "a46", "a47", "a48", "a49", "a50", "a51", "a52", "a53", "a54", "a55", "a56", "a57", "a58", "a59", "a60", "a61", "a62", "a63", "a64", "a65", "a66", "a67", "a68", "a69", "a70", "a71", "a72", "a73", "a74", "a75", "a76", "a77", "a78", "a79", "a80", "a81", "a82", "a83", "a84", "a85", "a86", "a87", "a88", "a89", "a90", "a91", "a92", "a93", "a94", "a95", "a96", "a97", "a98", "a99", "a100", "a101", "a102", "a103", "a104", "a105", "a106", "a107", "a108", "a109", "a110", "a111", "a112", "a113", "a114", "a115", "a116", "a117", "a118", "a119", "a120", "a121", "a122", "a123", "a124", "a125", "a126", "a127", "a128", "a129", "a130", "a131", "a132", "a133", "a134", "a135", "a136", "a137", "a138", "a139", "a140", "a141", "a142", "a143", "a144", "a145", "a146", "a147", "a148", "a149", "a150", "a151", "a152", "a153", "a154", "a155", "a156", "a157", "a158", "a159", "a160", "a161", "a162", "a163", "a164", "a165", "a166", "a167", "a168", "a169", "a170", "a171", "a172", "a173", "a174", "a175", "a176", "a177", "a178", "a179", "a180", "a181", "a182", "a183", "a184", "a185", "a186", "a187", "a188", "a189", "a190", "a191", "a192", "a193", "a194", "a195", "a196", "a197", "a198", "a199", "a200", "a201", "a202", "a203", "a204", "a205", "a206", "a207", "a208", "a209", "a210", "a211", "a212", "a213", "a214", "a215", "a216", "a217", "a218", "a219", "a220", "a221", "a222", "a223", "a224", "a225", "a226", "a227", "a228", "a229", "a230", "a231", "a232", "a233", "a234", "a235", "a236", "a237", "a238", "a239", "a240", "a241", "a242", "a243", "a244", "a245", "a246", "a247", "a248", "a249", "a250", "a251", "a252", "a253", "a254", "a255", "s68", "s69", "s70", "s71", "s72", "s73", "s74", "s75", "s76", "s77", "s78", "s79", "s80", "s81", "s82", "s83", "s84", "s85", "s86", "s87", "s88", "s89", "s90", "s91", "s92", "s93", "s94", "s95", "s96", "s97", "s98", "s99", "s100", "s101"
#define CLOB_W "memory", "scc", "vcc", "v16", "v17", "v18", "v19", "v20", "v21", "v22", "v23", "v24", "v25", "v26", "v27", "v28", "v29", "v30", "v31", "v32", "v33", "v34", "v35", "v36", "v37", "v38", "v39", "v40", "v41", "v42", "v43", "v44", "v45", "v46", "v47", "v48", "v49", "v50", "v51", "v52", "v53", "v54", "v55", "v56", "v57", "v58", "v59", "v60", "v61", "v62", "v63", "v64", "v65", "v66", "v67", "v68", "v69", "v70", "v71", "v72", "v73", "v74", "v75", "v76", "v77", "v78", "v79", "v80", "v81", "v82", "v83", "v84", "v85", "v86", "v87", "v88", "v89", "v90", "v91", "v92", "v93", "v94", "v95", "v96", "v97", "v98", "v99", "v100", "v101", "v102", "v103", "v104", "v105", "v106", "v107", "v108", "v109", "v110", "v111", "v112", "v113", "v114", "v115", "v116", "v117", "v118", "v119", "v120", "v121", "v122", "v123", "v124", "v125", "v126", "v127", "v128", "v129", "v130", "v131", "v132", "v133", "v134", "v135", "v136", "v137", "v138", "v139", "v140", "v141", "v142", "v143", "v144", "v145", "v146", "v147", "v148", "v149", "v150", "v151", "v152", "v153", "v154", "v155", "v156", "v157", "v158", "v159", "v160", "v161", "v162", "v163", "v164", "v165", "v166", "v167", "v168", "v169", "v170", "v171", "v172", "v173", "v174", "v175", "v176", "v177", "v178", "v179", "v180", "v181", "v182", "v183", "v184", "v185", "v186", "v187", "v188", "v189", "v190", "v191", "v192", "v193", "v194", "v195", "v196", "v197", "v198", "v199", "v200", "v201", "v202", "v203", "v204", "v205", "v206", "v207", "v208", "v209", "v210", "v211", "v212", "v213", "v214", "v215", "v216", "v217", "v218", "v219", "v220", "v221", "v222", "v223", "v224", "v225", "v226", "v227", "v228", "v229", "v230", "v231", "v232", "v233", "v234", "v235", "v236", "v237", "v238", "v239", "v240", "v241", "v242", "v243", "v244", "v245", "v246", "v247", "v248", "v249", "v250", "v251", "v252", "v253", "v254", "v255", "a0", "a1", "a2", "a3", "a4", "a5", "a6", "a7", "a8", "a9", "a10", "a11", "a12", "a13", "a14", "a15", "a16", "a17", "a18", "a19", "a20", "a21", "a22", "a23", "a24", "a25", "a26", "a27", "a28", "a29", "a30", "a31", "a32", "a33", "a34", "a35", "a36", "a37", "a38", "a39", "a40", "a41", "a42", "a43", "a44", "a45", "a46", "a47", "a48", "a49", "a50", "a51", "a52", "a53", "a54", "a55", "a56", "a57", "a58", "a59", "a60", "a61", "a62", "a63", "a64", "a65", "a66", "a67", "a68", "a69", "a70", "a71", "a72", "a73", "a74", "a75", "a76", "a77", "a78", "a79", "a80", "a81", "a82", "a83", "a84", "a85", "a86", "a87", "a88", "a89", "a90", "a91", "a92", "a93", "a94", "a95", "a96", "a97", "a98", "a99", "a100", "a101", "a102", "a103", "a104", "a105", "a106", "a107", "a108", "a109", "a110", "a111", "a112", "a113", "a114", "a115", "a116", "a117", "a118", "a119", "a120", "a121", "a122", "a123", "a124", "a125", "a126", "a127", "a128", "a129", "a130", "a131", "a132", "a133", "a134", "a135", "a136", "a137", "a138", "a139", "a140", "a141", "a142", "a143", "a144", "a145", "a146", "a147", "a148", "a149", "a150", "a151", "a152", "a153", "a154", "a155", "a156", "a157", "a158", "a159", "a160", "a161", "a162", "a163", "a164", "a165", "a166", "a167", "a168", "a169", "a170", "a171", "a172", "a173", "a174", "a175", "a176", "a177", "a178", "a179", "a180", "a181", "a182", "a183", "a184", "a185", "a186", "a187", "a188", "a189", "a190", "a191", "a192", "a193", "a194", "a195", "a196", "a197", "a198", "a199", "a200", "a201", "a202", "a203", "a204", "a205", "a206", "a207", "a208", "a209", "a210", "a211", "a212", "a213", "a214", "a215", "a216", "a217", "a218", "a219", "a220", "a221", "a222", "a223", "a224", "a225", "a226", "a227", "a228", "a229", "a230", "a231", "a232", "a233", "a234", "a235", "a236", "a237", "a238", "a239", "a240", "a241", "a242", "a243", "a244", "a245", "a246", "a247", "a248", "a249", "a250", "a251", "a252", "a253", "a254", "a255", "s68", "s69", "s70", "s71", "s72", "s73", "s74", "s75", "s76", "s77", "s78", "s79", "s80", "s81", "s82", "s83", "s84", "s85", "s86", "s87", "s88", "s89", "s90", "s91", "s92", "s93", "s94", "s95", "s96", "s97", "s98", "s99", "s100", "s101"
#define CLOB_D "memory", "scc", "vcc", "v24", "v25", "v26", "v27", "v28", "v29", "v30", "v31", "v32", "v33", "v34", "v35", "v36", "v37", "v38", "v39", "v40", "v41", "v42", "v43", "v44", "v45", "v46", "v47", "v48", "v49", "v50", "v51", "v52", "v53", "v54", "v55", "v56", "v57", "v58", "v59", "v60", "v61", "v62", "v63", "v64", "v65", "v66", "v67", "v68", "v69", "v70", "v71", "v72", "v73", "v74", "v75", "v76", "v77", "v78", "v79", "v80", "v81", "v82", "v83", "v84", "v85", "v86", "v87", "v88", "v89", "v90", "v91", "v92", "v93", "v94", "v95", "v96", "v97", "v98", "v99", "v100", "v101", "v102", "v103", "v104", "v105", "v106", "v107", "v108", "v109", "v110", "v111", "v112", "v113", "v114", "v115", "v116", "v117", "v118", "v119", "v120", "v121", "v122", "v123", "v124", "v125", "v126", "v127", "v128", "v129", "v130", "v131", "v132", "v133", "v134", "v135", "v136", "v137", "v138", "v139", "v140", "v141", "v142", "v143", "v144", "v145", "v146", "v147", "v148", "v149", "v150", "v151", "v152", "v153", "v154", "v155", "v156", "v157", "v158", "v159", "v160", "v161", "v162", "v163", "v164", "v165", "v166", "v167", "v168", "v169", "v170", "v171", "v172", "v173", "v174", "v175", "v176", "v177", "v178", "v179", "v180", "v181", "v182", "v183", "v184", "v185", "v186", "v187", "v188", "v189", "v190", "v191", "v192", "v193", "v194", "v195", "v196", "v197", "v198", "v199", "v200", "v201", "v202", "v203", "v204", "v205", "v206", "v207", "v208", "v209", "v210", "v211", "v212", "v213", "v214", "v215", "v216", "v217", "v218", "v219", "v220", "v221", "v222", "v223", "v224", "v225", "v226", "v227", "v228", "v229", "v230", "v231", "v232", "v233", "v234", "v235", "v236", "v237", "v238", "v239", "v240", "v241", "v242", "v243", "v244", "v245", "v246", "v247", "v248", "v249", "v250", "v251", "v252", "v253", "v254", "v255", "a0", "a1", "a2", "a3", "a4", "a5", "a6", "a7", "a8", "a9", "a10", "a11", "a12", "a13", "a14", "a15", "a16", "a17", "a18", "a19", "a20", "a21", "a22", "a23", "a24", "a25", "a26", "a27", "a28", "a29", "a30", "a31", "a32", "a33", "a34", "a35", "a36", "a37", "a38", "a39", "a40", "a41", "a42", "a43", "a44", "a45", "a46", "a47", "a48", "a49", "a50", "a51", "a52", "a53", "a54", "a55", "a56", "a57", "a58", "a59", "a60", "a61", "a62", "a63", "a64", "a65", "a66", "a67", "a68", "a69", "a70", "a71", "a72", "a73", "a74", "a75", "a76", "a77", "a78", "a79", "a80", "a81", "a82", "a83", "a84", "a85", "a86", "a87", "a88", "a89", "a90", "a91", "a92", "a93", "a94", "a95", "a96", "a97", "a98", "a99", "a100", "a101", "a102", "a103", "a104", "a105", "a106", "a107", "a108", "a109", "a110", "a111", "a112", "a113", "a114", "a115", "a116", "a117", "a118", "a119", "a120", "a121", "a122", "a123", "a124", "a125", "a126", "a127", "a128", "a129", "a130", "a131", "a132", "a133", "a134", "a135", "a136", "a137", "a138", "a139", "a140", "a141", "a142", "a143", "a144", "a145", "a146", "a147", "a148", "a149", "a150", "a151", "a152", "a153", "a154", "a155", "a156", "a157", "a158", "a159", "a160", "a161", "a162", "a163", "a164", "a165", "a166", "a167", "a168", "a169", "a170", "a171", "a172", "a173", "a174", "a175", "a176", "a177", "a178", "a179", "a180", "a181", "a182", "a183", "a184", "a185", "a186", "a187", "a188", "a189", "a190", "a191", "a192", "a193", "a194", "a195", "a196", "a197", "a198", "a199", "a200", "a201", "a202", "a203", "a204", "a205", "a206", "a207", "a208", "a209", "a210", "a211", "a212", "a213", "a214", "a215", "a216", "a217", "a218", "a219", "a220", "a221", "a222", "a223", "a224", "a225", "a226", "a227", "a228", "a229", "a230", "a231", "a232", "a233", "a234", "a235", "a236", "a237", "a238", "a239", "a240", "a241", "a242", "a243", "a244", "a245", "a246", "a247", "a248", "a249", "a250", "a251", "a252", "a253", "a254", "a255", "s68", "s69", "s70", "s71", "s72", "s73", "s74", "s75", "s76", "s77", "s78", "s79", "s80", "s81", "s82", "s83", "s84", "s85", "s86", "s87", "s88", "s89", "s90", "s91", "s92", "s93", "s94", "s95", "s96", "s97", "s98", "s99", "s100", "s101"
#define K1C 200
#define C1C 1900
#define K2C 1040
DI void put_srd(v16u& s, int i, const void* p, unsigned off, unsigned bytes) {
    const uintptr_t a = (uintptr_t)p + off;
    s[i] = (unsigned)a; s[i + 1] = (unsigned)(a >> 32) & 0xffffu; s[i + 2] = off < bytes ? bytes - off : 0u; s[i + 3] = 0x00020000u;
}
DI void uni(v16u& s) {
#pragma unroll
    for (int j = 0; j < 16; ++j) s[j] = __builtin_amdgcn_readfirstlane(s[j]);
}
DI void strip_consts(v16u& C, int l, unsigned stg, unsigned rs, bool dorder) {
    const int c = l & 15, g = l >> 4;
    const int sw = dorder ? (l >> 4) & 1 : (l >> 5) & 1;
    C[0] = l * 16;
    C[1] = (l >> 2) * rs + ((l & 3) ^ (2 * sw)) * 16;
    const unsigned base = stg + ((dorder ? 4 : 8) * g + (c >> 2)) * 64 + (c & 3) * 8;
    C[2] = base + 32 * (g & 1); C[3] = base + 32 * ((g & 1) ^ 1);
}

typedef __attribute__((address_space(4))) const Params* kparg_t;
DI Params args() { kparg_t k = (kparg_t)__builtin_amdgcn_kernarg_segment_ptr(); asm volatile("" : "+s"(k));
    Params p; p.x = k->x; p.mem0 = k->mem0; p.Wq = k->Wq; p.Wk = k->Wk; p.Wv = k->Wv; p.Wo = k->Wo; p.w1 = k->w1; p.w3 = k->w3; p.w2 = k->w2;
    p.out = k->out; p.ws = k->ws; p.k1 = k->k1; p.k2 = k->k2; p.c1 = k->c1; p.pad = 0; return p; }
DI int opq(int x) { asm volatile("" : "+s"(x)); return x; }
#define ROLE_BEGIN const Params p = args(); const int slot = opq(b0) >> 3, xi = opq(b0) & 3, w = opq(w0);
__global__ void __launch_bounds__(256, 1) k_main(Params pp) {
    extern __shared__ __attribute__((aligned(16))) char lds[];
    const int b0 = blockIdx.x;
    const int xcd = b0 & 7, slot = b0 >> 3;
    const int w0 = __builtin_amdgcn_readfirstlane(threadIdx.x >> 6);
    if (xcd < 4) {
        if (slot >= 16) {
            { ROLE_BEGIN fill_and_warm(p, (slot - 16) * 4 + xi, xi, w, lane_id()); }
            ROLE_BEGIN
            const int l = lane_id(), c = l & 15, g = l >> 4, hb = (c >> 1) & 1;
            const int j = slot - 16, h = xi * 8 + (j >> 1), hf = j & 1, r = 2 * h + hf;
            const int ru = w == 0 ? r : 64 + (r >> 2);
            v16u C = {}, SA, SB = {}, SC = {}; v4u SEL;
            const unsigned stg = L_W + w * L_WS;
            const rsrc_t rm = mkrsrc(p.mem0, 8 * D * 2);
            C[0] = l * 16; C[1] = vsrc(w, l); C[2] = L_W + (8 * g + (c >> 2)) * 128 + (w ^ (((c >> 3) & 1) | ((g & 1) << 1))) * 32 + (c & 3) * 8;
            C[3] = (16 * w + c) * 16 + 4 * g;
            C[4] = (4 * g + (c >> 2)) * 16 + 8 * (c & 1) + hb * 512;
            C[5] = (c >> 3) * 512 + g * 128 + (c & 7) * 16;
            C[6] = spread((unsigned)((c >> 3) * 16384 + ru * 128 + (4 * (g & 1) + 2 * (g >> 1) + (c & 1)) * 16 + ((c & 7) >> 1) * 4));
            C[7] = hb ? 0xEEEEu : 0x4444u;
            C[8] = (16 * w + c) * 16 + 4 * g;
            C[9] = j < 11 ? spread((unsigned)(xi * 16384 + j * 1024 + l * 16)) : OOB;
            C[10] = ((l >> 2) & 7) * 1280 + (l >> 5) * 4 + (l & 3) * 8;
            C[12] = L_MEM + g * 256 + hb * 128 + (2 * (c >> 2) + (c & 1)) * 16;
#pragma unroll
            for (int q = 0; q < 4; ++q) SEL[q] = ((c & 7) == 2 * q ? 0x3f80u : 0u) | ((c & 7) == 2 * q + 1 ? 0x3f800000u : 0u);
            const int piece = w == 0 ? 0 : (w == 1 ? 8 : (w == 2 ? 2 : 5));
            put_srd(SA, 0, p.ws, 0, WS_BYTES);
            SA[4] = comm_off(p.ws); SA[5] = (w == 0 || (r & 3) == 0) ? spread((unsigned)(ru * 4096)) : ~0u;
            SA[6] = spread((unsigned)(xi * 16384 + piece * 1024)); SA[7] = L_MEM + piece * 1024;
            SA[8] = 0; SA[9] = 0; SA[10] = xi * 32768; SA[11] = 0;
            SA[12] = WS_KM + h * 2048 + hf * 1024; SA[13] = WS_VM + h * 2048 + hf * 1024; SA[14] = w; SA[15] = stg;
            SB[1] = FL_M + r * 128;
            SB[3] = 2830; SB[5] = p.c1; SB[6] = p.k1 * 4 / 3; SB[7] = p.k2 * 4 / 3; SB[8] = 0x80000 + spread((unsigned)(r * 1024)); SB[9] = WS_DBG + (256 + r * 4 + w) * 8192; SB[10] = 16384 + 2048 + r * 16;
            put_srd(SC, 4, p.Wk, (h * 128 + 64 * hf) * 2, D * HD * 2);
            uni(SA); uni(SB); uni(SC);
            asm volatile(
    "s_mul_i32 s88, s50, 2560\n"
    "s_mul_i32 s89, s50, 320\n"
    "s_add_u32 m0, s88, 32768\n"
    "s_add_u32 s90, s89, 0\n"
    "buffer_load_dword v10, s[84:87], s90 offen lds\n"
    "s_add_u32 m0, s88, 33024\n"
    "s_add_u32 s90, s89, 32\n"
    "buffer_load_dword v10, s[84:87], s90 offen lds\n"
    "s_add_u32 m0, s88, 33280\n"
    "s_add_u32 s90, s89, 64\n"
    "buffer_load_dword v10, s[84:87], s90 offen lds\n"
    "s_add_u32 m0, s88, 33536\n"
    "s_add_u32 s90, s89, 96\n"
    "buffer_load_dword v10, s[84:87], s90 offen lds\n"
    "s_add_u32 m0, s88, 33792\n"
    "s_add_u32 s90, s89, 128\n"
    "buffer_load_dword v10, s[84:87], s90 offen lds\n"
    "s_add_u32 m0, s88, 34048\n"
    "s_add_u32 s90, s89, 160\n"
    "buffer_load_dword v10, s[84:87], s90 offen lds\n"
    "s_add_u32 m0, s88, 34304\n"
    "s_add_u32 s90, s89, 192\n"
    "buffer_load_dword v10, s[84:87], s90 offen lds\n"
    "s_add_u32 m0, s88, 34560\n"
    "s_add_u32 s90, s89, 224\n"
    "buffer_load_dword v10, s[84:87], s90 offen lds\n"
    "s_add_u32 m0, s88, 34816\n"
    "s_add_u32 s90, s89, 256\n"
    "buffer_load_dword v10, s[84:87], s90 offen lds\n"
    "s_add_u32 m0, s88, 35072\n"
    "s_add_u32 s90, s89, 288\n"
    "buffer_load_dword v10, s[84:87], s90 offen lds\n"
    "s_barrier\n"
    "ds_read_b64_tr_b16 a[0:1], v2 offset:0\n"
    "ds_read_b64_tr_b16 a[2:3], v2 offset:512\n"
    "ds_read_b64_tr_b16 a[4:5], v2 offset:4096\n"
    "ds_read_b64_tr_b16 a[6:7], v2 offset:4608\n"
    "ds_read_b64_tr_b16 a[8:9], v2 offset:8192\n"
    "ds_read_b64_tr_b16 a[10:11], v2 offset:8704\n"
    "ds_read_b64_tr_b16 a[12:13], v2 offset:12288\n"
    "ds_read_b64_tr_b16 a[14:15], v2 offset:12800\n"
    "ds_read_b64_tr_b16 a[16:17], v2 offset:16384\n"
    "ds_read_b64_tr_b16 a[18:19], v2 offset:16896\n"
    "ds_read_b64_tr_b16 a[20:21], v2 offset:20480\n"
    "ds_read_b64_tr_b16 a[22:23], v2 offset:20992\n"
    "ds_read_b64_tr_b16 a[24:25], v2 offset:24576\n"
    "ds_read_b64_tr_b16 a[26:27], v2 offset:25088\n"
    "s_waitcnt lgkmcnt(7)\n"
    "ds_read_b64_tr_b16 a[28:29], v2 offset:28672\n"
    "ds_read_b64_tr_b16 a[30:31], v2 offset:29184\n"
    "ds_read_b64_tr_b16 a[32:33], v2 offset:32768\n"
    "ds_read_b64_tr_b16 a[34:35], v2 offset:33280\n"
    "ds_read_b64_tr_b16 a[36:37], v2 offset:36864\n"
    "ds_read_b64_tr_b16 a[38:39], v2 offset:37376\n"
    "v_add_u32 v80, 0xa000, v2\n"
    "ds_read_b64_tr_b16 a[40:41], v80 offset:0\n"
    "s_waitcnt lgkmcnt(7)\n"
    "ds_read_b64_tr_b16 a[42:43], v80 offset:512\n"
    "ds_read_b64_tr_b16 a[44:45], v80 offset:4096\n"
    "ds_read_b64_tr_b16 a[46:47], v80 offset:4608\n"
    "ds_read_b64_tr_b16 a[48:49], v80 offset:8192\n"
    "ds_read_b64_tr_b16 a[50:51], v80 offset:8704\n"
    "ds_read_b64_tr_b16 a[52:53], v80 offset:12288\n"
    "ds_read_b64_tr_b16 a[54:55], v80 offset:12800\n"
    "s_waitcnt lgkmcnt(7)\n"
    "ds_read_b64_tr_b16 a[56:57], v80 offset:16384\n"
    "ds_read_b64_tr_b16 a[58:59], v80 offset:16896\n"
    "ds_read_b64_tr_b16 a[60:61], v80 offset:20480\n"
    "ds_read_b64_tr_b16 a[62:63], v80 offset:20992\n"
    "ds_read_b64_tr_b16 a[64:65], v80 offset:24576\n"
    "ds_read_b64_tr_b16 a[66:67], v80 offset:25088\n"
    "ds_read_b64_tr_b16 a[68:69], v80 offset:28672\n"
    "s_waitcnt lgkmcnt(7)\n"
    "ds_read_b64_tr_b16 a[70:71], v80 offset:29184\n"
    "ds_read_b64_tr_b16 a[72:73], v80 offset:32768\n"
    "ds_read_b64_tr_b16 a[74:75], v80 offset:33280\n"
    "ds_read_b64_tr_b16 a[76:77], v80 offset:36864\n"
    "ds_read_b64_tr_b16 a[78:79], v80 offset:37376\n"
    "s_waitcnt lgkmcnt(0)\n"
    "s_barrier\n"
    "s_lshl_b32 s88, s50, 10\n"
    "s_lshl_b32 s89, s50, 16\n"
    "s_add_u32 m0, s88, 43008\n"
    "s_add_u32 s90, s89, 0\n"
    "buffer_load_dwordx4 v1, s[72:75], s90 offen lds\n"
    "s_add_u32 m0, s88, 47104\n"
    "s_add_u32 s90, s89, 262144\n"
    "buffer_load_dwordx4 v1, s[72:75], s90 offen lds\n"
    "s_add_u32 m0, s88, 51200\n"
    "s_add_u32 s90, s89, 524288\n"
    "buffer_load_dwordx4 v1, s[72:75], s90 offen lds\n"
    "s_add_u32 m0, s88, 55296\n"
    "s_add_u32 s90, s89, 786432\n"
    "buffer_load_dwordx4 v1, s[72:75], s90 offen lds\n"
    "s_add_u32 m0, s88, 59392\n"
    "s_add_u32 s90, s89, 1048576\n"
    "buffer_load_dwordx4 v1, s[72:75], s90 offen lds\n"
    "s_add_u32 m0, s88, 63488\n"
    "s_add_u32 s90, s89, 1310720\n"
    "buffer_load_dwordx4 v1, s[72:75], s90 offen lds\n"
    "s_add_u32 m0, s88, 67584\n"
    "s_add_u32 s90, s89, 1572864\n"
    "buffer_load_dwordx4 v1, s[72:75], s90 offen lds\n"
    "s_add_u32 m0, s88, 71680\n"
    "s_add_u32 s90, s89, 1835008\n"
    "buffer_load_dwordx4 v1, s[72:75], s90 offen lds\n"
    "s_add_u32 m0, s88, 75776\n"
    "s_add_u32 s90, s89, 2097152\n"
    "buffer_load_dwordx4 v1, s[72:75], s90 offen lds\n"
    "s_add_u32 m0, s88, 79872\n"
    "s_add_u32 s90, s89, 2359296\n"
    "buffer_load_dwordx4 v1, s[72:75], s90 offen lds\n"
    "s_add_u32 m0, s88, 83968\n"
    "s_add_u32 s90, s89, 2621440\n"
    "buffer_load_dwordx4 v1, s[72:75], s90 offen lds\n"
    "s_add_u32 m0, s88, 88064\n"
    "s_add_u32 s90, s89, 2883584\n"
    "buffer_load_dwordx4 v1, s[72:75], s90 offen lds\n"
    "s_add_u32 m0, s88, 92160\n"
    "s_add_u32 s90, s89, 3145728\n"
    "buffer_load_dwordx4 v1, s[72:75], s90 offen lds\n"
    "s_add_u32 m0, s88, 96256\n"
    "s_add_u32 s90, s89, 3407872\n"
    "buffer_load_dwordx4 v1, s[72:75], s90 offen lds\n"
    "s_add_u32 m0, s88, 100352\n"
    "s_add_u32 s90, s89, 3670016\n"
    "buffer_load_dwordx4 v1, s[72:75], s90 offen lds\n"
    "s_add_u32 m0, s88, 104448\n"
    "s_add_u32 s90, s89, 3932160\n"
    "buffer_load_dwordx4 v1, s[72:75], s90 offen lds\n"
    "s_add_u32 m0, s88, 108544\n"
    "s_add_u32 s90, s89, 4194304\n"
    "buffer_load_dwordx4 v1, s[72:75], s90 offen lds\n"
    "s_add_u32 m0, s88, 112640\n"
    "s_add_u32 s90, s89, 4456448\n"
    "buffer_load_dwordx4 v1, s[72:75], s90 offen lds\n"
    "s_add_u32 m0, s88, 116736\n"
    "s_add_u32 s90, s89, 4718592\n"
    "buffer_load_dwordx4 v1, s[72:75], s90 offen lds\n"
    "s_add_u32 m0, s88, 120832\n"
    "s_add_u32 s90, s89, 4980736\n"
    "buffer_load_dwordx4 v1, s[72:75], s90 offen lds\n"
    "s_waitcnt vmcnt(0)\n"
    "s_barrier\n"
    "ds_read_b64_tr_b16 a[80:81], v2 offset:0\n"
    "ds_read_b64_tr_b16 a[82:83], v2 offset:512\n"
    "ds_read_b64_tr_b16 a[84:85], v2 offset:4096\n"
    "ds_read_b64_tr_b16 a[86:87], v2 offset:4608\n"
    "ds_read_b64_tr_b16 a[88:89], v2 offset:8192\n"
    "ds_read_b64_tr_b16 a[90:91], v2 offset:8704\n"
    "ds_read_b64_tr_b16 a[92:93], v2 offset:12288\n"
    "ds_read_b64_tr_b16 a[94:95], v2 offset:12800\n"
    "ds_read_b64_tr_b16 a[96:97], v2 offset:16384\n"
    "ds_read_b64_tr_b16 a[98:99], v2 offset:16896\n"
    "ds_read_b64_tr_b16 a[100:101], v2 offset:20480\n"
    "ds_read_b64_tr_b16 a[102:103], v2 offset:20992\n"
    "ds_read_b64_tr_b16 a[104:105], v2 offset:24576\n"
    "ds_read_b64_tr_b16 a[106:107], v2 offset:25088\n"
    "s_waitcnt lgkmcnt(7)\n"
    "ds_read_b64_tr_b16 a[108:109], v2 offset:28672\n"
    "ds_read_b64_tr_b16 a[110:111], v2 offset:29184\n"
    "ds_read_b64_tr_b16 a[112:113], v2 offset:32768\n"
    "ds_read_b64_tr_b16 a[114:115], v2 offset:33280\n"
    "ds_read_b64_tr_b16 a[116:117], v2 offset:36864\n"
    "ds_read_b64_tr_b16 a[118:119], v2 offset:37376\n"
    "v_add_u32 v80, 0xa000, v2\n"
    "ds_read_b64_tr_b16 a[120:121], v80 offset:0\n"
    "s_waitcnt lgkmcnt(7)\n"
    "ds_read_b64_tr_b16 a[122:123], v80 offset:512\n"
    "ds_read_b64_tr_b16 a[124:125], v80 offset:4096\n"
    "ds_read_b64_tr_b16 a[126:127], v80 offset:4608\n"
    "ds_read_b64_tr_b16 a[128:129], v80 offset:8192\n"
    "ds_read_b64_tr_b16 a[130:131], v80 offset:8704\n"
    "ds_read_b64_tr_b16 a[132:133], v80 offset:12288\n"
    "ds_read_b64_tr_b16 a[134:135], v80 offset:12800\n"
    "s_waitcnt lgkmcnt(7)\n"
    "ds_read_b64_tr_b16 a[136:137], v80 offset:16384\n"
    "ds_read_b64_tr_b16 a[138:139], v80 offset:16896\n"
    "ds_read_b64_tr_b16 a[140:141], v80 offset:20480\n"
    "ds_read_b64_tr_b16 a[142:143], v80 offset:20992\n"
    "ds_read_b64_tr_b16 a[144:145], v80 offset:24576\n"
    "ds_read_b64_tr_b16 a[146:147], v80 offset:25088\n"
    "ds_read_b64_tr_b16 a[148:149], v80 offset:28672\n"
    "s_waitcnt lgkmcnt(7)\n"
    "ds_read_b64_tr_b16 a[150:151], v80 offset:29184\n"
    "ds_read_b64_tr_b16 a[152:153], v80 offset:32768\n"
    "ds_read_b64_tr_b16 a[154:155], v80 offset:33280\n"
    "ds_read_b64_tr_b16 a[156:157], v80 offset:36864\n"
    "ds_read_b64_tr_b16 a[158:159], v80 offset:37376\n"
    "s_waitcnt lgkmcnt(0)\n"
    "v_mov_b32 v76, -1\n"
    "v_mov_b32 v77, -1\n"
    "v_mov_b32 v78, -1\n"
    "v_mov_b32 v79, -1\n"
    "s_mov_b32 s69, 0\n"
    "s_mov_b32 s79, 0\n"
    "s_mov_b32 exec_hi, -1\n"
    "buffer_load_dwordx2 v[28:29], off, s[36:39], 0 sc1\n"
    "R7_ih_%=:\n"
    "buffer_load_dwordx2 v[30:31], v0, s[36:39], 0 offen offset:2048 sc1\n"
    "s_waitcnt vmcnt(0)\n"
    "v_sub_u32 v30, v30, v28\n"
    "v_sub_u32 v31, v31, v29\n"
    "v_subrev_u32 v30, 0x7f4a7c16, v30\n"
    "v_cmp_ne_u32 vcc, 0x9e3779b9, v31\n"
    "v_cmp_lt_u32 s[94:95], 1, v30\n"
    "s_nop 1\n"
    "s_or_b64 vcc, vcc, s[94:95]\n"
    "s_cbranch_scc0 R7_jh_%=\n"
    "s_cmp_lg_u32 s69, 0\n"
    "s_cbranch_scc1 R7_jh_%=\n"
    "s_sleep 4\n"
    "s_add_u32 s79, s79, 1\n"
    "s_cmp_lt_u32 s79, 0x40000\n"
    "s_cbranch_scc1 R7_ih_%=\n"
    "s_mov_b32 s69, 1\n"
    "R7_jh_%=:\n"
    "s_mov_b64 exec, -1\n"
    "s_mov_b32 s79, 0\n"
    "s_mov_b32 exec_hi, 1\n"
    "buffer_load_dwordx2 v[28:29], off, s[36:39], 0 sc1\n"
    "R7_iq_%=:\n"
    "buffer_load_dwordx2 v[30:31], v0, s[36:39], 0 offen offset:1024 sc1\n"
    "s_waitcnt vmcnt(0)\n"
    "v_sub_u32 v30, v30, v28\n"
    "v_sub_u32 v31, v31, v29\n"
    "v_subrev_u32 v30, 0x7f4a7c16, v30\n"
    "v_cmp_ne_u32 vcc, 0x9e3779b9, v31\n"
    "v_cmp_lt_u32 s[94:95], 1, v30\n"
    "s_nop 1\n"
    "s_or_b64 vcc, vcc, s[94:95]\n"
    "s_cbranch_scc0 R7_jq_%=\n"
    "s_cmp_lg_u32 s69, 0\n"
    "s_cbranch_scc1 R7_jq_%=\n"
    "s_sleep 4\n"
    "s_add_u32 s79, s79, 1\n"
    "s_cmp_lt_u32 s79, 0x40000\n"
    "s_cbranch_scc1 R7_iq_%=\n"
    "s_mov_b32 s69, 1\n"
    "R7_jq_%=:\n"
    "s_mov_b64 exec, -1\n"
    "v_readlane_b32 s76, v30, 32\n"
    "s_nop 1\n"
    "s_and_b32 s76, s76, 1\n"
    "s_lshl_b32 s76, s76, 13\n"
    "s_add_u32 s40, s40, s76\n"
    "s_mov_b32 s64, 0\n"
    "s_mov_b32 s87, 0\n"
    "s_mov_b32 s88, 0\n"
    "s_mov_b32 s89, 0\n"
    "s_and_b32 s76, s64, 1\n"
    "s_mul_i32 s76, s76, 0xa0000\n"
    "s_add_u32 s65, s40, s76\n"
    "s_xor_b32 s76, s76, 0xa0000\n"
    "s_add_u32 s71, s40, s76\n"
    "s_and_b32 s76, s64, 3\n"
    "s_lshl_b32 s76, s76, 17\n"
    "s_or_b32 s76, s76, 0x400000\n"
    "s_add_u32 s66, s40, s76\n"
    "s_xor_b32 s76, s76, 0x40000\n"
    "s_add_u32 s68, s40, s76\n"
    "s_add_u32 s67, s66, s60\n"
    "s_mov_b32 s70, 0\n"
    "s_mov_b32 s80, s66\n"
    "s_cmp_eq_u32 s50, 1\n"
    "s_cbranch_scc1 R1_entry_%=\n"
    "s_cmp_eq_u32 s50, 2\n"
    "s_cbranch_scc1 R2_entry_%=\n"
    "s_cmp_eq_u32 s50, 3\n"
    "s_cbranch_scc1 R3_entry_%=\n"
    "R0_entry_%=:\n"
    "v_mov_b64 v[20:21], 0\n"
    "v_mov_b64 v[22:23], 0\n"
    "v_mov_b64 v[24:25], 0\n"
    "v_mov_b64 v[26:27], 0\n"
    "s_waitcnt vmcnt(0)\n"
    "R0_wait_%=:\n"
    "s_barrier\n"
    "ds_read_b128 v[28:31], v12 offset:0\n"
    "ds_read_b128 v[32:35], v12 offset:1024\n"
    "ds_read_b128 v[36:39], v12 offset:2048\n"
    "ds_read_b128 v[40:43], v12 offset:3072\n"
    "s_waitcnt lgkmcnt(3)\n"
    "v_smfmac_f32_16x16x64_bf16 v[20:23], v[28:31], a[0:7], v7\n"
    "ds_read_b128 v[44:47], v12 offset:4096\n"
    "s_waitcnt lgkmcnt(3)\n"
    "v_smfmac_f32_16x16x64_bf16 v[20:23], v[32:35], a[8:15], v7\n"
    "ds_read_b128 v[48:51], v12 offset:5120\n"
    "s_waitcnt lgkmcnt(3)\n"
    "v_smfmac_f32_16x16x64_bf16 v[20:23], v[36:39], a[16:23], v7\n"
    "ds_read_b128 v[52:55], v12 offset:6144\n"
    "s_waitcnt lgkmcnt(3)\n"
    "v_smfmac_f32_16x16x64_bf16 v[20:23], v[40:43], a[24:31], v7\n"
    "ds_read_b128 v[56:59], v12 offset:7168\n"
    "s_waitcnt lgkmcnt(3)\n"
    "v_smfmac_f32_16x16x64_bf16 v[20:23], v[44:47], a[32:39], v7\n"
    "ds_read_b128 v[60:63], v12 offset:8192\n"
    "s_waitcnt lgkmcnt(3)\n"
    "v_smfmac_f32_16x16x64_bf16 v[20:23], v[48:51], a[40:47], v7\n"
    "ds_read_b128 v[64:67], v12 offset:9216\n"
    "s_waitcnt lgkmcnt(3)\n"
    "v_smfmac_f32_16x16x64_bf16 v[20:23], v[52:55], a[48:55], v7\n"
    "s_waitcnt lgkmcnt(2)\n"
    "v_smfmac_f32_16x16x64_bf16 v[20:23], v[56:59], a[56:63], v7\n"
    "s_waitcnt lgkmcnt(1)\n"
    "v_smfmac_f32_16x16x64_bf16 v[20:23], v[60:63], a[64:71], v7\n"
    "s_waitcnt lgkmcnt(0)\n"
    "v_smfmac_f32_16x16x64_bf16 v[20:23], v[64:67], a[72:79], v7\n"
    "v_smfmac_f32_16x16x64_bf16 v[24:27], v[28:31], a[80:87], v7\n"
    "v_smfmac_f32_16x16x64_bf16 v[24:27], v[32:35], a[88:95], v7\n"
    "s_nop 1\n"
    "v_add_f32 v20, v20, v22\n"
    "v_add_f32 v21, v21, v23\n"
    "v_cmp_u_f32 s[74:75], v20, v21\n"
    "v_cvt_pk_bf16_f32 v20, v20, v21\n"
    "v_min_u32 v20, -2, v20\n"
    "v_cndmask_b32 v21, v20, v76, s[74:75]\n"
    "buffer_store_dword v21, v8, s[36:39], s67 offen sc1\n"
    "v_max3_u32 v68, v28, v29, v30\n"
    "v_max3_u32 v68, v68, v31, v32\n"
    "v_max3_u32 v68, v68, v33, v34\n"
    "v_max3_u32 v68, v68, v35, v36\n"
    "v_max3_u32 v68, v68, v37, v38\n"
    "v_max3_u32 v68, v68, v39, v40\n"
    "v_max3_u32 v68, v68, v41, v42\n"
    "v_max3_u32 v68, v68, v43, v44\n"
    "v_max3_u32 v68, v68, v45, v46\n"
    "v_max3_u32 v68, v68, v47, v48\n"
    "v_max3_u32 v68, v68, v49, v50\n"
    "v_max3_u32 v68, v68, v51, v52\n"
    "v_max3_u32 v68, v68, v53, v54\n"
    "v_max3_u32 v68, v68, v55, v56\n"
    "v_max3_u32 v68, v68, v57, v58\n"
    "v_max3_u32 v68, v68, v59, v60\n"
    "v_max3_u32 v68, v68, v61, v62\n"
    "v_max3_u32 v68, v68, v63, v64\n"
    "v_max3_u32 v68, v68, v65, v66\n"
    "v_max_u32 v68, v68, v67\n"
    "v_cmp_eq_u32 vcc, -1, v68\n"
    "s_cbranch_vccnz R0_miss_%=\n"
    "R0_missok_%=:\n"
    "s_cmp_eq_u64 s[74:75], 0\n"
    "s_cbranch_scc1 R0_vclean_%=\n"
    "s_waitcnt vmcnt(0)\n"
    "buffer_store_dword v20, v8, s[36:39], s67 offen sc1\n"
    "R0_vclean_%=:\n"
    "v_smfmac_f32_16x16x64_bf16 v[24:27], v[36:39], a[96:103], v7\n"
    "v_smfmac_f32_16x16x64_bf16 v[24:27], v[40:43], a[104:111], v7\n"
    "v_smfmac_f32_16x16x64_bf16 v[24:27], v[44:47], a[112:119], v7\n"
    "v_smfmac_f32_16x16x64_bf16 v[24:27], v[48:51], a[120:127], v7\n"
    "v_smfmac_f32_16x16x64_bf16 v[24:27], v[52:55], a[128:135], v7\n"
    "v_smfmac_f32_16x16x64_bf16 v[24:27], v[56:59], a[136:143], v7\n"
    "v_smfmac_f32_16x16x64_bf16 v[24:27], v[60:63], a[144:151], v7\n"
    "v_smfmac_f32_16x16x64_bf16 v[24:27], v[64:67], a[152:159], v7\n"
    "v_mov_b32 v15, v15\n"
    "v_mov_b32 v15, v15\n"
    "v_mov_b32 v15, v15\n"
    "v_mov_b32 v15, v15\n"
    "v_mov_b32 v15, v15\n"
    "v_mov_b32 v15, v15\n"
    "v_mov_b32 v15, v15\n"
    "v_mov_b32 v15, v15\n"
    "v_add_f32 v24, v24, v26\n"
    "v_add_f32 v25, v25, v27\n"
    "v_cvt_pk_bf16_f32 v24, v24, v25\n"
    "ds_write2st64_b32 v3, v24, v20 offset0:0 offset1:4\n"
    "s_waitcnt lgkmcnt(0)\n"
    "s_barrier\n"
    "s_cmp_eq_u32 s64, 63\n"
    "s_cbranch_scc1 L_exit_%=\n"
    "v_mov_b64 v[20:21], 0\n"
    "v_mov_b64 v[22:23], 0\n"
    "v_mov_b64 v[24:25], 0\n"
    "v_mov_b64 v[26:27], 0\n"
    "s_add_u32 s76, s71, s41\n"
    "buffer_store_dwordx4 v[76:79], v0, s[36:39], s76 offen offset:0 sc1\n"
    "buffer_store_dwordx4 v[76:79], v0, s[36:39], s76 offen offset:1024 sc1\n"
    "buffer_store_dwordx4 v[76:79], v0, s[36:39], s76 offen offset:2048 sc1\n"
    "buffer_store_dwordx4 v[76:79], v0, s[36:39], s76 offen offset:3072 sc1\n"
    "s_add_u32 s76, s68, s60\n"
    "buffer_store_dwordx4 v[76:79], v0, s[36:39], s76 offen sc1\n"
    "buffer_store_dwordx4 v[76:79], v9, s[36:39], s68 offen sc1\n"
    "R0_nocl_%=:\n"
    "s_add_u32 s80, s65, s41\n"
    "s_add_u32 s78, s66, 0x10000\n"
    "s_waitcnt vmcnt(0)\n"
    "s_barrier\n"
    "buffer_load_dwordx4 v[80:83], v0, s[36:39], s80 offen offset:0 sc1\n"
    "buffer_load_dwordx4 v[84:87], v0, s[36:39], s80 offen offset:1024 sc1\n"
    "buffer_load_dwordx4 v[88:91], v0, s[36:39], s80 offen offset:2048 sc1\n"
    "buffer_load_dwordx4 v[92:95], v0, s[36:39], s80 offen offset:3072 sc1\n"
    "s_waitcnt vmcnt(3)\n"
    "v_mfma_f32_16x16x32_bf16 v[72:75], v[80:83], v[16:19], 0\n"
    "s_waitcnt vmcnt(2)\n"
    "v_mfma_f32_16x16x32_bf16 v[72:75], v[84:87], v[16:19], v[72:75]\n"
    "s_waitcnt vmcnt(1)\n"
    "v_mfma_f32_16x16x32_bf16 v[72:75], v[88:91], v[16:19], v[72:75]\n"
    "s_waitcnt vmcnt(0)\n"
    "v_mfma_f32_16x16x32_bf16 v[72:75], v[92:95], v[16:19], v[72:75]\n"
    "s_nop 7\n"
    "v_permlane32_swap_b32 v72, v74\n"
    "v_permlane32_swap_b32 v73, v75\n"
    "v_pk_add_f32 v[72:73], v[72:73], v[74:75]\n"
    "s_nop 0\n"
    "v_cmp_u_f32 vcc, v72, v73\n"
    "v_cvt_pk_bf16_f32 v72, v72, v73\n"
    "s_cbranch_vccnz R0_redslow_%=\n"
    "R0_redok_%=:\n"
    "v_min_u32 v72, -2, v72\n"
    "buffer_store_dword v72, v6, s[36:39], s66 offen sc1\n"
    "buffer_store_dword v72, v6, s[36:39], s78 offen sc1\n"
    "R0_nored_%=:\n"
    "s_add_u32 s80, s66, s42\n"
    "s_add_u32 s64, s64, 1\n"
    "s_and_b32 s76, s64, 1\n"
    "s_mul_i32 s76, s76, 0xa0000\n"
    "s_add_u32 s65, s40, s76\n"
    "s_xor_b32 s76, s76, 0xa0000\n"
    "s_add_u32 s71, s40, s76\n"
    "s_and_b32 s76, s64, 3\n"
    "s_lshl_b32 s76, s76, 17\n"
    "s_or_b32 s76, s76, 0x400000\n"
    "s_add_u32 s66, s40, s76\n"
    "s_xor_b32 s76, s76, 0x40000\n"
    "s_add_u32 s68, s40, s76\n"
    "s_add_u32 s67, s66, s60\n"
    "s_mov_b32 s70, 0\n"
    "s_branch R0_wait_%=\n"
    "R0_miss_%=:\n"
    "s_cmp_lg_u32 s70, 0\n"
    "s_cbranch_scc1 R0_missok_%=\n"
    "R0_redo_%=:\n"
    "buffer_load_dwordx4 v[80:83], v0, s[36:39], s80 offen offset:0 sc1\n"
    "buffer_load_dwordx4 v[84:87], v0, s[36:39], s80 offen offset:1024 sc1\n"
    "s_add_u32 s88, s88, 1\n"
    "s_mov_b32 s79, 0\n"
    "s_mov_b32 s70, 400\n"
    "s_nop 7\n"
    "s_waitcnt lgkmcnt(0)\n"
    "s_barrier\n"
    "v_add_u32 v28, s43, v0\n"
    "s_cmp_eq_u32 s64, 0\n"
    "s_cbranch_scc1 R0_rdone_%=\n"
    "R0_rl_%=:\n"
    "s_waitcnt vmcnt(0)\n"
    "v_max3_u32 v29, v80, v81, v82\n"
    "v_max3_u32 v29, v29, v83, v84\n"
    "v_max3_u32 v29, v29, v85, v86\n"
    "v_max_u32 v29, v29, v87\n"
    "v_cmp_eq_u32 vcc, -1, v29\n"
    "s_cbranch_vccz R0_rw_%=\n"
    "buffer_load_dwordx4 v[80:83], v0, s[36:39], s80 offen offset:0 sc1\n"
    "buffer_load_dwordx4 v[84:87], v0, s[36:39], s80 offen offset:1024 sc1\n"
    "s_cmp_lg_u32 s69, 0\n"
    "s_cbranch_scc1 R0_rdone_%=\n"
    "s_add_u32 s79, s79, 1\n"
    "s_cmp_lt_u32 s79, 0x40000\n"
    "s_cbranch_scc1 R0_rl_%=\n"
    "s_mov_b32 s69, 1\n"
    "s_branch R0_rdone_%=\n"
    "R0_rw_%=:\n"
    "ds_write_b128 v28, v[80:83] offset:0\n"
    "ds_write_b128 v28, v[84:87] offset:1024\n"
    "R0_rdone_%=:\n"
    "s_waitcnt vmcnt(0) lgkmcnt(0)\n"
    "v_mov_b64 v[20:21], 0\n"
    "v_mov_b64 v[22:23], 0\n"
    "v_mov_b64 v[24:25], 0\n"
    "v_mov_b64 v[26:27], 0\n"
    "s_branch R0_wait_%=\n"
    "R0_redslow_%=:\n"
    "s_mov_b32 s79, 0\n"
    "R0_redl_%=:\n"
    "s_add_u32 s87, s87, 1\n"
    "v_max3_u32 v68, v80, v81, v82\n"
    "v_max3_u32 v68, v68, v83, v84\n"
    "v_max3_u32 v68, v68, v85, v86\n"
    "v_max3_u32 v68, v68, v87, v88\n"
    "v_max3_u32 v68, v68, v89, v90\n"
    "v_max3_u32 v68, v68, v91, v92\n"
    "v_max3_u32 v68, v68, v93, v94\n"
    "v_max_u32 v68, v68, v95\n"
    "v_cmp_eq_u32 vcc, -1, v68\n"
    "s_cbranch_vccz R0_redok_%=\n"
    "s_cmp_lg_u32 s69, 0\n"
    "s_cbranch_scc1 R0_redok_%=\n"
    "buffer_load_dwordx4 v[80:83], v0, s[36:39], s80 offen offset:0 sc1\n"
    "buffer_load_dwordx4 v[84:87], v0, s[36:39], s80 offen offset:1024 sc1\n"
    "buffer_load_dwordx4 v[88:91], v0, s[36:39], s80 offen offset:2048 sc1\n"
    "buffer_load_dwordx4 v[92:95], v0, s[36:39], s80 offen offset:3072 sc1\n"
    "s_waitcnt vmcnt(3)\n"
    "v_mfma_f32_16x16x32_bf16 v[72:75], v[80:83], v[16:19], 0\n"
    "s_waitcnt vmcnt(2)\n"
    "v_mfma_f32_16x16x32_bf16 v[72:75], v[84:87], v[16:19], v[72:75]\n"
    "s_waitcnt vmcnt(1)\n"
    "v_mfma_f32_16x16x32_bf16 v[72:75], v[88:91], v[16:19], v[72:75]\n"
    "s_waitcnt vmcnt(0)\n"
    "v_mfma_f32_16x16x32_bf16 v[72:75], v[92:95], v[16:19], v[72:75]\n"
    "s_nop 7\n"
    "v_permlane32_swap_b32 v72, v74\n"
    "v_permlane32_swap_b32 v73, v75\n"
    "v_pk_add_f32 v[72:73], v[72:73], v[74:75]\n"
    "s_nop 0\n"
    "v_cvt_pk_bf16_f32 v72, v72, v73\n"
    "s_cmp_lg_u32 s69, 0\n"
    "s_cbranch_scc1 R0_redok_%=\n"
    "s_add_u32 s79, s79, 1\n"
    "s_cmp_lt_u32 s79, 0x40000\n"
    "s_cbranch_scc1 R0_redl_%=\n"
    "s_mov_b32 s69, 1\n"
    "s_branch R0_redok_%=\n"
    "R1_entry_%=:\n"
    "v_mov_b64 v[20:21], 0\n"
    "v_mov_b64 v[22:23], 0\n"
    "v_mov_b64 v[24:25], 0\n"
    "v_mov_b64 v[26:27], 0\n"
    "s_waitcnt vmcnt(0)\n"
    "R1_wait_%=:\n"
    "s_barrier\n"
    "ds_read_b128 v[28:31], v12 offset:0\n"
    "ds_read_b128 v[32:35], v12 offset:1024\n"
    "ds_read_b128 v[36:39], v12 offset:2048\n"
    "ds_read_b128 v[40:43], v12 offset:3072\n"
    "s_waitcnt lgkmcnt(3)\n"
    "v_smfmac_f32_16x16x64_bf16 v[20:23], v[28:31], a[0:7], v7\n"
    "ds_read_b128 v[44:47], v12 offset:4096\n"
    "s_waitcnt lgkmcnt(3)\n"
    "v_smfmac_f32_16x16x64_bf16 v[20:23], v[32:35], a[8:15], v7\n"
    "ds_read_b128 v[48:51], v12 offset:5120\n"
    "s_waitcnt lgkmcnt(3)\n"
    "v_smfmac_f32_16x16x64_bf16 v[20:23], v[36:39], a[16:23], v7\n"
    "ds_read_b128 v[52:55], v12 offset:6144\n"
    "s_waitcnt lgkmcnt(3)\n"
    "v_smfmac_f32_16x16x64_bf16 v[20:23], v[40:43], a[24:31], v7\n"
    "ds_read_b128 v[56:59], v12 offset:7168\n"
    "s_waitcnt lgkmcnt(3)\n"
    "v_smfmac_f32_16x16x64_bf16 v[20:23], v[44:47], a[32:39], v7\n"
    "ds_read_b128 v[60:63], v12 offset:8192\n"
    "s_waitcnt lgkmcnt(3)\n"
    "v_smfmac_f32_16x16x64_bf16 v[20:23], v[48:51], a[40:47], v7\n"
    "ds_read_b128 v[64:67], v12 offset:9216\n"
    "s_waitcnt lgkmcnt(3)\n"
    "v_smfmac_f32_16x16x64_bf16 v[20:23], v[52:55], a[48:55], v7\n"
    "s_waitcnt lgkmcnt(2)\n"
    "v_smfmac_f32_16x16x64_bf16 v[20:23], v[56:59], a[56:63], v7\n"
    "s_waitcnt lgkmcnt(1)\n"
    "v_smfmac_f32_16x16x64_bf16 v[20:23], v[60:63], a[64:71], v7\n"
    "s_waitcnt lgkmcnt(0)\n"
    "v_smfmac_f32_16x16x64_bf16 v[20:23], v[64:67], a[72:79], v7\n"
    "v_smfmac_f32_16x16x64_bf16 v[24:27], v[28:31], a[80:87], v7\n"
    "v_smfmac_f32_16x16x64_bf16 v[24:27], v[32:35], a[88:95], v7\n"
    "s_nop 1\n"
    "v_add_f32 v20, v20, v22\n"
    "v_add_f32 v21, v21, v23\n"
    "v_cmp_u_f32 s[74:75], v20, v21\n"
    "v_cvt_pk_bf16_f32 v20, v20, v21\n"
    "v_min_u32 v20, -2, v20\n"
    "v_cndmask_b32 v21, v20, v76, s[74:75]\n"
    "buffer_store_dword v21, v8, s[36:39], s67 offen sc1\n"
    "v_max3_u32 v68, v28, v29, v30\n"
    "v_max3_u32 v68, v68, v31, v32\n"
    "v_max3_u32 v68, v68, v33, v34\n"
    "v_max3_u32 v68, v68, v35, v36\n"
    "v_max3_u32 v68, v68, v37, v38\n"
    "v_max3_u32 v68, v68, v39, v40\n"
    "v_max3_u32 v68, v68, v41, v42\n"
    "v_max3_u32 v68, v68, v43, v44\n"
    "v_max3_u32 v68, v68, v45, v46\n"
    "v_max3_u32 v68, v68, v47, v48\n"
    "v_max3_u32 v68, v68, v49, v50\n"
    "v_max3_u32 v68, v68, v51, v52\n"
    "v_max3_u32 v68, v68, v53, v54\n"
    "v_max3_u32 v68, v68, v55, v56\n"
    "v_max3_u32 v68, v68, v57, v58\n"
    "v_max3_u32 v68, v68, v59, v60\n"
    "v_max3_u32 v68, v68, v61, v62\n"
    "v_max3_u32 v68, v68, v63, v64\n"
    "v_max3_u32 v68, v68, v65, v66\n"
    "v_max_u32 v68, v68, v67\n"
    "v_cmp_eq_u32 vcc, -1, v68\n"
    "s_cbranch_vccnz R1_miss_%=\n"
    "R1_missok_%=:\n"
    "s_cmp_eq_u64 s[74:75], 0\n"
    "s_cbranch_scc1 R1_vclean_%=\n"
    "s_waitcnt vmcnt(0)\n"
    "buffer_store_dword v20, v8, s[36:39], s67 offen sc1\n"
    "R1_vclean_%=:\n"
    "v_smfmac_f32_16x16x64_bf16 v[24:27], v[36:39], a[96:103], v7\n"
    "v_smfmac_f32_16x16x64_bf16 v[24:27], v[40:43], a[104:111], v7\n"
    "v_smfmac_f32_16x16x64_bf16 v[24:27], v[44:47], a[112:119], v7\n"
    "v_smfmac_f32_16x16x64_bf16 v[24:27], v[48:51], a[120:127], v7\n"
    "v_smfmac_f32_16x16x64_bf16 v[24:27], v[52:55], a[128:135], v7\n"
    "v_smfmac_f32_16x16x64_bf16 v[24:27], v[56:59], a[136:143], v7\n"
    "v_smfmac_f32_16x16x64_bf16 v[24:27], v[60:63], a[144:151], v7\n"
    "v_smfmac_f32_16x16x64_bf16 v[24:27], v[64:67], a[152:159], v7\n"
    "v_mov_b32 v15, v15\n"
    "v_mov_b32 v15, v15\n"
    "v_mov_b32 v15, v15\n"
    "v_mov_b32 v15, v15\n"
    "v_mov_b32 v15, v15\n"
    "v_mov_b32 v15, v15\n"
    "v_mov_b32 v15, v15\n"
    "v_mov_b32 v15, v15\n"
    "v_add_f32 v24, v24, v26\n"
    "v_add_f32 v25, v25, v27\n"
    "v_cvt_pk_bf16_f32 v24, v24, v25\n"
    "ds_write2st64_b32 v3, v24, v20 offset0:0 offset1:4\n"
    "s_waitcnt lgkmcnt(0)\n"
    "s_barrier\n"
    "s_cmp_eq_u32 s64, 63\n"
    "s_cbranch_scc1 L_exit_%=\n"
    "v_mov_b64 v[20:21], 0\n"
    "v_mov_b64 v[22:23], 0\n"
    "v_mov_b64 v[24:25], 0\n"
    "v_mov_b64 v[26:27], 0\n"
    "s_cmp_eq_u32 s41, -1\n"
    "s_cbranch_scc1 R1_nocl_%=\n"
    "s_add_u32 s76, s71, s41\n"
    "buffer_store_dwordx4 v[76:79], v0, s[36:39], s76 offen offset:0 sc1\n"
    "buffer_store_dwordx4 v[76:79], v0, s[36:39], s76 offen offset:1024 sc1\n"
    "buffer_store_dwordx4 v[76:79], v0, s[36:39], s76 offen offset:2048 sc1\n"
    "buffer_store_dwordx4 v[76:79], v0, s[36:39], s76 offen offset:3072 sc1\n"
    "R1_nocl_%=:\n"
    "s_add_u32 s80, s65, s41\n"
    "s_add_u32 s78, s66, 0x10000\n"
    "s_waitcnt vmcnt(0)\n"
    "s_barrier\n"
    "s_cmp_eq_u32 s41, -1\n"
    "s_cbranch_scc1 R1_nored_%=\n"
    "buffer_load_dwordx4 v[80:83], v0, s[36:39], s80 offen offset:0 sc1\n"
    "buffer_load_dwordx4 v[84:87], v0, s[36:39], s80 offen offset:1024 sc1\n"
    "buffer_load_dwordx4 v[88:91], v0, s[36:39], s80 offen offset:2048 sc1\n"
    "buffer_load_dwordx4 v[92:95], v0, s[36:39], s80 offen offset:3072 sc1\n"
    "s_waitcnt vmcnt(3)\n"
    "v_mfma_f32_16x16x32_bf16 v[72:75], v[80:83], v[16:19], 0\n"
    "s_waitcnt vmcnt(2)\n"
    "v_mfma_f32_16x16x32_bf16 v[72:75], v[84:87], v[16:19], v[72:75]\n"
    "s_waitcnt vmcnt(1)\n"
    "v_mfma_f32_16x16x32_bf16 v[72:75], v[88:91], v[16:19], v[72:75]\n"
    "s_waitcnt vmcnt(0)\n"
    "v_mfma_f32_16x16x32_bf16 v[72:75], v[92:95], v[16:19], v[72:75]\n"
    "s_nop 7\n"
    "v_permlane32_swap_b32 v72, v74\n"
    "v_permlane32_swap_b32 v73, v75\n"
    "v_pk_add_f32 v[72:73], v[72:73], v[74:75]\n"
    "s_nop 0\n"
    "v_cmp_u_f32 vcc, v72, v73\n"
    "v_cvt_pk_bf16_f32 v72, v72, v73\n"
    "s_cbranch_vccnz R1_redslow_%=\n"
    "R1_redok_%=:\n"
    "v_min_u32 v72, -2, v72\n"
    "buffer_store_dword v72, v6, s[36:39], s66 offen sc1\n"
    "buffer_store_dword v72, v6, s[36:39], s78 offen sc1\n"
    "R1_nored_%=:\n"
    "s_add_u32 s80, s66, s42\n"
    "s_add_u32 s64, s64, 1\n"
    "s_and_b32 s76, s64, 1\n"
    "s_mul_i32 s76, s76, 0xa0000\n"
    "s_add_u32 s65, s40, s76\n"
    "s_xor_b32 s76, s76, 0xa0000\n"
    "s_add_u32 s71, s40, s76\n"
    "s_and_b32 s76, s64, 3\n"
    "s_lshl_b32 s76, s76, 17\n"
    "s_or_b32 s76, s76, 0x400000\n"
    "s_add_u32 s66, s40, s76\n"
    "s_xor_b32 s76, s76, 0x40000\n"
    "s_add_u32 s68, s40, s76\n"
    "s_add_u32 s67, s66, s60\n"
    "s_mov_b32 s70, 0\n"
    "s_branch R1_wait_%=\n"
    "R1_miss_%=:\n"
    "s_cmp_lg_u32 s70, 0\n"
    "s_cbranch_scc1 R1_missok_%=\n"
    "R1_redo_%=:\n"
    "buffer_load_dwordx4 v[80:83], v0, s[36:39], s80 offen offset:0 sc1\n"
    "buffer_load_dwordx4 v[84:87], v0, s[36:39], s80 offen offset:1024 sc1\n"
    "s_add_u32 s88, s88, 1\n"
    "s_mov_b32 s79, 0\n"
    "s_mov_b32 s70, 400\n"
    "s_nop 7\n"
    "s_waitcnt lgkmcnt(0)\n"
    "s_barrier\n"
    "v_add_u32 v28, s43, v0\n"
    "s_cmp_eq_u32 s64, 0\n"
    "s_cbranch_scc1 R1_rdone_%=\n"
    "R1_rl_%=:\n"
    "s_waitcnt vmcnt(0)\n"
    "v_max3_u32 v29, v80, v81, v82\n"
    "v_max3_u32 v29, v29, v83, v84\n"
    "v_max3_u32 v29, v29, v85, v86\n"
    "v_max_u32 v29, v29, v87\n"
    "v_cmp_eq_u32 vcc, -1, v29\n"
    "s_cbranch_vccz R1_rw_%=\n"
    "buffer_load_dwordx4 v[80:83], v0, s[36:39], s80 offen offset:0 sc1\n"
    "buffer_load_dwordx4 v[84:87], v0, s[36:39], s80 offen offset:1024 sc1\n"
    "s_cmp_lg_u32 s69, 0\n"
    "s_cbranch_scc1 R1_rdone_%=\n"
    "s_add_u32 s79, s79, 1\n"
    "s_cmp_lt_u32 s79, 0x40000\n"
    "s_cbranch_scc1 R1_rl_%=\n"
    "s_mov_b32 s69, 1\n"
    "s_branch R1_rdone_%=\n"
    "R1_rw_%=:\n"
    "ds_write_b128 v28, v[80:83] offset:0\n"
    "ds_write_b128 v28, v[84:87] offset:1024\n"
    "R1_rdone_%=:\n"
    "s_waitcnt vmcnt(0) lgkmcnt(0)\n"
    "v_mov_b64 v[20:21], 0\n"
    "v_mov_b64 v[22:23], 0\n"
    "v_mov_b64 v[24:25], 0\n"
    "v_mov_b64 v[26:27], 0\n"
    "s_branch R1_wait_%=\n"
    "R1_redslow_%=:\n"
    "s_mov_b32 s79, 0\n"
    "R1_redl_%=:\n"
    "s_add_u32 s87, s87, 1\n"
    "v_max3_u32 v68, v80, v81, v82\n"
    "v_max3_u32 v68, v68, v83, v84\n"
    "v_max3_u32 v68, v68, v85, v86\n"
    "v_max3_u32 v68, v68, v87, v88\n"
    "v_max3_u32 v68, v68, v89, v90\n"
    "v_max3_u32 v68, v68, v91, v92\n"
    "v_max3_u32 v68, v68, v93, v94\n"
    "v_max_u32 v68, v68, v95\n"
    "v_cmp_eq_u32 vcc, -1, v68\n"
    "s_cbranch_vccz R1_redok_%=\n"
    "s_cmp_lg_u32 s69, 0\n"
    "s_cbranch_scc1 R1_redok_%=\n"
    "buffer_load_dwordx4 v[80:83], v0, s[36:39], s80 offen offset:0 sc1\n"
    "buffer_load_dwordx4 v[84:87], v0, s[36:39], s80 offen offset:1024 sc1\n"
    "buffer_load_dwordx4 v[88:91], v0, s[36:39], s80 offen offset:2048 sc1\n"
    "buffer_load_dwordx4 v[92:95], v0, s[36:39], s80 offen offset:3072 sc1\n"
    "s_waitcnt vmcnt(3)\n"
    "v_mfma_f32_16x16x32_bf16 v[72:75], v[80:83], v[16:19], 0\n"
    "s_waitcnt vmcnt(2)\n"
    "v_mfma_f32_16x16x32_bf16 v[72:75], v[84:87], v[16:19], v[72:75]\n"
    "s_waitcnt vmcnt(1)\n"
    "v_mfma_f32_16x16x32_bf16 v[72:75], v[88:91], v[16:19], v[72:75]\n"
    "s_waitcnt vmcnt(0)\n"
    "v_mfma_f32_16x16x32_bf16 v[72:75], v[92:95], v[16:19], v[72:75]\n"
    "s_nop 7\n"
    "v_permlane32_swap_b32 v72, v74\n"
    "v_permlane32_swap_b32 v73, v75\n"
    "v_pk_add_f32 v[72:73], v[72:73], v[74:75]\n"
    "s_nop 0\n"
    "v_cvt_pk_bf16_f32 v72, v72, v73\n"
    "s_cmp_lg_u32 s69, 0\n"
    "s_cbranch_scc1 R1_redok_%=\n"
    "s_add_u32 s79, s79, 1\n"
    "s_cmp_lt_u32 s79, 0x40000\n"
    "s_cbranch_scc1 R1_redl_%=\n"
    "s_mov_b32 s69, 1\n"
    "s_branch R1_redok_%=\n"
    "R2_entry_%=:\n"
    "v_mov_b64 v[20:21], 0\n"
    "v_mov_b64 v[22:23], 0\n"
    "v_mov_b64 v[24:25], 0\n"
    "v_mov_b64 v[26:27], 0\n"
    "s_waitcnt vmcnt(0)\n"
    "R2_wait_%=:\n"
    "s_barrier\n"
    "ds_read_b128 v[28:31], v12 offset:0\n"
    "ds_read_b128 v[32:35], v12 offset:1024\n"
    "ds_read_b128 v[36:39], v12 offset:2048\n"
    "ds_read_b128 v[40:43], v12 offset:3072\n"
    "s_waitcnt lgkmcnt(3)\n"
    "v_smfmac_f32_16x16x64_bf16 v[20:23], v[28:31], a[0:7], v7\n"
    "ds_read_b128 v[44:47], v12 offset:4096\n"
    "s_waitcnt lgkmcnt(3)\n"
    "v_smfmac_f32_16x16x64_bf16 v[20:23], v[32:35], a[8:15], v7\n"
    "ds_read_b128 v[48:51], v12 offset:5120\n"
    "s_waitcnt lgkmcnt(3)\n"
    "v_smfmac_f32_16x16x64_bf16 v[20:23], v[36:39], a[16:23], v7\n"
    "ds_read_b128 v[52:55], v12 offset:6144\n"
    "s_waitcnt lgkmcnt(3)\n"
    "v_smfmac_f32_16x16x64_bf16 v[20:23], v[40:43], a[24:31], v7\n"
    "ds_read_b128 v[56:59], v12 offset:7168\n"
    "s_waitcnt lgkmcnt(3)\n"
    "v_smfmac_f32_16x16x64_bf16 v[20:23], v[44:47], a[32:39], v7\n"
    "ds_read_b128 v[60:63], v12 offset:8192\n"
    "s_waitcnt lgkmcnt(3)\n"
    "v_smfmac_f32_16x16x64_bf16 v[20:23], v[48:51], a[40:47], v7\n"
    "ds_read_b128 v[64:67], v12 offset:9216\n"
    "s_waitcnt lgkmcnt(3)\n"
    "v_smfmac_f32_16x16x64_bf16 v[20:23], v[52:55], a[48:55], v7\n"
    "s_waitcnt lgkmcnt(2)\n"
    "v_smfmac_f32_16x16x64_bf16 v[20:23], v[56:59], a[56:63], v7\n"
    "s_waitcnt lgkmcnt(1)\n"
    "v_smfmac_f32_16x16x64_bf16 v[20:23], v[60:63], a[64:71], v7\n"
    "s_waitcnt lgkmcnt(0)\n"
    "v_smfmac_f32_16x16x64_bf16 v[20:23], v[64:67], a[72:79], v7\n"
    "v_smfmac_f32_16x16x64_bf16 v[24:27], v[28:31], a[80:87], v7\n"
    "v_smfmac_f32_16x16x64_bf16 v[24:27], v[32:35], a[88:95], v7\n"
    "s_nop 1\n"
    "v_add_f32 v20, v20, v22\n"
    "v_add_f32 v21, v21, v23\n"
    "v_cmp_u_f32 s[74:75], v20, v21\n"
    "v_cvt_pk_bf16_f32 v20, v20, v21\n"
    "v_min_u32 v20, -2, v20\n"
    "v_cndmask_b32 v21, v20, v76, s[74:75]\n"
    "buffer_store_dword v21, v8, s[36:39], s67 offen sc1\n"
    "v_max3_u32 v68, v28, v29, v30\n"
    "v_max3_u32 v68, v68, v31, v32\n"
    "v_max3_u32 v68, v68, v33, v34\n"
    "v_max3_u32 v68, v68, v35, v36\n"
    "v_max3_u32 v68, v68, v37, v38\n"
    "v_max3_u32 v68, v68, v39, v40\n"
    "v_max3_u32 v68, v68, v41, v42\n"
    "v_max3_u32 v68, v68, v43, v44\n"
    "v_max3_u32 v68, v68, v45, v46\n"
    "v_max3_u32 v68, v68, v47, v48\n"
    "v_max3_u32 v68, v68, v49, v50\n"
    "v_max3_u32 v68, v68, v51, v52\n"
    "v_max3_u32 v68, v68, v53, v54\n"
    "v_max3_u32 v68, v68, v55, v56\n"
    "v_max3_u32 v68, v68, v57, v58\n"
    "v_max3_u32 v68, v68, v59, v60\n"
    "v_max3_u32 v68, v68, v61, v62\n"
    "v_max3_u32 v68, v68, v63, v64\n"
    "v_max3_u32 v68, v68, v65, v66\n"
    "v_max_u32 v68, v68, v67\n"
    "v_cmp_eq_u32 vcc, -1, v68\n"
    "s_cbranch_vccnz R2_miss_%=\n"
    "R2_missok_%=:\n"
    "s_cmp_eq_u64 s[74:75], 0\n"
    "s_cbranch_scc1 R2_vclean_%=\n"
    "s_waitcnt vmcnt(0)\n"
    "buffer_store_dword v20, v8, s[36:39], s67 offen sc1\n"
    "R2_vclean_%=:\n"
    "v_smfmac_f32_16x16x64_bf16 v[24:27], v[36:39], a[96:103], v7\n"
    "v_smfmac_f32_16x16x64_bf16 v[24:27], v[40:43], a[104:111], v7\n"
    "v_smfmac_f32_16x16x64_bf16 v[24:27], v[44:47], a[112:119], v7\n"
    "v_smfmac_f32_16x16x64_bf16 v[24:27], v[48:51], a[120:127], v7\n"
    "v_smfmac_f32_16x16x64_bf16 v[24:27], v[52:55], a[128:135], v7\n"
    "v_smfmac_f32_16x16x64_bf16 v[24:27], v[56:59], a[136:143], v7\n"
    "v_smfmac_f32_16x16x64_bf16 v[24:27], v[60:63], a[144:151], v7\n"
    "v_smfmac_f32_16x16x64_bf16 v[24:27], v[64:67], a[152:159], v7\n"
    "v_mov_b32 v15, v15\n"
    "v_mov_b32 v15, v15\n"
    "v_mov_b32 v15, v15\n"
    "v_mov_b32 v15, v15\n"
    "v_mov_b32 v15, v15\n"
    "v_mov_b32 v15, v15\n"
    "v_mov_b32 v15, v15\n"
    "v_mov_b32 v15, v15\n"
    "v_add_f32 v24, v24, v26\n"
    "v_add_f32 v25, v25, v27\n"
    "v_cvt_pk_bf16_f32 v24, v24, v25\n"
    "ds_write2st64_b32 v3, v24, v20 offset0:0 offset1:4\n"
    "s_waitcnt lgkmcnt(0)\n"
    "s_barrier\n"
    "ds_read_b64_tr_b16 v[80:81], v4 offset:0\n"
    "ds_read_b64_tr_b16 v[82:83], v4 offset:256\n"
    "ds_read_b128 v[84:87], v0 offset:1024\n"
    "s_lshl_b32 s76, s64, 16\n"
    "s_add_u32 s77, s76, s49\n"
    "s_add_u32 s76, s76, s48\n"
    "s_waitcnt lgkmcnt(0)\n"
    "buffer_store_dwordx4 v[80:83], v5, s[36:39], s76 offen sc1\n"
    "buffer_store_dwordx4 v[84:87], v0, s[36:39], s77 offen sc1\n"
    "s_add_u32 s76, s64, 1\n"
    "v_mov_b32 v88, s76\n"
    "s_waitcnt vmcnt(0)\n"
    "s_mov_b64 exec, 1\n"
    "buffer_store_dword v88, off, s[36:39], s53 sc1\n"
    "s_mov_b64 exec, -1\n"
    "s_cmp_eq_u32 s64, 63\n"
    "s_cbranch_scc1 L_exit_%=\n"
    "v_mov_b64 v[20:21], 0\n"
    "v_mov_b64 v[22:23], 0\n"
    "v_mov_b64 v[24:25], 0\n"
    "v_mov_b64 v[26:27], 0\n"
    "s_barrier\n"
    "s_add_u32 s80, s66, s42\n"
    "s_add_u32 s64, s64, 1\n"
    "s_and_b32 s76, s64, 1\n"
    "s_mul_i32 s76, s76, 0xa0000\n"
    "s_add_u32 s65, s40, s76\n"
    "s_xor_b32 s76, s76, 0xa0000\n"
    "s_add_u32 s71, s40, s76\n"
    "s_and_b32 s76, s64, 3\n"
    "s_lshl_b32 s76, s76, 17\n"
    "s_or_b32 s76, s76, 0x400000\n"
    "s_add_u32 s66, s40, s76\n"
    "s_xor_b32 s76, s76, 0x40000\n"
    "s_add_u32 s68, s40, s76\n"
    "s_add_u32 s67, s66, s60\n"
    "s_mov_b32 s70, 0\n"
    "s_branch R2_wait_%=\n"
    "R2_miss_%=:\n"
    "s_cmp_lg_u32 s70, 0\n"
    "s_cbranch_scc1 R2_missok_%=\n"
    "R2_redo_%=:\n"
    "buffer_load_dwordx4 v[80:83], v0, s[36:39], s80 offen offset:0 sc1\n"
    "buffer_load_dwordx4 v[84:87], v0, s[36:39], s80 offen offset:1024 sc1\n"
    "buffer_load_dwordx4 v[88:91], v0, s[36:39], s80 offen offset:2048 sc1\n"
    "s_add_u32 s88, s88, 1\n"
    "s_mov_b32 s79, 0\n"
    "s_mov_b32 s70, 400\n"
    "s_nop 7\n"
    "s_waitcnt lgkmcnt(0)\n"
    "s_barrier\n"
    "v_add_u32 v28, s43, v0\n"
    "s_cmp_eq_u32 s64, 0\n"
    "s_cbranch_scc1 R2_rdone_%=\n"
    "R2_rl_%=:\n"
    "s_waitcnt vmcnt(0)\n"
    "v_max3_u32 v29, v80, v81, v82\n"
    "v_max3_u32 v29, v29, v83, v84\n"
    "v_max3_u32 v29, v29, v85, v86\n"
    "v_max3_u32 v29, v29, v87, v88\n"
    "v_max3_u32 v29, v29, v89, v90\n"
    "v_max_u32 v29, v29, v91\n"
    "v_cmp_eq_u32 vcc, -1, v29\n"
    "s_cbranch_vccz R2_rw_%=\n"
    "buffer_load_dwordx4 v[80:83], v0, s[36:39], s80 offen offset:0 sc1\n"
    "buffer_load_dwordx4 v[84:87], v0, s[36:39], s80 offen offset:1024 sc1\n"
    "buffer_load_dwordx4 v[88:91], v0, s[36:39], s80 offen offset:2048 sc1\n"
    "s_cmp_lg_u32 s69, 0\n"
    "s_cbranch_scc1 R2_rdone_%=\n"
    "s_add_u32 s79, s79, 1\n"
    "s_cmp_lt_u32 s79, 0x40000\n"
    "s_cbranch_scc1 R2_rl_%=\n"
    "s_mov_b32 s69, 1\n"
    "s_branch R2_rdone_%=\n"
    "R2_rw_%=:\n"
    "ds_write_b128 v28, v[80:83] offset:0\n"
    "ds_write_b128 v28, v[84:87] offset:1024\n"
    "ds_write_b128 v28, v[88:91] offset:2048\n"
    "R2_rdone_%=:\n"
    "s_waitcnt vmcnt(0) lgkmcnt(0)\n"
    "v_mov_b64 v[20:21], 0\n"
    "v_mov_b64 v[22:23], 0\n"
    "v_mov_b64 v[24:25], 0\n"
    "v_mov_b64 v[26:27], 0\n"
    "s_branch R2_wait_%=\n"
    "R3_entry_%=:\n"
    "s_memrealtime s[76:77]\n"
    "s_waitcnt lgkmcnt(0)\n"
    "R3_cali_%=:\n"
    "s_memrealtime s[78:79]\n"
    "s_memtime s[80:81]\n"
    "s_waitcnt lgkmcnt(0)\n"
    "s_cmp_lg_u32 s78, s76\n"
    "s_cbranch_scc1 R3_caldi_%=\n"
    "s_memrealtime s[78:79]\n"
    "s_memtime s[80:81]\n"
    "s_waitcnt lgkmcnt(0)\n"
    "s_cmp_lg_u32 s78, s76\n"
    "s_cbranch_scc1 R3_caldi_%=\n"
    "s_memrealtime s[78:79]\n"
    "s_memtime s[80:81]\n"
    "s_waitcnt lgkmcnt(0)\n"
    "s_cmp_lg_u32 s78, s76\n"
    "s_cbranch_scc1 R3_caldi_%=\n"
    "s_memrealtime s[78:79]\n"
    "s_memtime s[80:81]\n"
    "s_waitcnt lgkmcnt(0)\n"
    "s_cmp_lg_u32 s78, s76\n"
    "s_cbranch_scc1 R3_caldi_%=\n"
    "s_branch R3_cali_%=\n"
    "R3_caldi_%=:\n"
    "s_lshl_b32 s78, s78, 5\n"
    "s_mov_b32 s82, s78\n"
    "s_mov_b32 s83, s80\n"
    "s_mov_b32 s80, s66\n"
    "v_mov_b64 v[20:21], 0\n"
    "v_mov_b64 v[22:23], 0\n"
    "v_mov_b64 v[24:25], 0\n"
    "v_mov_b64 v[26:27], 0\n"
    "s_waitcnt vmcnt(0)\n"
    "R3_wait_%=:\n"
    "s_waitcnt vmcnt(0)\n"
    "s_barrier\n"
    "s_memtime s[84:85]\n"
    "ds_read_b128 v[28:31], v12 offset:0\n"
    "ds_read_b128 v[32:35], v12 offset:1024\n"
    "ds_read_b128 v[36:39], v12 offset:2048\n"
    "ds_read_b128 v[40:43], v12 offset:3072\n"
    "s_waitcnt lgkmcnt(3)\n"
    "v_smfmac_f32_16x16x64_bf16 v[20:23], v[28:31], a[0:7], v7\n"
    "ds_read_b128 v[44:47], v12 offset:4096\n"
    "s_waitcnt lgkmcnt(3)\n"
    "v_smfmac_f32_16x16x64_bf16 v[20:23], v[32:35], a[8:15], v7\n"
    "ds_read_b128 v[48:51], v12 offset:5120\n"
    "s_waitcnt lgkmcnt(3)\n"
    "v_smfmac_f32_16x16x64_bf16 v[20:23], v[36:39], a[16:23], v7\n"
    "ds_read_b128 v[52:55], v12 offset:6144\n"
    "s_waitcnt lgkmcnt(3)\n"
    "v_smfmac_f32_16x16x64_bf16 v[20:23], v[40:43], a[24:31], v7\n"
    "ds_read_b128 v[56:59], v12 offset:7168\n"
    "s_waitcnt lgkmcnt(3)\n"
    "v_smfmac_f32_16x16x64_bf16 v[20:23], v[44:47], a[32:39], v7\n"
    "ds_read_b128 v[60:63], v12 offset:8192\n"
    "s_waitcnt lgkmcnt(3)\n"
    "v_smfmac_f32_16x16x64_bf16 v[20:23], v[48:51], a[40:47], v7\n"
    "ds_read_b128 v[64:67], v12 offset:9216\n"
    "s_waitcnt lgkmcnt(3)\n"
    "v_smfmac_f32_16x16x64_bf16 v[20:23], v[52:55], a[48:55], v7\n"
    "s_waitcnt lgkmcnt(2)\n"
    "v_smfmac_f32_16x16x64_bf16 v[20:23], v[56:59], a[56:63], v7\n"
    "s_waitcnt lgkmcnt(1)\n"
    "v_smfmac_f32_16x16x64_bf16 v[20:23], v[60:63], a[64:71], v7\n"
    "s_waitcnt lgkmcnt(0)\n"
    "v_smfmac_f32_16x16x64_bf16 v[20:23], v[64:67], a[72:79], v7\n"
    "v_smfmac_f32_16x16x64_bf16 v[24:27], v[28:31], a[80:87], v7\n"
    "v_smfmac_f32_16x16x64_bf16 v[24:27], v[32:35], a[88:95], v7\n"
    "s_nop 1\n"
    "v_add_f32 v20, v20, v22\n"
    "v_add_f32 v21, v21, v23\n"
    "v_cmp_u_f32 s[74:75], v20, v21\n"
    "v_cvt_pk_bf16_f32 v20, v20, v21\n"
    "v_min_u32 v20, -2, v20\n"
    "v_cndmask_b32 v21, v20, v76, s[74:75]\n"
    "buffer_store_dword v21, v8, s[36:39], s67 offen sc1\n"
    "v_max3_u32 v68, v28, v29, v30\n"
    "v_max3_u32 v68, v68, v31, v32\n"
    "v_max3_u32 v68, v68, v33, v34\n"
    "v_max3_u32 v68, v68, v35, v36\n"
    "v_max3_u32 v68, v68, v37, v38\n"
    "v_max3_u32 v68, v68, v39, v40\n"
    "v_max3_u32 v68, v68, v41, v42\n"
    "v_max3_u32 v68, v68, v43, v44\n"
    "v_max3_u32 v68, v68, v45, v46\n"
    "v_max3_u32 v68, v68, v47, v48\n"
    "v_max3_u32 v68, v68, v49, v50\n"
    "v_max3_u32 v68, v68, v51, v52\n"
    "v_max3_u32 v68, v68, v53, v54\n"
    "v_max3_u32 v68, v68, v55, v56\n"
    "v_max3_u32 v68, v68, v57, v58\n"
    "v_max3_u32 v68, v68, v59, v60\n"
    "v_max3_u32 v68, v68, v61, v62\n"
    "v_max3_u32 v68, v68, v63, v64\n"
    "v_max3_u32 v68, v68, v65, v66\n"
    "v_max_u32 v68, v68, v67\n"
    "v_cmp_eq_u32 vcc, -1, v68\n"
    "s_cbranch_vccnz R3_miss_%=\n"
    "R3_missok_%=:\n"
    "s_cmp_eq_u64 s[74:75], 0\n"
    "s_cbranch_scc1 R3_vclean_%=\n"
    "s_waitcnt vmcnt(0)\n"
    "buffer_store_dword v20, v8, s[36:39], s67 offen sc1\n"
    "R3_vclean_%=:\n"
    "s_sub_u32 s76, s84, s83\n"
    "s_add_u32 s76, s76, s57\n"
    "s_mul_i32 s76, s76, s55\n"
    "s_lshr_b32 s76, s76, 11\n"
    "s_add_u32 s76, s76, s82\n"
    "s_add_u32 s76, s76, 0\n"
    "s_add_u32 s76, s76, s70\n"
    "s_bitset0_b32 s76, 0\n"
    "s_add_u32 s78, s66, s62\n"
    "v_mov_b32 v80, s76\n"
    "s_mov_b64 exec, 1\n"
    "buffer_store_dword v80, off, s[36:39], s78 sc1\n"
    "s_mov_b64 exec, -1\n"
    "v_smfmac_f32_16x16x64_bf16 v[24:27], v[36:39], a[96:103], v7\n"
    "v_smfmac_f32_16x16x64_bf16 v[24:27], v[40:43], a[104:111], v7\n"
    "v_smfmac_f32_16x16x64_bf16 v[24:27], v[44:47], a[112:119], v7\n"
    "v_smfmac_f32_16x16x64_bf16 v[24:27], v[48:51], a[120:127], v7\n"
    "v_smfmac_f32_16x16x64_bf16 v[24:27], v[52:55], a[128:135], v7\n"
    "v_smfmac_f32_16x16x64_bf16 v[24:27], v[56:59], a[136:143], v7\n"
    "v_smfmac_f32_16x16x64_bf16 v[24:27], v[60:63], a[144:151], v7\n"
    "v_smfmac_f32_16x16x64_bf16 v[24:27], v[64:67], a[152:159], v7\n"
    "v_mov_b32 v15, v15\n"
    "v_mov_b32 v15, v15\n"
    "v_mov_b32 v15, v15\n"
    "v_mov_b32 v15, v15\n"
    "v_mov_b32 v15, v15\n"
    "v_mov_b32 v15, v15\n"
    "v_mov_b32 v15, v15\n"
    "v_mov_b32 v15, v15\n"
    "v_add_f32 v24, v24, v26\n"
    "v_add_f32 v25, v25, v27\n"
    "v_cvt_pk_bf16_f32 v24, v24, v25\n"
    "ds_write2st64_b32 v3, v24, v20 offset0:0 offset1:4\n"
    "s_waitcnt lgkmcnt(0)\n"
    "s_barrier\n"
    "s_cmp_eq_u32 s64, 63\n"
    "s_cbranch_scc1 L_exit_%=\n"
    "v_mov_b64 v[20:21], 0\n"
    "v_mov_b64 v[22:23], 0\n"
    "v_mov_b64 v[24:25], 0\n"
    "v_mov_b64 v[26:27], 0\n"
    "s_add_u32 s86, s84, 1000\n"
    "s_mov_b32 s72, s82\n"
    "s_mov_b32 s73, s83\n"
    "R3_wct_%=:\n"
    "s_memtime s[76:77]\n"
    "s_getpc_b64 s[78:79]\n"
    "R3_wpt_%=:\n"
    "s_add_u32 s78, s78, R3_wet_%=-R3_wpt_%=\n"
    "s_addc_u32 s79, s79, 0\n"
    "s_waitcnt lgkmcnt(0)\n"
    "s_sub_u32 s76, s86, s76\n"
    "s_max_i32 s76, s76, 0\n"
    "s_lshr_b32 s76, s76, 3\n"
    "s_cmp_gt_u32 s76, 40\n"
    "s_cbranch_scc1 R3_wct_%=\n"
    "s_lshl_b32 s76, s76, 2\n"
    "s_sub_u32 s78, s78, s76\n"
    "s_subb_u32 s79, s79, 0\n"
    "s_setpc_b64 s[78:79]\n"
    ".rept 40\n"
    "s_nop 1\n"
    ".endr\n"
    "R3_wet_%=:\n"
    "s_mov_b32 s79, 0\n"
    "s_add_u32 s78, s66, 18432\n"
    "R3_tabl_%=:\n"
    "buffer_load_dword v80, v0, s[36:39], s78 offen sc1\n"
    "s_waitcnt vmcnt(0)\n"
    "R3_tabc_%=:\n"
    "v_cmp_eq_u32 vcc, -1, v80\n"
    "s_cbranch_vccz R3_tabok_%=\n"
    "s_add_u32 s89, s89, 1\n"
    "s_sleep 1\n"
    "s_cmp_lg_u32 s69, 0\n"
    "s_cbranch_scc1 R3_tabok_%=\n"
    "s_add_u32 s79, s79, 1\n"
    "s_cmp_lt_u32 s79, 0x40000\n"
    "s_cbranch_scc1 R3_tabl_%=\n"
    "s_mov_b32 s69, 1\n"
    "s_branch R3_tabok_%=\n"
    "R3_tabok_%=:\n"
    "v_subrev_u32 v80, s82, v80\n"
    "s_nop 1\n"
    "v_max_i32_dpp v80, v80, v80 row_shr:1 row_mask:0xf bank_mask:0xf\n"
    "s_nop 1\n"
    "v_max_i32_dpp v80, v80, v80 row_shr:2 row_mask:0xf bank_mask:0xf\n"
    "s_nop 1\n"
    "v_max_i32_dpp v80, v80, v80 row_shr:4 row_mask:0xf bank_mask:0xf\n"
    "s_nop 1\n"
    "v_max_i32_dpp v80, v80, v80 row_shr:8 row_mask:0xf bank_mask:0xf\n"
    "s_nop 1\n"
    "v_max_i32_dpp v80, v80, v80 row_bcast:15 row_mask:0xa bank_mask:0xf\n"
    "s_nop 1\n"
    "v_max_i32_dpp v80, v80, v80 row_bcast:31 row_mask:0xc bank_mask:0xf\n"
    "s_nop 0\n"
    "v_readlane_b32 s86, v80, 63\n"
    "s_nop 1\n"
    "s_add_u32 s86, s86, s82\n"
    "s_add_u32 s86, s86, s58\n"
    "s_memrealtime s[76:77]\n"
    "s_waitcnt lgkmcnt(0)\n"
    "R3_cala_%=:\n"
    "s_memrealtime s[78:79]\n"
    "s_memtime s[80:81]\n"
    "s_waitcnt lgkmcnt(0)\n"
    "s_cmp_lg_u32 s78, s76\n"
    "s_cbranch_scc1 R3_calda_%=\n"
    "s_memrealtime s[78:79]\n"
    "s_memtime s[80:81]\n"
    "s_waitcnt lgkmcnt(0)\n"
    "s_cmp_lg_u32 s78, s76\n"
    "s_cbranch_scc1 R3_calda_%=\n"
    "s_memrealtime s[78:79]\n"
    "s_memtime s[80:81]\n"
    "s_waitcnt lgkmcnt(0)\n"
    "s_cmp_lg_u32 s78, s76\n"
    "s_cbranch_scc1 R3_calda_%=\n"
    "s_memrealtime s[78:79]\n"
    "s_memtime s[80:81]\n"
    "s_waitcnt lgkmcnt(0)\n"
    "s_cmp_lg_u32 s78, s76\n"
    "s_cbranch_scc1 R3_calda_%=\n"
    "s_branch R3_cala_%=\n"
    "R3_calda_%=:\n"
    "s_lshl_b32 s78, s78, 5\n"
    "s_mov_b32 s82, s78\n"
    "s_mov_b32 s83, s80\n"
    "s_sub_u32 s84, s86, s82\n"
    "s_mul_i32 s84, s84, 3\n"
    "s_ashr_i32 s84, s84, 2\n"
    "s_min_i32 s84, s84, 0x4000\n"
    "s_add_u32 s84, s84, s83\n"
    "R3_wca_%=:\n"
    "s_memtime s[76:77]\n"
    "s_getpc_b64 s[78:79]\n"
    "R3_wpa_%=:\n"
    "s_add_u32 s78, s78, R3_wea_%=-R3_wpa_%=\n"
    "s_addc_u32 s79, s79, 0\n"
    "s_waitcnt lgkmcnt(0)\n"
    "s_sub_u32 s76, s84, s76\n"
    "s_max_i32 s76, s76, 0\n"
    "s_lshr_b32 s76, s76, 3\n"
    "s_cmp_gt_u32 s76, 40\n"
    "s_cbranch_scc1 R3_wca_%=\n"
    "s_lshl_b32 s76, s76, 2\n"
    "s_sub_u32 s78, s78, s76\n"
    "s_subb_u32 s79, s79, 0\n"
    "s_setpc_b64 s[78:79]\n"
    ".rept 40\n"
    "s_nop 1\n"
    ".endr\n"
    "R3_wea_%=:\n"
    "s_barrier\n"
    "s_add_u32 s86, s86, s59\n"
    "s_sleep 4\n"
    "s_mov_b32 s82, s72\n"
    "s_mov_b32 s83, s73\n"
    "s_memrealtime s[76:77]\n"
    "s_waitcnt lgkmcnt(0)\n"
    "R3_calb_%=:\n"
    "s_memrealtime s[78:79]\n"
    "s_memtime s[80:81]\n"
    "s_waitcnt lgkmcnt(0)\n"
    "s_cmp_lg_u32 s78, s76\n"
    "s_cbranch_scc1 R3_caldb_%=\n"
    "s_memrealtime s[78:79]\n"
    "s_memtime s[80:81]\n"
    "s_waitcnt lgkmcnt(0)\n"
    "s_cmp_lg_u32 s78, s76\n"
    "s_cbranch_scc1 R3_caldb_%=\n"
    "s_memrealtime s[78:79]\n"
    "s_memtime s[80:81]\n"
    "s_waitcnt lgkmcnt(0)\n"
    "s_cmp_lg_u32 s78, s76\n"
    "s_cbranch_scc1 R3_caldb_%=\n"
    "s_memrealtime s[78:79]\n"
    "s_memtime s[80:81]\n"
    "s_waitcnt lgkmcnt(0)\n"
    "s_cmp_lg_u32 s78, s76\n"
    "s_cbranch_scc1 R3_caldb_%=\n"
    "s_branch R3_calb_%=\n"
    "R3_caldb_%=:\n"
    "s_lshl_b32 s78, s78, 5\n"
    "s_sub_u32 s76, s80, s83\n"
    "s_mul_i32 s76, s76, s55\n"
    "s_lshr_b32 s76, s76, 11\n"
    "s_sub_u32 s77, s78, s82\n"
    "s_sub_u32 s76, s77, s76\n"
    "s_ashr_i32 s76, s76, 3\n"
    "s_max_i32 s76, s76, -64\n"
    "s_min_i32 s76, s76, 64\n"
    "s_add_u32 s55, s55, s76\n"
    "s_mov_b32 s82, s78\n"
    "s_mov_b32 s83, s80\n"
    "s_sub_u32 s84, s86, s82\n"
    "s_mul_i32 s84, s84, 3\n"
    "s_ashr_i32 s84, s84, 2\n"
    "s_min_i32 s84, s84, 0x4000\n"
    "s_add_u32 s84, s84, s83\n"
    "s_add_u32 s80, s66, s46\n"
    "s_add_u32 s81, s80, 4096\n"
    "s_mov_b32 m0, 32768\n"
    "R3_wcb_%=:\n"
    "s_memtime s[76:77]\n"
    "s_getpc_b64 s[78:79]\n"
    "R3_wpb_%=:\n"
    "s_add_u32 s78, s78, R3_web_%=-R3_wpb_%=\n"
    "s_addc_u32 s79, s79, 0\n"
    "s_waitcnt lgkmcnt(0)\n"
    "s_sub_u32 s76, s84, s76\n"
    "s_max_i32 s76, s76, 0\n"
    "s_lshr_b32 s76, s76, 3\n"
    "s_cmp_gt_u32 s76, 40\n"
    "s_cbranch_scc1 R3_wcb_%=\n"
    "s_lshl_b32 s76, s76, 2\n"
    "s_sub_u32 s78, s78, s76\n"
    "s_subb_u32 s79, s79, 0\n"
    "s_setpc_b64 s[78:79]\n"
    ".rept 40\n"
    "s_nop 1\n"
    ".endr\n"
    "R3_web_%=:\n"
    "buffer_load_dwordx4 v0, s[36:39], s80 offen offset:0 sc1 lds\n"
    "buffer_load_dwordx4 v0, s[36:39], s80 offen offset:1024 sc1 lds\n"
    "buffer_load_dwordx4 v0, s[36:39], s80 offen offset:2048 sc1 lds\n"
    "buffer_load_dwordx4 v0, s[36:39], s80 offen offset:3072 sc1 lds\n"
    "s_mov_b32 m0, 36864\n"
    "s_nop 0\n"
    "buffer_load_dwordx4 v0, s[36:39], s81 offen offset:0 sc1 lds\n"
    "buffer_load_dwordx4 v0, s[36:39], s81 offen offset:1024 sc1 lds\n"
    "buffer_load_dwordx4 v0, s[36:39], s81 offen offset:2048 sc1 lds\n"
    "buffer_load_dwordx4 v0, s[36:39], s81 offen offset:3072 sc1 lds\n"
    "s_mov_b32 m0, 40960\n"
    "s_add_u32 s80, s80, 16384\n"
    "buffer_load_dwordx4 v0, s[36:39], s80 offen offset:0 sc1 lds\n"
    "buffer_load_dwordx4 v0, s[36:39], s80 offen offset:1024 sc1 lds\n"
    "s_add_u32 s80, s66, s42\n"
    "s_add_u32 s64, s64, 1\n"
    "s_and_b32 s76, s64, 1\n"
    "s_mul_i32 s76, s76, 0xa0000\n"
    "s_add_u32 s65, s40, s76\n"
    "s_xor_b32 s76, s76, 0xa0000\n"
    "s_add_u32 s71, s40, s76\n"
    "s_and_b32 s76, s64, 3\n"
    "s_lshl_b32 s76, s76, 17\n"
    "s_or_b32 s76, s76, 0x400000\n"
    "s_add_u32 s66, s40, s76\n"
    "s_xor_b32 s76, s76, 0x40000\n"
    "s_add_u32 s68, s40, s76\n"
    "s_add_u32 s67, s66, s60\n"
    "s_mov_b32 s70, 0\n"
    "s_branch R3_wait_%=\n"
    "R3_miss_%=:\n"
    "s_cmp_lg_u32 s70, 0\n"
    "s_cbranch_scc1 R3_missok_%=\n"
    "R3_redo_%=:\n"
    "buffer_load_dwordx4 v[80:83], v0, s[36:39], s80 offen offset:0 sc1\n"
    "buffer_load_dwordx4 v[84:87], v0, s[36:39], s80 offen offset:1024 sc1\n"
    "buffer_load_dwordx4 v[88:91], v0, s[36:39], s80 offen offset:2048 sc1\n"
    "s_add_u32 s88, s88, 1\n"
    "s_mov_b32 s79, 0\n"
    "s_mov_b32 s70, 400\n"
    "s_nop 7\n"
    "s_waitcnt lgkmcnt(0)\n"
    "s_barrier\n"
    "v_add_u32 v28, s43, v0\n"
    "s_cmp_eq_u32 s64, 0\n"
    "s_cbranch_scc1 R3_rdone_%=\n"
    "R3_rl_%=:\n"
    "s_waitcnt vmcnt(0)\n"
    "v_max3_u32 v29, v80, v81, v82\n"
    "v_max3_u32 v29, v29, v83, v84\n"
    "v_max3_u32 v29, v29, v85, v86\n"
    "v_max3_u32 v29, v29, v87, v88\n"
    "v_max3_u32 v29, v29, v89, v90\n"
    "v_max_u32 v29, v29, v91\n"
    "v_cmp_eq_u32 vcc, -1, v29\n"
    "s_cbranch_vccz R3_rw_%=\n"
    "buffer_load_dwordx4 v[80:83], v0, s[36:39], s80 offen offset:0 sc1\n"
    "buffer_load_dwordx4 v[84:87], v0, s[36:39], s80 offen offset:1024 sc1\n"
    "buffer_load_dwordx4 v[88:91], v0, s[36:39], s80 offen offset:2048 sc1\n"
    "s_cmp_lg_u32 s69, 0\n"
    "s_cbranch_scc1 R3_rdone_%=\n"
    "s_add_u32 s79, s79, 1\n"
    "s_cmp_lt_u32 s79, 0x40000\n"
    "s_cbranch_scc1 R3_rl_%=\n"
    "s_mov_b32 s69, 1\n"
    "s_branch R3_rdone_%=\n"
    "R3_rw_%=:\n"
    "ds_write_b128 v28, v[80:83] offset:0\n"
    "ds_write_b128 v28, v[84:87] offset:1024\n"
    "ds_write_b128 v28, v[88:91] offset:2048\n"
    "R3_rdone_%=:\n"
    "s_waitcnt vmcnt(0) lgkmcnt(0)\n"
    "v_mov_b64 v[20:21], 0\n"
    "v_mov_b64 v[22:23], 0\n"
    "v_mov_b64 v[24:25], 0\n"
    "v_mov_b64 v[26:27], 0\n"
    "s_branch R3_wait_%=\n"
    "L_exit_%=:\n"
    "s_waitcnt vmcnt(0) lgkmcnt(0)\n"
    "s_endpgm\n"
                :: "{v[0:15]}"(C), "{v[16:19]}"(SEL), "{s[36:51]}"(SA), "{s[52:67]}"(SB), "{s[68:83]}"(SC), "{s[84:87]}"(rm) : CLOB_V);
            __builtin_unreachable();
        }
        {
            ROLE_BEGIN
            const int l = lane_id(), c = l & 15, g = l >> 4, hb = (c >> 1) & 1;
            const int h = xi * 8 + (slot >> 1), hf = slot & 1, r = 2 * h + hf;
            v16u C, SA, SB = {}, SC = {}; v8u P;
            const unsigned stg = L_W + w * L_WS;
            const rsrc_t rm = mkrsrc(p.mem0, 8 * D * 2);
            const int u0 = 40 * hf + 10 * w;
            C[0] = l * 16; C[1] = l * 16; C[2] = l * 16 + stg;
            C[3] = L_KX + g * 576 + (32 * (w >> 1) + c) * 8 + (w & 1) * 4;
            C[4] = L_QX + (4 * g + (c >> 2)) * 16 + 8 * (c & 1);
            C[5] = spread((unsigned)((u0 + 8 + hb) * 4096 + h * 128 + g * 32 + (c & 1) * 16 + (c >> 2) * 4));
            C[6] = 8192 + w * 2048 + l * 16;
            C[7] = hb ? 0xEEEEu : 0x4444u;
            C[8] = spread((unsigned)((u0 + (c >> 1)) * 4096 + h * 128 + g * 32 + (c & 1) * 16));
            C[9] = (32 * w + c) * 16 + 4 * g;
            C[10] = L_MEM + l * 128 + (l & 31) * 4;
            C[11] = L_QX + (32 * w + c) * 16 + 4 * g;
            C[12] = L_MEM + g * 256 + hb * 128 + (2 * (c >> 2) + (c & 1)) * 16;
            C[13] = L_KX + (c & 3) * 576 + (4 * g + (c >> 2)) * 8;
            C[14] = L_OX + w * 512 + (4 * ((c & 7) >> 1) + (c & 1)) * 16 + g * 4;
            C[15] = l == 0 ? 16384 + 2048 + r * 16 : OOB;
            { v16u T; strip_consts(T, l, stg, 8192, false); P[0] = T[1]; P[3] = T[2]; P[4] = T[3]; }
            P[1] = (l >> 2) * 1280 + (l & 3) * 16;
            P[2] = (l >> 1) * 1280 + (l & 1) * 16;
            P[5] = stg + (8 * g + (c >> 2)) * 64 + ((c >> 1) & 1) * 4096 + (c & 1) * 32;
            P[6] = stg + (8 * g + (c >> 2)) * 32 + (c & 3) * 8;
            P[7] = ((l >> 2) & 7) * 1280 + (l >> 5) * 4 + (l & 3) * 8;
            const int piece = w == 0 ? 0 : (w == 1 ? 8 : (w == 2 ? 2 : 5));
            put_srd(SA, 0, p.ws, 0, WS_BYTES);
            SA[4] = comm_off(p.ws); SA[5] = 0;
            SA[6] = spread((unsigned)(xi * 16384 + piece * 1024)); SA[7] = L_MEM + piece * 1024;
            SA[8] = WS_KF + h * 8192; SA[9] = WS_VF + h * 8192; SA[10] = xi * 32768; SA[11] = 0;
            SA[12] = xi < 3 ? NHC : 99; SA[13] = 0; SA[14] = w; SA[15] = stg;
            SB[0] = FL_KV + h * 512;
            SB[3] = 2830; SB[5] = p.c1; SB[6] = p.k1 * 4 / 3; SB[7] = p.k2 * 4 / 3; SB[8] = 0x80000 + spread((unsigned)(h * 2048)); SB[9] = WS_DBG + (r * 4 + w) * 8192; SB[10] = 16384 + 2048 + r * 16;
            put_srd(SC, 0, p.Wq, (h * 128 + 32 * w) * 2, D * HD * 2); put_srd(SC, 4, p.Wk, (h * 128 + 32 * w) * 2, D * HD * 2);
            put_srd(SC, 12, p.Wo, (h * 128 * D + 320 * hf + 80 * w) * 2, D * HD * 2);
            uni(SA); uni(SB); uni(SC);
            asm volatile(
    "s_mul_i32 s88, s50, 2560\n"
    "s_mul_i32 s89, s50, 320\n"
    "s_add_u32 m0, s88, 32768\n"
    "s_add_u32 s90, s89, 0\n"
    "buffer_load_dword v23, s[84:87], s90 offen lds\n"
    "s_add_u32 m0, s88, 33024\n"
    "s_add_u32 s90, s89, 32\n"
    "buffer_load_dword v23, s[84:87], s90 offen lds\n"
    "s_add_u32 m0, s88, 33280\n"
    "s_add_u32 s90, s89, 64\n"
    "buffer_load_dword v23, s[84:87], s90 offen lds\n"
    "s_add_u32 m0, s88, 33536\n"
    "s_add_u32 s90, s89, 96\n"
    "buffer_load_dword v23, s[84:87], s90 offen lds\n"
    "s_add_u32 m0, s88, 33792\n"
    "s_add_u32 s90, s89, 128\n"
    "buffer_load_dword v23, s[84:87], s90 offen lds\n"
    "s_add_u32 m0, s88, 34048\n"
    "s_add_u32 s90, s89, 160\n"
    "buffer_load_dword v23, s[84:87], s90 offen lds\n"
    "s_add_u32 m0, s88, 34304\n"
    "s_add_u32 s90, s89, 192\n"
    "buffer_load_dword v23, s[84:87], s90 offen lds\n"
    "s_add_u32 m0, s88, 34560\n"
    "s_add_u32 s90, s89, 224\n"
    "buffer_load_dword v23, s[84:87], s90 offen lds\n"
    "s_add_u32 m0, s88, 34816\n"
    "s_add_u32 s90, s89, 256\n"
    "buffer_load_dword v23, s[84:87], s90 offen lds\n"
    "s_add_u32 m0, s88, 35072\n"
    "s_add_u32 s90, s89, 288\n"
    "buffer_load_dword v23, s[84:87], s90 offen lds\n"
    "s_add_u32 m0, s51, 0\n"
    "s_mov_b32 s88, 0\n"
    "buffer_load_dwordx4 v16, s[72:75], s88 offen lds\n"
    "s_add_u32 m0, s51, 1024\n"
    "s_mov_b32 s88, 131072\n"
    "buffer_load_dwordx4 v16, s[72:75], s88 offen lds\n"
    "s_add_u32 m0, s51, 2048\n"
    "s_mov_b32 s88, 262144\n"
    "buffer_load_dwordx4 v16, s[72:75], s88 offen lds\n"
    "s_add_u32 m0, s51, 3072\n"
    "s_mov_b32 s88, 393216\n"
    "buffer_load_dwordx4 v16, s[72:75], s88 offen lds\n"
    "s_add_u32 m0, s51, 4096\n"
    "s_mov_b32 s88, 524288\n"
    "buffer_load_dwordx4 v16, s[72:75], s88 offen lds\n"
    "s_add_u32 m0, s51, 5120\n"
    "s_mov_b32 s88, 655360\n"
    "buffer_load_dwordx4 v16, s[72:75], s88 offen lds\n"
    "s_add_u32 m0, s51, 6144\n"
    "s_mov_b32 s88, 786432\n"
    "buffer_load_dwordx4 v16, s[72:75], s88 offen lds\n"
    "s_add_u32 m0, s51, 7168\n"
    "s_mov_b32 s88, 917504\n"
    "buffer_load_dwordx4 v16, s[72:75], s88 offen lds\n"
    "s_add_u32 m0, s51, 8192\n"
    "s_mov_b32 s88, 1048576\n"
    "buffer_load_dwordx4 v16, s[72:75], s88 offen lds\n"
    "s_add_u32 m0, s51, 9216\n"
    "s_mov_b32 s88, 1179648\n"
    "buffer_load_dwordx4 v16, s[72:75], s88 offen lds\n"
    "s_add_u32 m0, s51, 10240\n"
    "s_mov_b32 s88, 1310720\n"
    "buffer_load_dwordx4 v16, s[72:75], s88 offen lds\n"
    "s_add_u32 m0, s51, 11264\n"
    "s_mov_b32 s88, 1441792\n"
    "buffer_load_dwordx4 v16, s[72:75], s88 offen lds\n"
    "s_add_u32 m0, s51, 12288\n"
    "s_mov_b32 s88, 1572864\n"
    "buffer_load_dwordx4 v16, s[72:75], s88 offen lds\n"
    "s_add_u32 m0, s51, 13312\n"
    "s_mov_b32 s88, 1703936\n"
    "buffer_load_dwordx4 v16, s[72:75], s88 offen lds\n"
    "s_add_u32 m0, s51, 14336\n"
    "s_mov_b32 s88, 1835008\n"
    "buffer_load_dwordx4 v16, s[72:75], s88 offen lds\n"
    "s_add_u32 m0, s51, 15360\n"
    "s_mov_b32 s88, 1966080\n"
    "buffer_load_dwordx4 v16, s[72:75], s88 offen lds\n"
    "s_add_u32 m0, s51, 16384\n"
    "s_mov_b32 s88, 2097152\n"
    "buffer_load_dwordx4 v16, s[72:75], s88 offen lds\n"
    "s_add_u32 m0, s51, 17408\n"
    "s_mov_b32 s88, 2228224\n"
    "buffer_load_dwordx4 v16, s[72:75], s88 offen lds\n"
    "s_add_u32 m0, s51, 18432\n"
    "s_mov_b32 s88, 2359296\n"
    "buffer_load_dwordx4 v16, s[72:75], s88 offen lds\n"
    "s_add_u32 m0, s51, 19456\n"
    "s_mov_b32 s88, 2490368\n"
    "buffer_load_dwordx4 v16, s[72:75], s88 offen lds\n"
    "s_add_u32 m0, s51, 20480\n"
    "s_mov_b32 s88, 2621440\n"
    "buffer_load_dwordx4 v16, s[72:75], s88 offen lds\n"
    "s_add_u32 m0, s51, 21504\n"
    "s_mov_b32 s88, 2752512\n"
    "buffer_load_dwordx4 v16, s[72:75], s88 offen lds\n"
    "s_add_u32 m0, s51, 22528\n"
    "s_mov_b32 s88, 2883584\n"
    "buffer_load_dwordx4 v16, s[72:75], s88 offen lds\n"
    "s_add_u32 m0, s51, 23552\n"
    "s_mov_b32 s88, 3014656\n"
    "buffer_load_dwordx4 v16, s[72:75], s88 offen lds\n"
    "s_add_u32 m0, s51, 24576\n"
    "s_mov_b32 s88, 3145728\n"
    "buffer_load_dwordx4 v16, s[72:75], s88 offen lds\n"
    "s_add_u32 m0, s51, 25600\n"
    "s_mov_b32 s88, 3276800\n"
    "buffer_load_dwordx4 v16, s[72:75], s88 offen lds\n"
    "s_add_u32 m0, s51, 26624\n"
    "s_mov_b32 s88, 3407872\n"
    "buffer_load_dwordx4 v16, s[72:75], s88 offen lds\n"
    "s_add_u32 m0, s51, 27648\n"
    "s_mov_b32 s88, 3538944\n"
    "buffer_load_dwordx4 v16, s[72:75], s88 offen lds\n"
    "s_waitcnt vmcnt(24)\n"
    "ds_read_b64_tr_b16 a[128:129], v19 offset:0\n"
    "ds_read_b64_tr_b16 a[130:131], v19 offset:256\n"
    "ds_read_b64_tr_b16 a[132:133], v19 offset:2048\n"
    "ds_read_b64_tr_b16 a[134:135], v19 offset:2304\n"
    "ds_read_b64_tr_b16 a[136:137], v20 offset:0\n"
    "ds_read_b64_tr_b16 a[138:139], v20 offset:256\n"
    "ds_read_b64_tr_b16 a[140:141], v20 offset:2048\n"
    "ds_read_b64_tr_b16 a[142:143], v20 offset:2304\n"
    "s_waitcnt lgkmcnt(0)\n"
    "s_add_u32 m0, s51, 0\n"
    "s_mov_b32 s88, 3670016\n"
    "buffer_load_dwordx4 v16, s[72:75], s88 offen lds\n"
    "s_add_u32 m0, s51, 1024\n"
    "s_mov_b32 s88, 3801088\n"
    "buffer_load_dwordx4 v16, s[72:75], s88 offen lds\n"
    "s_add_u32 m0, s51, 2048\n"
    "s_mov_b32 s88, 3932160\n"
    "buffer_load_dwordx4 v16, s[72:75], s88 offen lds\n"
    "s_add_u32 m0, s51, 3072\n"
    "s_mov_b32 s88, 4063232\n"
    "buffer_load_dwordx4 v16, s[72:75], s88 offen lds\n"
    "s_waitcnt vmcnt(24)\n"
    "ds_read_b64_tr_b16 a[144:145], v19 offset:4096\n"
    "ds_read_b64_tr_b16 a[146:147], v19 offset:4352\n"
    "ds_read_b64_tr_b16 a[148:149], v19 offset:6144\n"
    "ds_read_b64_tr_b16 a[150:151], v19 offset:6400\n"
    "ds_read_b64_tr_b16 a[152:153], v20 offset:4096\n"
    "ds_read_b64_tr_b16 a[154:155], v20 offset:4352\n"
    "ds_read_b64_tr_b16 a[156:157], v20 offset:6144\n"
    "ds_read_b64_tr_b16 a[158:159], v20 offset:6400\n"
    "s_waitcnt lgkmcnt(0)\n"
    "s_add_u32 m0, s51, 4096\n"
    "s_mov_b32 s88, 4194304\n"
    "buffer_load_dwordx4 v16, s[72:75], s88 offen lds\n"
    "s_add_u32 m0, s51, 5120\n"
    "s_mov_b32 s88, 4325376\n"
    "buffer_load_dwordx4 v16, s[72:75], s88 offen lds\n"
    "s_add_u32 m0, s51, 6144\n"
    "s_mov_b32 s88, 4456448\n"
    "buffer_load_dwordx4 v16, s[72:75], s88 offen lds\n"
    "s_add_u32 m0, s51, 7168\n"
    "s_mov_b32 s88, 4587520\n"
    "buffer_load_dwordx4 v16, s[72:75], s88 offen lds\n"
    "s_waitcnt vmcnt(24)\n"
    "ds_read_b64_tr_b16 a[160:161], v19 offset:8192\n"
    "ds_read_b64_tr_b16 a[162:163], v19 offset:8448\n"
    "ds_read_b64_tr_b16 a[164:165], v19 offset:10240\n"
    "ds_read_b64_tr_b16 a[166:167], v19 offset:10496\n"
    "ds_read_b64_tr_b16 a[168:169], v20 offset:8192\n"
    "ds_read_b64_tr_b16 a[170:171], v20 offset:8448\n"
    "ds_read_b64_tr_b16 a[172:173], v20 offset:10240\n"
    "ds_read_b64_tr_b16 a[174:175], v20 offset:10496\n"
    "s_waitcnt lgkmcnt(0)\n"
    "s_add_u32 m0, s51, 8192\n"
    "s_mov_b32 s88, 4718592\n"
    "buffer_load_dwordx4 v16, s[72:75], s88 offen lds\n"
    "s_add_u32 m0, s51, 9216\n"
    "s_mov_b32 s88, 4849664\n"
    "buffer_load_dwordx4 v16, s[72:75], s88 offen lds\n"
    "s_add_u32 m0, s51, 10240\n"
    "s_mov_b32 s88, 4980736\n"
    "buffer_load_dwordx4 v16, s[72:75], s88 offen lds\n"
    "s_add_u32 m0, s51, 11264\n"
    "s_mov_b32 s88, 5111808\n"
    "buffer_load_dwordx4 v16, s[72:75], s88 offen lds\n"
    "s_waitcnt vmcnt(24)\n"
    "ds_read_b64_tr_b16 a[176:177], v19 offset:12288\n"
    "ds_read_b64_tr_b16 a[178:179], v19 offset:12544\n"
    "ds_read_b64_tr_b16 a[180:181], v19 offset:14336\n"
    "ds_read_b64_tr_b16 a[182:183], v19 offset:14592\n"
    "ds_read_b64_tr_b16 a[184:185], v20 offset:12288\n"
    "ds_read_b64_tr_b16 a[186:187], v20 offset:12544\n"
    "ds_read_b64_tr_b16 a[188:189], v20 offset:14336\n"
    "ds_read_b64_tr_b16 a[190:191], v20 offset:14592\n"
    "s_waitcnt lgkmcnt(0)\n"
    "s_add_u32 m0, s51, 12288\n"
    "s_mov_b32 s88, 0\n"
    "buffer_load_dwordx4 v16, s[68:71], s88 offen lds\n"
    "s_add_u32 m0, s51, 13312\n"
    "s_mov_b32 s88, 131072\n"
    "buffer_load_dwordx4 v16, s[68:71], s88 offen lds\n"
    "s_add_u32 m0, s51, 14336\n"
    "s_mov_b32 s88, 262144\n"
    "buffer_load_dwordx4 v16, s[68:71], s88 offen lds\n"
    "s_add_u32 m0, s51, 15360\n"
    "s_mov_b32 s88, 393216\n"
    "buffer_load_dwordx4 v16, s[68:71], s88 offen lds\n"
    "s_waitcnt vmcnt(24)\n"
    "ds_read_b64_tr_b16 a[192:193], v19 offset:16384\n"
    "ds_read_b64_tr_b16 a[194:195], v19 offset:16640\n"
    "ds_read_b64_tr_b16 a[196:197], v19 offset:18432\n"
    "ds_read_b64_tr_b16 a[198:199], v19 offset:18688\n"
    "ds_read_b64_tr_b16 a[200:201], v20 offset:16384\n"
    "ds_read_b64_tr_b16 a[202:203], v20 offset:16640\n"
    "ds_read_b64_tr_b16 a[204:205], v20 offset:18432\n"
    "ds_read_b64_tr_b16 a[206:207], v20 offset:18688\n"
    "s_waitcnt lgkmcnt(0)\n"
    "s_add_u32 m0, s51, 16384\n"
    "s_mov_b32 s88, 524288\n"
    "buffer_load_dwordx4 v16, s[68:71], s88 offen lds\n"
    "s_add_u32 m0, s51, 17408\n"
    "s_mov_b32 s88, 655360\n"
    "buffer_load_dwordx4 v16, s[68:71], s88 offen lds\n"
    "s_add_u32 m0, s51, 18432\n"
    "s_mov_b32 s88, 786432\n"
    "buffer_load_dwordx4 v16, s[68:71], s88 offen lds\n"
    "s_add_u32 m0, s51, 19456\n"
    "s_mov_b32 s88, 917504\n"
    "buffer_load_dwordx4 v16, s[68:71], s88 offen lds\n"
    "s_waitcnt vmcnt(24)\n"
    "ds_read_b64_tr_b16 a[208:209], v19 offset:20480\n"
    "ds_read_b64_tr_b16 a[210:211], v19 offset:20736\n"
    "ds_read_b64_tr_b16 a[212:213], v19 offset:22528\n"
    "ds_read_b64_tr_b16 a[214:215], v19 offset:22784\n"
    "ds_read_b64_tr_b16 a[216:217], v20 offset:20480\n"
    "ds_read_b64_tr_b16 a[218:219], v20 offset:20736\n"
    "ds_read_b64_tr_b16 a[220:221], v20 offset:22528\n"
    "ds_read_b64_tr_b16 a[222:223], v20 offset:22784\n"
    "s_waitcnt lgkmcnt(0)\n"
    "s_add_u32 m0, s51, 20480\n"
    "s_mov_b32 s88, 1048576\n"
    "buffer_load_dwordx4 v16, s[68:71], s88 offen lds\n"
    "s_add_u32 m0, s51, 21504\n"
    "s_mov_b32 s88, 1179648\n"
    "buffer_load_dwordx4 v16, s[68:71], s88 offen lds\n"
    "s_add_u32 m0, s51, 22528\n"
    "s_mov_b32 s88, 1310720\n"
    "buffer_load_dwordx4 v16, s[68:71], s88 offen lds\n"
    "s_add_u32 m0, s51, 23552\n"
    "s_mov_b32 s88, 1441792\n"
    "buffer_load_dwordx4 v16, s[68:71], s88 offen lds\n"
    "s_waitcnt vmcnt(24)\n"
    "ds_read_b64_tr_b16 a[224:225], v19 offset:24576\n"
    "ds_read_b64_tr_b16 a[226:227], v19 offset:24832\n"
    "ds_read_b64_tr_b16 a[228:229], v19 offset:26624\n"
    "ds_read_b64_tr_b16 a[230:231], v19 offset:26880\n"
    "ds_read_b64_tr_b16 a[232:233], v20 offset:24576\n"
    "ds_read_b64_tr_b16 a[234:235], v20 offset:24832\n"
    "ds_read_b64_tr_b16 a[236:237], v20 offset:26624\n"
    "ds_read_b64_tr_b16 a[238:239], v20 offset:26880\n"
    "s_waitcnt lgkmcnt(0)\n"
    "s_add_u32 m0, s51, 24576\n"
    "s_mov_b32 s88, 1572864\n"
    "buffer_load_dwordx4 v16, s[68:71], s88 offen lds\n"
    "s_add_u32 m0, s51, 25600\n"
    "s_mov_b32 s88, 1703936\n"
    "buffer_load_dwordx4 v16, s[68:71], s88 offen lds\n"
    "s_add_u32 m0, s51, 26624\n"
    "s_mov_b32 s88, 1835008\n"
    "buffer_load_dwordx4 v16, s[68:71], s88 offen lds\n"
    "s_add_u32 m0, s51, 27648\n"
    "s_mov_b32 s88, 1966080\n"
    "buffer_load_dwordx4 v16, s[68:71], s88 offen lds\n"
    "s_waitcnt vmcnt(24)\n"
    "ds_read_b64_tr_b16 a[240:241], v19 offset:0\n"
    "ds_read_b64_tr_b16 a[242:243], v19 offset:256\n"
    "ds_read_b64_tr_b16 a[244:245], v19 offset:2048\n"
    "ds_read_b64_tr_b16 a[246:247], v19 offset:2304\n"
    "ds_read_b64_tr_b16 a[248:249], v20 offset:0\n"
    "ds_read_b64_tr_b16 a[250:251], v20 offset:256\n"
    "ds_read_b64_tr_b16 a[252:253], v20 offset:2048\n"
    "ds_read_b64_tr_b16 a[254:255], v20 offset:2304\n"
    "s_waitcnt lgkmcnt(0)\n"
    "s_add_u32 m0, s51, 0\n"
    "s_mov_b32 s88, 2097152\n"
    "buffer_load_dwordx4 v16, s[68:71], s88 offen lds\n"
    "s_add_u32 m0, s51, 1024\n"
    "s_mov_b32 s88, 2228224\n"
    "buffer_load_dwordx4 v16, s[68:71], s88 offen lds\n"
    "s_add_u32 m0, s51, 2048\n"
    "s_mov_b32 s88, 2359296\n"
    "buffer_load_dwordx4 v16, s[68:71], s88 offen lds\n"
    "s_add_u32 m0, s51, 3072\n"
    "s_mov_b32 s88, 2490368\n"
    "buffer_load_dwordx4 v16, s[68:71], s88 offen lds\n"
    "s_waitcnt vmcnt(24)\n"
    "ds_read_b64_tr_b16 v[144:145], v19 offset:4096\n"
    "ds_read_b64_tr_b16 v[146:147], v19 offset:4352\n"
    "ds_read_b64_tr_b16 v[148:149], v19 offset:6144\n"
    "ds_read_b64_tr_b16 v[150:151], v19 offset:6400\n"
    "ds_read_b64_tr_b16 v[152:153], v20 offset:4096\n"
    "ds_read_b64_tr_b16 v[154:155], v20 offset:4352\n"
    "ds_read_b64_tr_b16 v[156:157], v20 offset:6144\n"
    "ds_read_b64_tr_b16 v[158:159], v20 offset:6400\n"
    "s_waitcnt lgkmcnt(0)\n"
    "s_add_u32 m0, s51, 4096\n"
    "s_mov_b32 s88, 2621440\n"
    "buffer_load_dwordx4 v16, s[68:71], s88 offen lds\n"
    "s_add_u32 m0, s51, 5120\n"
    "s_mov_b32 s88, 2752512\n"
    "buffer_load_dwordx4 v16, s[68:71], s88 offen lds\n"
    "s_add_u32 m0, s51, 6144\n"
    "s_mov_b32 s88, 2883584\n"
    "buffer_load_dwordx4 v16, s[68:71], s88 offen lds\n"
    "s_add_u32 m0, s51, 7168\n"
    "s_mov_b32 s88, 3014656\n"
    "buffer_load_dwordx4 v16, s[68:71], s88 offen lds\n"
    "s_waitcnt vmcnt(24)\n"
    "ds_read_b64_tr_b16 v[160:161], v19 offset:8192\n"
    "ds_read_b64_tr_b16 v[162:163], v19 offset:8448\n"
    "ds_read_b64_tr_b16 v[164:165], v19 offset:10240\n"
    "ds_read_b64_tr_b16 v[166:167], v19 offset:10496\n"
    "ds_read_b64_tr_b16 v[168:169], v20 offset:8192\n"
    "ds_read_b64_tr_b16 v[170:171], v20 offset:8448\n"
    "ds_read_b64_tr_b16 v[172:173], v20 offset:10240\n"
    "ds_read_b64_tr_b16 v[174:175], v20 offset:10496\n"
    "s_waitcnt lgkmcnt(0)\n"
    "s_add_u32 m0, s51, 8192\n"
    "s_mov_b32 s88, 3145728\n"
    "buffer_load_dwordx4 v16, s[68:71], s88 offen lds\n"
    "s_add_u32 m0, s51, 9216\n"
    "s_mov_b32 s88, 3276800\n"
    "buffer_load_dwordx4 v16, s[68:71], s88 offen lds\n"
    "s_add_u32 m0, s51, 10240\n"
    "s_mov_b32 s88, 3407872\n"
    "buffer_load_dwordx4 v16, s[68:71], s88 offen lds\n"
    "s_add_u32 m0, s51, 11264\n"
    "s_mov_b32 s88, 3538944\n"
    "buffer_load_dwordx4 v16, s[68:71], s88 offen lds\n"
    "s_waitcnt vmcnt(24)\n"
    "ds_read_b64_tr_b16 v[28:29], v19 offset:12288\n"
    "ds_read_b64_tr_b16 v[30:31], v19 offset:12544\n"
    "ds_read_b64_tr_b16 v[32:33], v19 offset:14336\n"
    "ds_read_b64_tr_b16 v[34:35], v19 offset:14592\n"
    "ds_read_b64_tr_b16 v[36:37], v20 offset:12288\n"
    "ds_read_b64_tr_b16 v[38:39], v20 offset:12544\n"
    "ds_read_b64_tr_b16 v[40:41], v20 offset:14336\n"
    "ds_read_b64_tr_b16 v[42:43], v20 offset:14592\n"
    "s_waitcnt lgkmcnt(0)\n"
    "s_add_u32 m0, s51, 12288\n"
    "s_mov_b32 s88, 3670016\n"
    "buffer_load_dwordx4 v16, s[68:71], s88 offen lds\n"
    "s_add_u32 m0, s51, 13312\n"
    "s_mov_b32 s88, 3801088\n"
    "buffer_load_dwordx4 v16, s[68:71], s88 offen lds\n"
    "s_add_u32 m0, s51, 14336\n"
    "s_mov_b32 s88, 3932160\n"
    "buffer_load_dwordx4 v16, s[68:71], s88 offen lds\n"
    "s_add_u32 m0, s51, 15360\n"
    "s_mov_b32 s88, 4063232\n"
    "buffer_load_dwordx4 v16, s[68:71], s88 offen lds\n"
    "s_waitcnt vmcnt(24)\n"
    "ds_read_b64_tr_b16 v[44:45], v19 offset:16384\n"
    "ds_read_b64_tr_b16 v[46:47], v19 offset:16640\n"
    "ds_read_b64_tr_b16 v[48:49], v19 offset:18432\n"
    "ds_read_b64_tr_b16 v[50:51], v19 offset:18688\n"
    "ds_read_b64_tr_b16 v[52:53], v20 offset:16384\n"
    "ds_read_b64_tr_b16 v[54:55], v20 offset:16640\n"
    "ds_read_b64_tr_b16 v[56:57], v20 offset:18432\n"
    "ds_read_b64_tr_b16 v[58:59], v20 offset:18688\n"
    "s_waitcnt lgkmcnt(0)\n"
    "s_add_u32 m0, s51, 16384\n"
    "s_mov_b32 s88, 4194304\n"
    "buffer_load_dwordx4 v16, s[68:71], s88 offen lds\n"
    "s_add_u32 m0, s51, 17408\n"
    "s_mov_b32 s88, 4325376\n"
    "buffer_load_dwordx4 v16, s[68:71], s88 offen lds\n"
    "s_add_u32 m0, s51, 18432\n"
    "s_mov_b32 s88, 4456448\n"
    "buffer_load_dwordx4 v16, s[68:71], s88 offen lds\n"
    "s_add_u32 m0, s51, 19456\n"
    "s_mov_b32 s88, 4587520\n"
    "buffer_load_dwordx4 v16, s[68:71], s88 offen lds\n"
    "s_waitcnt vmcnt(24)\n"
    "ds_read_b64_tr_b16 a[0:1], v19 offset:20480\n"
    "ds_read_b64_tr_b16 a[2:3], v19 offset:20736\n"
    "ds_read_b64_tr_b16 a[4:5], v19 offset:22528\n"
    "ds_read_b64_tr_b16 a[6:7], v19 offset:22784\n"
    "ds_read_b64_tr_b16 a[8:9], v20 offset:20480\n"
    "ds_read_b64_tr_b16 a[10:11], v20 offset:20736\n"
    "ds_read_b64_tr_b16 a[12:13], v20 offset:22528\n"
    "ds_read_b64_tr_b16 a[14:15], v20 offset:22784\n"
    "s_waitcnt lgkmcnt(0)\n"
    "s_add_u32 m0, s51, 20480\n"
    "s_mov_b32 s88, 4718592\n"
    "buffer_load_dwordx4 v16, s[68:71], s88 offen lds\n"
    "s_add_u32 m0, s51, 21504\n"
    "s_mov_b32 s88, 4849664\n"
    "buffer_load_dwordx4 v16, s[68:71], s88 offen lds\n"
    "s_add_u32 m0, s51, 22528\n"
    "s_mov_b32 s88, 4980736\n"
    "buffer_load_dwordx4 v16, s[68:71], s88 offen lds\n"
    "s_add_u32 m0, s51, 23552\n"
    "s_mov_b32 s88, 5111808\n"
    "buffer_load_dwordx4 v16, s[68:71], s88 offen lds\n"
    "s_waitcnt vmcnt(24)\n"
    "ds_read_b64_tr_b16 a[16:17], v19 offset:24576\n"
    "ds_read_b64_tr_b16 a[18:19], v19 offset:24832\n"
    "ds_read_b64_tr_b16 a[20:21], v19 offset:26624\n"
    "ds_read_b64_tr_b16 a[22:23], v19 offset:26880\n"
    "ds_read_b64_tr_b16 a[24:25], v20 offset:24576\n"
    "ds_read_b64_tr_b16 a[26:27], v20 offset:24832\n"
    "ds_read_b64_tr_b16 a[28:29], v20 offset:26624\n"
    "ds_read_b64_tr_b16 a[30:31], v20 offset:26880\n"
    "s_waitcnt lgkmcnt(0)\n"
    "s_add_u32 m0, s51, 24576\n"
    "s_mov_b32 s88, 128\n"
    "buffer_load_dwordx4 v18, s[80:83], s88 offen lds\n"
    "s_add_u32 m0, s51, 25600\n"
    "s_mov_b32 s88, 41088\n"
    "buffer_load_dwordx4 v18, s[80:83], s88 offen lds\n"
    "s_add_u32 m0, s51, 26624\n"
    "s_mov_b32 s88, 82048\n"
    "buffer_load_dwordx4 v18, s[80:83], s88 offen lds\n"
    "s_add_u32 m0, s51, 27648\n"
    "s_mov_b32 s88, 123008\n"
    "buffer_load_dwordx4 v18, s[80:83], s88 offen lds\n"
    "s_waitcnt vmcnt(24)\n"
    "ds_read_b64_tr_b16 a[32:33], v19 offset:0\n"
    "ds_read_b64_tr_b16 a[34:35], v19 offset:256\n"
    "ds_read_b64_tr_b16 a[36:37], v19 offset:2048\n"
    "ds_read_b64_tr_b16 a[38:39], v19 offset:2304\n"
    "ds_read_b64_tr_b16 a[40:41], v20 offset:0\n"
    "ds_read_b64_tr_b16 a[42:43], v20 offset:256\n"
    "ds_read_b64_tr_b16 a[44:45], v20 offset:2048\n"
    "ds_read_b64_tr_b16 a[46:47], v20 offset:2304\n"
    "s_waitcnt lgkmcnt(0)\n"
    "s_add_u32 m0, s51, 0\n"
    "s_mov_b32 s88, 0\n"
    "buffer_load_dwordx4 v17, s[80:83], s88 offen lds\n"
    "s_add_u32 m0, s51, 1024\n"
    "s_mov_b32 s88, 20480\n"
    "buffer_load_dwordx4 v17, s[80:83], s88 offen lds\n"
    "s_add_u32 m0, s51, 2048\n"
    "s_mov_b32 s88, 40960\n"
    "buffer_load_dwordx4 v17, s[80:83], s88 offen lds\n"
    "s_add_u32 m0, s51, 3072\n"
    "s_mov_b32 s88, 61440\n"
    "buffer_load_dwordx4 v17, s[80:83], s88 offen lds\n"
    "s_waitcnt vmcnt(24)\n"
    "ds_read_b64_tr_b16 a[48:49], v19 offset:4096\n"
    "ds_read_b64_tr_b16 a[50:51], v19 offset:4352\n"
    "ds_read_b64_tr_b16 a[52:53], v19 offset:6144\n"
    "ds_read_b64_tr_b16 a[54:55], v19 offset:6400\n"
    "ds_read_b64_tr_b16 a[56:57], v20 offset:4096\n"
    "ds_read_b64_tr_b16 a[58:59], v20 offset:4352\n"
    "ds_read_b64_tr_b16 a[60:61], v20 offset:6144\n"
    "ds_read_b64_tr_b16 a[62:63], v20 offset:6400\n"
    "s_waitcnt lgkmcnt(0)\n"
    "s_add_u32 m0, s51, 4096\n"
    "s_mov_b32 s88, 64\n"
    "buffer_load_dwordx4 v17, s[80:83], s88 offen lds\n"
    "s_add_u32 m0, s51, 5120\n"
    "s_mov_b32 s88, 20544\n"
    "buffer_load_dwordx4 v17, s[80:83], s88 offen lds\n"
    "s_add_u32 m0, s51, 6144\n"
    "s_mov_b32 s88, 41024\n"
    "buffer_load_dwordx4 v17, s[80:83], s88 offen lds\n"
    "s_add_u32 m0, s51, 7168\n"
    "s_mov_b32 s88, 61504\n"
    "buffer_load_dwordx4 v17, s[80:83], s88 offen lds\n"
    "s_waitcnt vmcnt(24)\n"
    "ds_read_b64_tr_b16 a[64:65], v19 offset:8192\n"
    "ds_read_b64_tr_b16 a[66:67], v19 offset:8448\n"
    "ds_read_b64_tr_b16 a[68:69], v19 offset:10240\n"
    "ds_read_b64_tr_b16 a[70:71], v19 offset:10496\n"
    "ds_read_b64_tr_b16 a[72:73], v20 offset:8192\n"
    "ds_read_b64_tr_b16 a[74:75], v20 offset:8448\n"
    "ds_read_b64_tr_b16 a[76:77], v20 offset:10240\n"
    "ds_read_b64_tr_b16 a[78:79], v20 offset:10496\n"
    "s_waitcnt lgkmcnt(0)\n"
    "s_add_u32 m0, s51, 8192\n"
    "s_mov_b32 s88, 81920\n"
    "buffer_load_dwordx4 v17, s[80:83], s88 offen lds\n"
    "s_add_u32 m0, s51, 9216\n"
    "s_mov_b32 s88, 102400\n"
    "buffer_load_dwordx4 v17, s[80:83], s88 offen lds\n"
    "s_add_u32 m0, s51, 10240\n"
    "s_mov_b32 s88, 122880\n"
    "buffer_load_dwordx4 v17, s[80:83], s88 offen lds\n"
    "s_add_u32 m0, s51, 11264\n"
    "s_mov_b32 s88, 143360\n"
    "buffer_load_dwordx4 v17, s[80:83], s88 offen lds\n"
    "s_waitcnt vmcnt(24)\n"
    "ds_read_b64_tr_b16 a[80:81], v19 offset:12288\n"
    "ds_read_b64_tr_b16 a[82:83], v19 offset:12544\n"
    "ds_read_b64_tr_b16 a[84:85], v19 offset:14336\n"
    "ds_read_b64_tr_b16 a[86:87], v19 offset:14592\n"
    "ds_read_b64_tr_b16 a[88:89], v20 offset:12288\n"
    "ds_read_b64_tr_b16 a[90:91], v20 offset:12544\n"
    "ds_read_b64_tr_b16 a[92:93], v20 offset:14336\n"
    "ds_read_b64_tr_b16 a[94:95], v20 offset:14592\n"
    "s_waitcnt lgkmcnt(0)\n"
    "s_add_u32 m0, s51, 12288\n"
    "s_mov_b32 s88, 81984\n"
    "buffer_load_dwordx4 v17, s[80:83], s88 offen lds\n"
    "s_add_u32 m0, s51, 13312\n"
    "s_mov_b32 s88, 102464\n"
    "buffer_load_dwordx4 v17, s[80:83], s88 offen lds\n"
    "s_add_u32 m0, s51, 14336\n"
    "s_mov_b32 s88, 122944\n"
    "buffer_load_dwordx4 v17, s[80:83], s88 offen lds\n"
    "s_add_u32 m0, s51, 15360\n"
    "s_mov_b32 s88, 143424\n"
    "buffer_load_dwordx4 v17, s[80:83], s88 offen lds\n"
    "s_waitcnt vmcnt(24)\n"
    "ds_read_b64_tr_b16 a[96:97], v19 offset:16384\n"
    "ds_read_b64_tr_b16 a[98:99], v19 offset:16640\n"
    "ds_read_b64_tr_b16 a[100:101], v19 offset:18432\n"
    "ds_read_b64_tr_b16 a[102:103], v19 offset:18688\n"
    "ds_read_b64_tr_b16 a[104:105], v20 offset:16384\n"
    "ds_read_b64_tr_b16 a[106:107], v20 offset:16640\n"
    "ds_read_b64_tr_b16 a[108:109], v20 offset:18432\n"
    "ds_read_b64_tr_b16 a[110:111], v20 offset:18688\n"
    "s_waitcnt vmcnt(20)\n"
    "ds_read_b64_tr_b16 a[112:113], v19 offset:20480\n"
    "ds_read_b64_tr_b16 a[114:115], v19 offset:20736\n"
    "ds_read_b64_tr_b16 a[116:117], v19 offset:22528\n"
    "ds_read_b64_tr_b16 a[118:119], v19 offset:22784\n"
    "ds_read_b64_tr_b16 a[120:121], v20 offset:20480\n"
    "ds_read_b64_tr_b16 a[122:123], v20 offset:20736\n"
    "s_waitcnt lgkmcnt(7)\n"
    "ds_read_b64_tr_b16 a[124:125], v20 offset:22528\n"
    "ds_read_b64_tr_b16 a[126:127], v20 offset:22784\n"
    "s_waitcnt vmcnt(16)\n"
    "ds_read_b64_tr_b16 v[240:241], v22 offset:24576\n"
    "ds_read_b64_tr_b16 v[242:243], v22 offset:24704\n"
    "ds_read_b64_tr_b16 v[244:245], v22 offset:25600\n"
    "ds_read_b64_tr_b16 v[246:247], v22 offset:25728\n"
    "ds_read_b64_tr_b16 v[248:249], v22 offset:26624\n"
    "s_waitcnt lgkmcnt(7)\n"
    "ds_read_b64_tr_b16 v[250:251], v22 offset:26752\n"
    "ds_read_b64_tr_b16 v[252:253], v22 offset:27648\n"
    "ds_read_b64_tr_b16 v[254:255], v22 offset:27776\n"
    "s_waitcnt vmcnt(8)\n"
    "ds_read_b64_tr_b16 v[176:177], v21 offset:0\n"
    "ds_read_b64_tr_b16 v[178:179], v21 offset:256\n"
    "ds_read_b64_tr_b16 v[180:181], v21 offset:2048\n"
    "ds_read_b64_tr_b16 v[182:183], v21 offset:2304\n"
    "s_waitcnt lgkmcnt(7)\n"
    "ds_read_b64_tr_b16 v[192:193], v21 offset:8\n"
    "ds_read_b64_tr_b16 v[194:195], v21 offset:264\n"
    "ds_read_b64_tr_b16 v[196:197], v21 offset:2056\n"
    "ds_read_b64_tr_b16 v[198:199], v21 offset:2312\n"
    "ds_read_b64_tr_b16 v[208:209], v21 offset:16\n"
    "ds_read_b64_tr_b16 v[210:211], v21 offset:272\n"
    "ds_read_b64_tr_b16 v[212:213], v21 offset:2064\n"
    "s_waitcnt lgkmcnt(7)\n"
    "ds_read_b64_tr_b16 v[214:215], v21 offset:2320\n"
    "ds_read_b64_tr_b16 v[224:225], v21 offset:24\n"
    "ds_read_b64_tr_b16 v[226:227], v21 offset:280\n"
    "ds_read_b64_tr_b16 v[228:229], v21 offset:2072\n"
    "ds_read_b64_tr_b16 v[230:231], v21 offset:2328\n"
    "s_waitcnt vmcnt(0)\n"
    "ds_read_b64_tr_b16 v[184:185], v21 offset:8192\n"
    "ds_read_b64_tr_b16 v[186:187], v21 offset:8448\n"
    "s_waitcnt lgkmcnt(7)\n"
    "ds_read_b64_tr_b16 v[188:189], v21 offset:10240\n"
    "ds_read_b64_tr_b16 v[190:191], v21 offset:10496\n"
    "ds_read_b64_tr_b16 v[200:201], v21 offset:8200\n"
    "ds_read_b64_tr_b16 v[202:203], v21 offset:8456\n"
    "ds_read_b64_tr_b16 v[204:205], v21 offset:10248\n"
    "ds_read_b64_tr_b16 v[206:207], v21 offset:10504\n"
    "ds_read_b64_tr_b16 v[216:217], v21 offset:8208\n"
    "s_waitcnt lgkmcnt(7)\n"
    "ds_read_b64_tr_b16 v[218:219], v21 offset:8464\n"
    "ds_read_b64_tr_b16 v[220:221], v21 offset:10256\n"
    "ds_read_b64_tr_b16 v[222:223], v21 offset:10512\n"
    "ds_read_b64_tr_b16 v[232:233], v21 offset:8216\n"
    "ds_read_b64_tr_b16 v[234:235], v21 offset:8472\n"
    "ds_read_b64_tr_b16 v[236:237], v21 offset:10264\n"
    "ds_read_b64_tr_b16 v[238:239], v21 offset:10520\n"
    "s_waitcnt lgkmcnt(0)\n"
    "ds_write_b128 v2, v[28:31] offset:0\n"
    "ds_write_b128 v2, v[32:35] offset:1024\n"
    "ds_write_b128 v2, v[36:39] offset:2048\n"
    "ds_write_b128 v2, v[40:43] offset:3072\n"
    "ds_write_b128 v2, v[44:47] offset:4096\n"
    "ds_write_b128 v2, v[48:51] offset:5120\n"
    "ds_write_b128 v2, v[52:55] offset:6144\n"
    "ds_write_b128 v2, v[56:59] offset:7168\n"
    "s_waitcnt lgkmcnt(0)\n"
    "v_mov_b32 v16, 0x3f803f80\n"
    "v_mov_b32 v17, 0x3f803f80\n"
    "v_mov_b32 v18, 0x3f803f80\n"
    "v_mov_b32 v19, 0x3f803f80\n"
    "v_mov_b32 v20, 0\n"
    "v_mov_b32 v21, 0\n"
    "v_mov_b32 v22, 0\n"
    "v_mov_b32 v23, 0\n"
    "v_mov_b32 v24, 0\n"
    "v_mov_b32 v25, 0\n"
    "s_mov_b32 s72, 0x3e0293ee\n"
    "s_mov_b32 s69, 0\n"
    "s_mov_b32 s79, 0\n"
    "s_mov_b32 exec_hi, -1\n"
    "buffer_load_dwordx2 v[28:29], off, s[36:39], 0 sc1\n"
    "R7_ih_%=:\n"
    "buffer_load_dwordx2 v[30:31], v0, s[36:39], 0 offen offset:2048 sc1\n"
    "s_waitcnt vmcnt(0)\n"
    "v_sub_u32 v30, v30, v28\n"
    "v_sub_u32 v31, v31, v29\n"
    "v_subrev_u32 v30, 0x7f4a7c16, v30\n"
    "v_cmp_ne_u32 vcc, 0x9e3779b9, v31\n"
    "v_cmp_lt_u32 s[94:95], 1, v30\n"
    "s_nop 1\n"
    "s_or_b64 vcc, vcc, s[94:95]\n"
    "s_cbranch_scc0 R7_jh_%=\n"
    "s_cmp_lg_u32 s69, 0\n"
    "s_cbranch_scc1 R7_jh_%=\n"
    "s_sleep 4\n"
    "s_add_u32 s79, s79, 1\n"
    "s_cmp_lt_u32 s79, 0x40000\n"
    "s_cbranch_scc1 R7_ih_%=\n"
    "s_mov_b32 s69, 1\n"
    "R7_jh_%=:\n"
    "s_mov_b64 exec, -1\n"
    "s_mov_b32 s79, 0\n"
    "s_mov_b32 exec_hi, 1\n"
    "buffer_load_dwordx2 v[28:29], off, s[36:39], 0 sc1\n"
    "R7_iq_%=:\n"
    "buffer_load_dwordx2 v[30:31], v0, s[36:39], 0 offen offset:1024 sc1\n"
    "s_waitcnt vmcnt(0)\n"
    "v_sub_u32 v30, v30, v28\n"
    "v_sub_u32 v31, v31, v29\n"
    "v_subrev_u32 v30, 0x7f4a7c16, v30\n"
    "v_cmp_ne_u32 vcc, 0x9e3779b9, v31\n"
    "v_cmp_lt_u32 s[94:95], 1, v30\n"
    "s_nop 1\n"
    "s_or_b64 vcc, vcc, s[94:95]\n"
    "s_cbranch_scc0 R7_jq_%=\n"
    "s_cmp_lg_u32 s69, 0\n"
    "s_cbranch_scc1 R7_jq_%=\n"
    "s_sleep 4\n"
    "s_add_u32 s79, s79, 1\n"
    "s_cmp_lt_u32 s79, 0x40000\n"
    "s_cbranch_scc1 R7_iq_%=\n"
    "s_mov_b32 s69, 1\n"
    "R7_jq_%=:\n"
    "s_mov_b64 exec, -1\n"
    "v_readlane_b32 s76, v30, 32\n"
    "s_nop 1\n"
    "s_and_b32 s76, s76, 1\n"
    "s_lshl_b32 s76, s76, 13\n"
    "s_add_u32 s40, s40, s76\n"
    "s_mov_b32 s64, 0\n"
    "s_mov_b32 s87, 0\n"
    "s_mov_b32 s88, 0\n"
    "s_mov_b32 s89, 0\n"
    "s_and_b32 s76, s64, 1\n"
    "s_mul_i32 s76, s76, 0xa0000\n"
    "s_add_u32 s65, s40, s76\n"
    "s_xor_b32 s76, s76, 0xa0000\n"
    "s_add_u32 s71, s40, s76\n"
    "s_and_b32 s76, s64, 3\n"
    "s_lshl_b32 s76, s76, 17\n"
    "s_or_b32 s76, s76, 0x400000\n"
    "s_add_u32 s66, s40, s76\n"
    "s_xor_b32 s76, s76, 0x40000\n"
    "s_add_u32 s68, s40, s76\n"
    "s_add_u32 s67, s66, s60\n"
    "s_mov_b32 s70, 0\n"
    "s_mov_b32 s80, s66\n"
    "s_cmp_lg_u32 s50, 1\n"
    "s_cbranch_scc1 R8_done_%=\n"
    "s_mov_b32 s76, 0\n"
    "s_add_u32 s77, s76, 1\n"
    "s_mul_i32 s78, s76, 43\n"
    "s_lshr_b32 s78, s78, 7\n"
    "s_mul_i32 s78, s78, 3\n"
    "s_sub_u32 s76, s76, s78\n"
    "s_cmp_le_u32 s77, s48\n"
    "s_cselect_b32 s76, s76, 0\n"
    "v_lshlrev_b32 v142, 3, v0\n"
    "s_mov_b32 s79, 0\n"
    "R8_pk0_%=:\n"
    "s_mov_b64 exec, 7\n"
    "buffer_load_dword v143, v142, s[36:39], s52 offen sc1\n"
    "s_waitcnt vmcnt(0)\n"
    "s_mov_b64 exec, -1\n"
    "v_readlane_b32 s78, v143, s76\n"
    "v_readlane_b32 s47, v143, 0\n"
    "v_readlane_b32 s94, v143, 1\n"
    "v_readlane_b32 s95, v143, 2\n"
    "s_nop 0\n"
    "s_min_u32 s47, s47, s94\n"
    "s_min_u32 s47, s47, s95\n"
    "s_cmp_ge_u32 s78, s77\n"
    "s_cbranch_scc1 R8_qk0_%=\n"
    "s_sleep 2\n"
    "s_cmp_lg_u32 s69, 0\n"
    "s_cbranch_scc1 R8_qk0_%=\n"
    "s_add_u32 s79, s79, 1\n"
    "s_cmp_lt_u32 s79, 0x40000\n"
    "s_cbranch_scc1 R8_pk0_%=\n"
    "s_mov_b32 s69, 1\n"
    "s_branch R8_qk0_%=\n"
    "R8_qk0_%=:\n"
    "s_mov_b32 s76, 0\n"
    "s_add_u32 m0, s76, 0\n"
    "s_add_u32 s77, s44, 0\n"
    "buffer_load_dwordx4 v0, s[36:39], s77 offen offset:0 sc1 lds\n"
    "buffer_load_dwordx4 v0, s[36:39], s77 offen offset:1024 sc1 lds\n"
    "buffer_load_dwordx4 v0, s[36:39], s77 offen offset:2048 sc1 lds\n"
    "buffer_load_dwordx4 v0, s[36:39], s77 offen offset:3072 sc1 lds\n"
    "s_add_u32 m0, s76, 4096\n"
    "s_add_u32 s77, s44, 4096\n"
    "buffer_load_dwordx4 v0, s[36:39], s77 offen offset:0 sc1 lds\n"
    "buffer_load_dwordx4 v0, s[36:39], s77 offen offset:1024 sc1 lds\n"
    "buffer_load_dwordx4 v0, s[36:39], s77 offen offset:2048 sc1 lds\n"
    "buffer_load_dwordx4 v0, s[36:39], s77 offen offset:3072 sc1 lds\n"
    "s_add_u32 m0, s76, 8192\n"
    "s_add_u32 s77, s45, 0\n"
    "buffer_load_dwordx4 v0, s[36:39], s77 offen offset:0 sc1 lds\n"
    "buffer_load_dwordx4 v0, s[36:39], s77 offen offset:1024 sc1 lds\n"
    "buffer_load_dwordx4 v0, s[36:39], s77 offen offset:2048 sc1 lds\n"
    "buffer_load_dwordx4 v0, s[36:39], s77 offen offset:3072 sc1 lds\n"
    "s_add_u32 m0, s76, 12288\n"
    "s_add_u32 s77, s45, 4096\n"
    "buffer_load_dwordx4 v0, s[36:39], s77 offen offset:0 sc1 lds\n"
    "buffer_load_dwordx4 v0, s[36:39], s77 offen offset:1024 sc1 lds\n"
    "buffer_load_dwordx4 v0, s[36:39], s77 offen offset:2048 sc1 lds\n"
    "buffer_load_dwordx4 v0, s[36:39], s77 offen offset:3072 sc1 lds\n"
    "s_add_u32 s44, s44, 0x40000\n"
    "s_add_u32 s45, s45, 0x40000\n"
    "s_mov_b32 s76, 1\n"
    "s_add_u32 s77, s76, 1\n"
    "s_mul_i32 s78, s76, 43\n"
    "s_lshr_b32 s78, s78, 7\n"
    "s_mul_i32 s78, s78, 3\n"
    "s_sub_u32 s76, s76, s78\n"
    "s_cmp_le_u32 s77, s48\n"
    "s_cselect_b32 s76, s76, 0\n"
    "v_lshlrev_b32 v142, 3, v0\n"
    "s_mov_b32 s79, 0\n"
    "R8_pk1_%=:\n"
    "s_mov_b64 exec, 7\n"
    "buffer_load_dword v143, v142, s[36:39], s52 offen sc1\n"
    "s_waitcnt vmcnt(0)\n"
    "s_mov_b64 exec, -1\n"
    "v_readlane_b32 s78, v143, s76\n"
    "v_readlane_b32 s47, v143, 0\n"
    "v_readlane_b32 s94, v143, 1\n"
    "v_readlane_b32 s95, v143, 2\n"
    "s_nop 0\n"
    "s_min_u32 s47, s47, s94\n"
    "s_min_u32 s47, s47, s95\n"
    "s_cmp_ge_u32 s78, s77\n"
    "s_cbranch_scc1 R8_qk1_%=\n"
    "s_sleep 2\n"
    "s_cmp_lg_u32 s69, 0\n"
    "s_cbranch_scc1 R8_qk1_%=\n"
    "s_add_u32 s79, s79, 1\n"
    "s_cmp_lt_u32 s79, 0x40000\n"
    "s_cbranch_scc1 R8_pk1_%=\n"
    "s_mov_b32 s69, 1\n"
    "s_branch R8_qk1_%=\n"
    "R8_qk1_%=:\n"
    "s_mov_b32 s76, 16384\n"
    "s_add_u32 m0, s76, 0\n"
    "s_add_u32 s77, s44, 0\n"
    "buffer_load_dwordx4 v0, s[36:39], s77 offen offset:0 sc1 lds\n"
    "buffer_load_dwordx4 v0, s[36:39], s77 offen offset:1024 sc1 lds\n"
    "buffer_load_dwordx4 v0, s[36:39], s77 offen offset:2048 sc1 lds\n"
    "buffer_load_dwordx4 v0, s[36:39], s77 offen offset:3072 sc1 lds\n"
    "s_add_u32 m0, s76, 4096\n"
    "s_add_u32 s77, s44, 4096\n"
    "buffer_load_dwordx4 v0, s[36:39], s77 offen offset:0 sc1 lds\n"
    "buffer_load_dwordx4 v0, s[36:39], s77 offen offset:1024 sc1 lds\n"
    "buffer_load_dwordx4 v0, s[36:39], s77 offen offset:2048 sc1 lds\n"
    "buffer_load_dwordx4 v0, s[36:39], s77 offen offset:3072 sc1 lds\n"
    "s_add_u32 m0, s76, 8192\n"
    "s_add_u32 s77, s45, 0\n"
    "buffer_load_dwordx4 v0, s[36:39], s77 offen offset:0 sc1 lds\n"
    "buffer_load_dwordx4 v0, s[36:39], s77 offen offset:1024 sc1 lds\n"
    "buffer_load_dwordx4 v0, s[36:39], s77 offen offset:2048 sc1 lds\n"
    "buffer_load_dwordx4 v0, s[36:39], s77 offen offset:3072 sc1 lds\n"
    "s_add_u32 m0, s76, 12288\n"
    "s_add_u32 s77, s45, 4096\n"
    "buffer_load_dwordx4 v0, s[36:39], s77 offen offset:0 sc1 lds\n"
    "buffer_load_dwordx4 v0, s[36:39], s77 offen offset:1024 sc1 lds\n"
    "buffer_load_dwordx4 v0, s[36:39], s77 offen offset:2048 sc1 lds\n"
    "buffer_load_dwordx4 v0, s[36:39], s77 offen offset:3072 sc1 lds\n"
    "s_add_u32 s44, s44, 0x40000\n"
    "s_add_u32 s45, s45, 0x40000\n"
    "s_mov_b32 s76, 2\n"
    "s_add_u32 s77, s76, 1\n"
    "s_mul_i32 s78, s76, 43\n"
    "s_lshr_b32 s78, s78, 7\n"
    "s_mul_i32 s78, s78, 3\n"
    "s_sub_u32 s76, s76, s78\n"
    "s_cmp_le_u32 s77, s48\n"
    "s_cselect_b32 s76, s76, 0\n"
    "v_lshlrev_b32 v142, 3, v0\n"
    "s_mov_b32 s79, 0\n"
    "R8_pk2_%=:\n"
    "s_mov_b64 exec, 7\n"
    "buffer_load_dword v143, v142, s[36:39], s52 offen sc1\n"
    "s_waitcnt vmcnt(0)\n"
    "s_mov_b64 exec, -1\n"
    "v_readlane_b32 s78, v143, s76\n"
    "v_readlane_b32 s47, v143, 0\n"
    "v_readlane_b32 s94, v143, 1\n"
    "v_readlane_b32 s95, v143, 2\n"
    "s_nop 0\n"
    "s_min_u32 s47, s47, s94\n"
    "s_min_u32 s47, s47, s95\n"
    "s_cmp_ge_u32 s78, s77\n"
    "s_cbranch_scc1 R8_qk2_%=\n"
    "s_sleep 2\n"
    "s_cmp_lg_u32 s69, 0\n"
    "s_cbranch_scc1 R8_qk2_%=\n"
    "s_add_u32 s79, s79, 1\n"
    "s_cmp_lt_u32 s79, 0x40000\n"
    "s_cbranch_scc1 R8_pk2_%=\n"
    "s_mov_b32 s69, 1\n"
    "s_branch R8_qk2_%=\n"
    "R8_qk2_%=:\n"
    "R8_done_%=:\n"
    "s_cmp_eq_u32 s50, 1\n"
    "s_cbranch_scc1 R1_entry_%=\n"
    "s_cmp_eq_u32 s50, 2\n"
    "s_cbranch_scc1 R2_entry_%=\n"
    "s_cmp_eq_u32 s50, 3\n"
    "s_cbranch_scc1 R3_entry_%=\n"
    "R0_entry_%=:\n"
    "s_waitcnt vmcnt(0)\n"
    "s_barrier\n"
    "ds_read_b128 v[68:71], v2 offset:0\n"
    "ds_read_b128 v[72:75], v2 offset:1024\n"
    "ds_read_b128 v[76:79], v2 offset:2048\n"
    "ds_read_b128 v[80:83], v2 offset:3072\n"
    "ds_read_b128 v[100:103], v2 offset:4096\n"
    "ds_read_b128 v[104:107], v2 offset:5120\n"
    "ds_read_b128 v[108:111], v2 offset:6144\n"
    "ds_read_b128 v[112:115], v2 offset:7168\n"
    "ds_read_b128 v[84:87], v1 offset:0\n"
    "ds_read_b128 v[88:91], v1 offset:4096\n"
    "ds_read_b128 v[92:95], v1 offset:1024\n"
    "ds_read_b128 v[96:99], v1 offset:5120\n"
    "ds_read_b128 v[116:119], v6 offset:0\n"
    "ds_read_b128 v[120:123], v6 offset:1024\n"
    "v_mov_b64 v[124:125], 0\n"
    "v_mov_b64 v[126:127], 0\n"
    "v_mov_b64 v[128:129], 0\n"
    "v_mov_b64 v[130:131], 0\n"
    "v_mov_b64 v[132:133], 0\n"
    "v_mov_b64 v[134:135], 0\n"
    "v_mov_b64 v[136:137], 0\n"
    "v_mov_b64 v[138:139], 0\n"
    "s_waitcnt lgkmcnt(0)\n"
    "R0_wait_%=:\n"
    "s_barrier\n"
    "ds_read_b128 v[28:31], v12 offset:0\n"
    "ds_read_b128 v[32:35], v12 offset:1024\n"
    "ds_read_b128 v[36:39], v12 offset:2048\n"
    "s_waitcnt lgkmcnt(2)\n"
    "v_smfmac_f32_16x16x64_bf16 v[124:127], v[28:31], v[68:75], v7\n"
    "ds_read2st64_b32 v[26:27], v10 offset0:0 offset1:8\n"
    "v_smfmac_f32_16x16x64_bf16 v[128:131], v[28:31], v[76:83], v7\n"
    "ds_read_b128 v[40:43], v12 offset:3072\n"
    "s_waitcnt lgkmcnt(3)\n"
    "v_smfmac_f32_16x16x64_bf16 v[124:127], v[32:35], v[100:107], v7\n"
    "ds_read_b128 v[44:47], v12 offset:4096\n"
    "v_smfmac_f32_16x16x64_bf16 v[128:131], v[32:35], v[108:115], v7\n"
    "ds_read_b128 v[48:51], v12 offset:5120\n"
    "s_waitcnt lgkmcnt(4)\n"
    "v_smfmac_f32_16x16x64_bf16 v[124:127], v[36:39], a[0:7], v7\n"
    "ds_read_b128 v[52:55], v12 offset:6144\n"
    "v_smfmac_f32_16x16x64_bf16 v[128:131], v[36:39], a[8:15], v7\n"
    "ds_read_b128 v[56:59], v12 offset:7168\n"
    "s_waitcnt lgkmcnt(4)\n"
    "v_smfmac_f32_16x16x64_bf16 v[124:127], v[40:43], a[16:23], v7\n"
    "ds_read_b128 v[60:63], v12 offset:8192\n"
    "v_smfmac_f32_16x16x64_bf16 v[128:131], v[40:43], a[24:31], v7\n"
    "ds_read_b128 v[64:67], v12 offset:9216\n"
    "s_waitcnt lgkmcnt(5)\n"
    "v_smfmac_f32_16x16x64_bf16 v[124:127], v[44:47], a[32:39], v7\n"
    "v_max_u32 v26, v26, v27\n"
    "v_smfmac_f32_16x16x64_bf16 v[128:131], v[44:47], a[40:47], v7\n"
    "v_cmp_eq_u32 vcc, -1, v26\n"
    "s_waitcnt lgkmcnt(4)\n"
    "v_smfmac_f32_16x16x64_bf16 v[124:127], v[48:51], a[48:55], v7\n"
    "v_smfmac_f32_16x16x64_bf16 v[128:131], v[48:51], a[56:63], v7\n"
    "s_cbranch_vccnz R0_miss_%=\n"
    "R0_missok_%=:\n"
    "s_waitcnt lgkmcnt(3)\n"
    "v_smfmac_f32_16x16x64_bf16 v[124:127], v[52:55], a[64:71], v7\n"
    "ds_read_b128 v[100:103], v1 offset:2048\n"
    "v_smfmac_f32_16x16x64_bf16 v[128:131], v[52:55], a[72:79], v7\n"
    "s_waitcnt lgkmcnt(3)\n"
    "v_smfmac_f32_16x16x64_bf16 v[124:127], v[56:59], a[80:87], v7\n"
    "ds_read_b128 v[104:107], v1 offset:6144\n"
    "v_smfmac_f32_16x16x64_bf16 v[128:131], v[56:59], a[88:95], v7\n"
    "s_waitcnt lgkmcnt(3)\n"
    "v_smfmac_f32_16x16x64_bf16 v[124:127], v[60:63], a[96:103], v7\n"
    "ds_read_b128 v[108:111], v1 offset:3072\n"
    "v_smfmac_f32_16x16x64_bf16 v[128:131], v[60:63], a[104:111], v7\n"
    "s_waitcnt lgkmcnt(3)\n"
    "v_smfmac_f32_16x16x64_bf16 v[124:127], v[64:67], a[112:119], v7\n"
    "ds_read_b128 v[112:115], v1 offset:7168\n"
    "v_smfmac_f32_16x16x64_bf16 v[128:131], v[64:67], a[120:127], v7\n"
    "v_smfmac_f32_16x16x64_bf16 v[132:135], v[28:31], a[128:135], v7\n"
    "v_smfmac_f32_16x16x64_bf16 v[136:139], v[28:31], a[136:143], v7\n"
    "v_add_f32 v124, v124, v126\n"
    "v_smfmac_f32_16x16x64_bf16 v[132:135], v[32:35], a[144:151], v7\n"
    "v_add_f32 v125, v125, v127\n"
    "v_smfmac_f32_16x16x64_bf16 v[136:139], v[32:35], a[152:159], v7\n"
    "v_cvt_pk_bf16_f32 v124, v124, v125\n"
    "v_smfmac_f32_16x16x64_bf16 v[132:135], v[36:39], a[160:167], v7\n"
    "v_add_f32 v128, v128, v130\n"
    "v_smfmac_f32_16x16x64_bf16 v[136:139], v[36:39], a[168:175], v7\n"
    "v_add_f32 v129, v129, v131\n"
    "v_smfmac_f32_16x16x64_bf16 v[132:135], v[40:43], a[176:183], v7\n"
    "v_cvt_pk_bf16_f32 v128, v128, v129\n"
    "v_smfmac_f32_16x16x64_bf16 v[136:139], v[40:43], a[184:191], v7\n"
    "ds_write2_b32 v11, v124, v128 offset0:0 offset1:64\n"
    "v_smfmac_f32_16x16x64_bf16 v[132:135], v[44:47], a[192:199], v7\n"
    "v_smfmac_f32_16x16x64_bf16 v[136:139], v[44:47], a[200:207], v7\n"
    "v_smfmac_f32_16x16x64_bf16 v[132:135], v[48:51], a[208:215], v7\n"
    "v_smfmac_f32_16x16x64_bf16 v[136:139], v[48:51], a[216:223], v7\n"
    "v_smfmac_f32_16x16x64_bf16 v[132:135], v[52:55], a[224:231], v7\n"
    "s_waitcnt lgkmcnt(0)\n"
    "s_barrier\n"
    "ds_read_b64_tr_b16 v[68:69], v4 offset:0\n"
    "v_smfmac_f32_16x16x64_bf16 v[136:139], v[52:55], a[232:239], v7\n"
    "buffer_load_dword v22, v9, s[36:39], s67 offen offset:0 sc1\n"
    "v_smfmac_f32_16x16x64_bf16 v[132:135], v[56:59], a[240:247], v7\n"
    "ds_read_b64_tr_b16 v[70:71], v4 offset:256\n"
    "v_smfmac_f32_16x16x64_bf16 v[132:135], v[60:63], v[144:151], v7\n"
    "buffer_load_dword v24, v9, s[36:39], s67 offen offset:256 sc1\n"
    "v_smfmac_f32_16x16x64_bf16 v[132:135], v[64:67], v[160:167], v7\n"
    "ds_read_b64_tr_b16 v[72:73], v4 offset:512\n"
    "v_smfmac_f32_16x16x64_bf16 v[136:139], v[56:59], a[248:255], v7\n"
    "ds_read_b64_tr_b16 v[74:75], v4 offset:768\n"
    "v_smfmac_f32_16x16x64_bf16 v[136:139], v[60:63], v[152:159], v7\n"
    "ds_read_b64_tr_b16 v[76:77], v4 offset:1024\n"
    "v_smfmac_f32_16x16x64_bf16 v[136:139], v[64:67], v[168:175], v7\n"
    "ds_read_b64_tr_b16 v[78:79], v4 offset:1280\n"
    "s_waitcnt lgkmcnt(4)\n"
    "v_mfma_f32_16x16x32_bf16 v[124:127], v[84:87], v[68:71], 0\n"
    "v_add_f32 v132, v132, v134\n"
    "v_add_f32 v133, v133, v135\n"
    "v_mfma_f32_16x16x32_bf16 v[128:131], v[88:91], v[68:71], 0\n"
    "v_cvt_pk_bf16_f32 v132, v132, v133\n"
    "ds_read_b64_tr_b16 v[80:81], v4 offset:1536\n"
    "v_add_f32 v136, v136, v138\n"
    "v_add_f32 v137, v137, v139\n"
    "v_cvt_pk_bf16_f32 v136, v136, v137\n"
    "ds_write2_b32 v3, v132, v136 offset0:0 offset1:32\n"
    "s_waitcnt lgkmcnt(4)\n"
    "v_mfma_f32_16x16x32_bf16 v[124:127], v[92:95], v[72:75], v[124:127]\n"
    "ds_read_b64_tr_b16 v[82:83], v4 offset:1792\n"
    "v_mfma_f32_16x16x32_bf16 v[128:131], v[96:99], v[72:75], v[128:131]\n"
    "v_cmp_u_f32 vcc, v68, v68\n"
    "s_waitcnt lgkmcnt(3)\n"
    "v_mfma_f32_16x16x32_bf16 v[124:127], v[100:103], v[76:79], v[124:127]\n"
    "v_mfma_f32_16x16x32_bf16 v[128:131], v[104:107], v[76:79], v[128:131]\n"
    "s_cbranch_vccnz R0_nan_%=\n"
    "R0_nanok_%=:\n"
    "s_waitcnt lgkmcnt(1)\n"
    "s_waitcnt lgkmcnt(0)\n"
    "v_mfma_f32_16x16x32_bf16 v[124:127], v[108:111], v[80:83], v[124:127]\n"
    "s_barrier\n"
    "ds_read_b64_tr_b16 v[52:53], v13 offset:0\n"
    "ds_read_b64_tr_b16 v[54:55], v13 offset:128\n"
    "v_mfma_f32_16x16x32_bf16 v[128:131], v[112:115], v[80:83], v[128:131]\n"
    "ds_read_b64_tr_b16 v[56:57], v13 offset:256\n"
    "ds_read_b64_tr_b16 v[58:59], v13 offset:384\n"
    "v_mov_b32 v15, v15\n"
    "v_mov_b32 v15, v15\n"
    "v_mov_b32 v15, v15\n"
    "v_mov_b32 v15, v15\n"
    "v_mov_b32 v15, v15\n"
    "v_mov_b32 v15, v15\n"
    "v_mov_b32 v15, v15\n"
    "s_waitcnt lgkmcnt(2)\n"
    "v_mfma_f32_16x16x32_bf16 v[140:143], v[52:55], v[68:71], 0\n"
    "v_mfma_f32_16x16x32_bf16 v[60:63], v[52:55], v[72:75], 0\n"
    "v_max3_f32 v138, v124, v125, v126\n"
    "v_max3_f32 v139, v128, v129, v130\n"
    "s_waitcnt lgkmcnt(0)\n"
    "v_mfma_f32_16x16x32_bf16 v[140:143], v[56:59], v[76:79], v[140:143]\n"
    "v_max3_f32 v138, v138, v127, v131\n"
    "v_max_f32 v138, v138, v139\n"
    "v_mfma_f32_16x16x32_bf16 v[60:63], v[56:59], v[80:83], v[60:63]\n"
    "v_mov_b32 v15, v15\n"
    "v_mov_b32 v15, v15\n"
    "v_mov_b32 v15, v15\n"
    "v_mov_b32 v15, v15\n"
    "v_mov_b32 v15, v15\n"
    "v_mov_b32 v15, v15\n"
    "v_mov_b32 v15, v15\n"
    "v_mov_b32 v15, v15\n"
    "v_add_f32 v140, v140, v62\n"
    "v_add_f32 v141, v141, v63\n"
    "v_max3_f32 v134, v138, v140, v141\n"
    "v_max3_f32 v138, v138, v140, v141\n"
    "s_nop 1\n"
    "v_permlane16_swap_b32 v138, v134\n"
    "v_max_f32 v139, v138, v134\n"
    "v_max_f32 v138, v138, v134\n"
    "s_nop 1\n"
    "v_permlane32_swap_b32 v138, v139\n"
    "v_max_f32 v138, v138, v139\n"
    "v_mul_f32 v138, 0xbe0293ee, v138\n"
    "v_pk_fma_f32 v[124:125], v[124:125], s[72:73], v[138:139] op_sel_hi:[1,0,0]\n"
    "v_pk_fma_f32 v[126:127], v[126:127], s[72:73], v[138:139] op_sel_hi:[1,0,0]\n"
    "v_pk_fma_f32 v[128:129], v[128:129], s[72:73], v[138:139] op_sel_hi:[1,0,0]\n"
    "v_pk_fma_f32 v[130:131], v[130:131], s[72:73], v[138:139] op_sel_hi:[1,0,0]\n"
    "v_pk_fma_f32 v[140:141], v[140:141], s[72:73], v[138:139] op_sel_hi:[1,0,0]\n"
    "v_exp_f32 v124, v124\n"
    "v_exp_f32 v125, v125\n"
    "v_exp_f32 v126, v126\n"
    "v_exp_f32 v127, v127\n"
    "v_exp_f32 v128, v128\n"
    "v_exp_f32 v129, v129\n"
    "v_exp_f32 v130, v130\n"
    "v_exp_f32 v131, v131\n"
    "v_cvt_pk_bf16_f32 v112, v124, v125\n"
    "v_cvt_pk_bf16_f32 v113, v126, v127\n"
    "v_cvt_pk_bf16_f32 v114, v128, v129\n"
    "v_cvt_pk_bf16_f32 v115, v130, v131\n"
    "v_exp_f32 v140, v140\n"
    "v_mfma_f32_16x16x32_bf16 v[108:111], v[16:19], v[112:115], 0\n"
    "v_exp_f32 v141, v141\n"
    "v_mfma_f32_16x16x32_bf16 v[100:103], v[116:119], v[112:115], 0\n"
    "v_cvt_pk_bf16_f32 v20, v140, v141\n"
    "s_waitcnt vmcnt(0)\n"
    "v_max_u32 v26, v22, v24\n"
    "v_mfma_f32_16x16x32_bf16 v[104:107], v[120:123], v[112:115], 0\n"
    "v_cmp_eq_u32 vcc, -1, v26\n"
    "v_mov_b32 v15, v15\n"
    "v_mfma_f32_16x16x16_bf16 v[108:111], v[16:17], v[20:21], v[108:111]\n"
    "v_mov_b32 v15, v15\n"
    "s_cbranch_vccnz R0_vslow_%=\n"
    "R0_vok_%=:\n"
    "v_mfma_f32_16x16x16_bf16 v[100:103], v[22:23], v[20:21], v[100:103]\n"
    "v_mfma_f32_16x16x16_bf16 v[104:107], v[24:25], v[20:21], v[104:107]\n"
    "s_nop 1\n"
    "v_rcp_f32 v108, v108\n"
    "s_nop 1\n"
    "v_mul_f32 v100, v100, v108\n"
    "v_mul_f32 v101, v101, v108\n"
    "v_mul_f32 v102, v102, v108\n"
    "v_mul_f32 v103, v103, v108\n"
    "v_cvt_pk_bf16_f32 v100, v100, v101\n"
    "v_cvt_pk_bf16_f32 v101, v102, v103\n"
    "v_pk_mul_f32 v[104:105], v[104:105], v[108:109] op_sel_hi:[1,0]\n"
    "v_pk_mul_f32 v[106:107], v[106:107], v[108:109] op_sel_hi:[1,0]\n"
    "ds_write2_b32 v14, v100, v101 offset0:0 offset1:8\n"
    "v_cvt_pk_bf16_f32 v104, v104, v105\n"
    "v_cvt_pk_bf16_f32 v105, v106, v107\n"
    "ds_write2_b32 v14, v104, v105 offset0:64 offset1:72\n"
    "v_mov_b64 v[44:45], 0\n"
    "v_mov_b64 v[46:47], 0\n"
    "v_mov_b32 v15, v15\n"
    "v_mov_b32 v15, v15\n"
    "v_mov_b32 v15, v15\n"
    "v_mov_b32 v15, v15\n"
    "v_mov_b32 v15, v15\n"
    "v_mov_b32 v15, v15\n"
    "v_mfma_f32_32x32x16_bf16 v[28:43], v[44:47], v[44:47], 0\n"
    "s_waitcnt lgkmcnt(0)\n"
    "s_barrier\n"
    "ds_read_b128 v[68:71], v0 offset:53248\n"
    "ds_read_b128 v[72:75], v0 offset:54272\n"
    "v_mfma_f32_32x32x16_bf16 v[124:139], v[44:47], v[44:47], 0\n"
    "s_waitcnt lgkmcnt(1)\n"
    "v_smfmac_f32_16x16x64_bf16 v[44:47], v[68:71], v[240:247], v7\n"
    "v_smfmac_f32_16x16x64_bf16 v[28:31], v[68:71], v[176:183], v7\n"
    "s_waitcnt lgkmcnt(0)\n"
    "v_smfmac_f32_16x16x64_bf16 v[44:47], v[72:75], v[248:255], v7\n"
    "v_smfmac_f32_16x16x64_bf16 v[28:31], v[72:75], v[184:191], v7\n"
    "v_smfmac_f32_16x16x64_bf16 v[32:35], v[68:71], v[192:199], v7\n"
    "v_smfmac_f32_16x16x64_bf16 v[32:35], v[72:75], v[200:207], v7\n"
    "v_add_f32 v44, v44, v46\n"
    "v_smfmac_f32_16x16x64_bf16 v[36:39], v[68:71], v[208:215], v7\n"
    "v_add_f32 v45, v45, v47\n"
    "v_smfmac_f32_16x16x64_bf16 v[36:39], v[72:75], v[216:223], v7\n"
    "v_cvt_pk_bf16_f32 v44, v44, v45\n"
    "v_smfmac_f32_16x16x64_bf16 v[40:43], v[68:71], v[224:231], v7\n"
    "v_add_f32 v28, v28, v30\n"
    "v_smfmac_f32_16x16x64_bf16 v[40:43], v[72:75], v[232:239], v7\n"
    "buffer_store_dword v44, v5, s[36:39], s65 offen sc1\n"
    "v_add_f32 v29, v29, v31\n"
    "v_cvt_pk_bf16_f32 v28, v28, v29\n"
    "v_add_f32 v32, v32, v34\n"
    "v_add_f32 v33, v33, v35\n"
    "v_cvt_pk_bf16_f32 v29, v32, v33\n"
    "v_add_f32 v36, v36, v38\n"
    "v_add_f32 v37, v37, v39\n"
    "v_cvt_pk_bf16_f32 v30, v36, v37\n"
    "v_add_f32 v40, v40, v42\n"
    "v_add_f32 v41, v41, v43\n"
    "v_cvt_pk_bf16_f32 v31, v40, v41\n"
    "buffer_store_dwordx4 v[28:31], v8, s[36:39], s65 offen sc1\n"
    "s_cmp_eq_u32 s64, 62\n"
    "s_cbranch_scc1 L_exit_%=\n"
    "v_xor_b32 v1, 0x4000, v1\n"
    "v_xor_b32 v6, 0x4000, v6\n"
    "ds_read_b128 v[68:71], v2 offset:0\n"
    "ds_read_b128 v[72:75], v2 offset:1024\n"
    "ds_read_b128 v[76:79], v2 offset:2048\n"
    "ds_read_b128 v[80:83], v2 offset:3072\n"
    "ds_read_b128 v[100:103], v2 offset:4096\n"
    "ds_read_b128 v[104:107], v2 offset:5120\n"
    "ds_read_b128 v[108:111], v2 offset:6144\n"
    "ds_read_b128 v[112:115], v2 offset:7168\n"
    "ds_read_b128 v[84:87], v1 offset:0\n"
    "ds_read_b128 v[88:91], v1 offset:4096\n"
    "ds_read_b128 v[92:95], v1 offset:1024\n"
    "ds_read_b128 v[96:99], v1 offset:5120\n"
    "ds_read_b128 v[116:119], v6 offset:0\n"
    "ds_read_b128 v[120:123], v6 offset:1024\n"
    "v_mov_b64 v[124:125], 0\n"
    "v_mov_b64 v[126:127], 0\n"
    "v_mov_b64 v[128:129], 0\n"
    "v_mov_b64 v[130:131], 0\n"
    "v_mov_b64 v[132:133], 0\n"
    "v_mov_b64 v[134:135], 0\n"
    "v_mov_b64 v[136:137], 0\n"
    "v_mov_b64 v[138:139], 0\n"
    "s_waitcnt lgkmcnt(0)\n"
    "s_add_u32 s80, s66, s42\n"
    "s_add_u32 s64, s64, 1\n"
    "s_and_b32 s76, s64, 1\n"
    "s_mul_i32 s76, s76, 0xa0000\n"
    "s_add_u32 s65, s40, s76\n"
    "s_xor_b32 s76, s76, 0xa0000\n"
    "s_add_u32 s71, s40, s76\n"
    "s_and_b32 s76, s64, 3\n"
    "s_lshl_b32 s76, s76, 17\n"
    "s_or_b32 s76, s76, 0x400000\n"
    "s_add_u32 s66, s40, s76\n"
    "s_xor_b32 s76, s76, 0x40000\n"
    "s_add_u32 s68, s40, s76\n"
    "s_add_u32 s67, s66, s60\n"
    "s_mov_b32 s70, 0\n"
    "s_branch R0_wait_%=\n"
    "R0_nan_%=:\n"
    "s_cmp_lg_u32 s70, 0\n"
    "s_cbranch_scc1 R0_nanok_%=\n"
    "s_branch R0_redo_%=\n"
    "R0_miss_%=:\n"
    "s_cmp_lg_u32 s70, 0\n"
    "s_cbranch_scc1 R0_missok_%=\n"
    "R0_redo_%=:\n"
    "buffer_load_dwordx4 v[84:87], v0, s[36:39], s80 offen offset:0 sc1\n"
    "buffer_load_dwordx4 v[88:91], v0, s[36:39], s80 offen offset:1024 sc1\n"
    "s_add_u32 s88, s88, 1\n"
    "s_mov_b32 s79, 0\n"
    "s_mov_b32 s70, 400\n"
    "s_nop 7\n"
    "s_waitcnt lgkmcnt(0)\n"
    "s_barrier\n"
    "v_add_u32 v28, s43, v0\n"
    "s_cmp_eq_u32 s64, 0\n"
    "s_cbranch_scc1 R0_rdone_%=\n"
    "R0_rl_%=:\n"
    "s_waitcnt vmcnt(0)\n"
    "v_max3_u32 v29, v84, v85, v86\n"
    "v_max3_u32 v29, v29, v87, v88\n"
    "v_max3_u32 v29, v29, v89, v90\n"
    "v_max_u32 v29, v29, v91\n"
    "v_cmp_eq_u32 vcc, -1, v29\n"
    "s_cbranch_vccz R0_rw_%=\n"
    "buffer_load_dwordx4 v[84:87], v0, s[36:39], s80 offen offset:0 sc1\n"
    "buffer_load_dwordx4 v[88:91], v0, s[36:39], s80 offen offset:1024 sc1\n"
    "s_cmp_lg_u32 s69, 0\n"
    "s_cbranch_scc1 R0_rdone_%=\n"
    "s_add_u32 s79, s79, 1\n"
    "s_cmp_lt_u32 s79, 0x40000\n"
    "s_cbranch_scc1 R0_rl_%=\n"
    "s_mov_b32 s69, 1\n"
    "s_branch R0_rdone_%=\n"
    "R0_rw_%=:\n"
    "ds_write_b128 v28, v[84:87] offset:0\n"
    "ds_write_b128 v28, v[88:91] offset:1024\n"
    "R0_rdone_%=:\n"
    "s_waitcnt vmcnt(0) lgkmcnt(0)\n"
    "ds_read_b128 v[68:71], v2 offset:0\n"
    "ds_read_b128 v[72:75], v2 offset:1024\n"
    "ds_read_b128 v[76:79], v2 offset:2048\n"
    "ds_read_b128 v[80:83], v2 offset:3072\n"
    "ds_read_b128 v[100:103], v2 offset:4096\n"
    "ds_read_b128 v[104:107], v2 offset:5120\n"
    "ds_read_b128 v[108:111], v2 offset:6144\n"
    "ds_read_b128 v[112:115], v2 offset:7168\n"
    "ds_read_b128 v[84:87], v1 offset:0\n"
    "ds_read_b128 v[88:91], v1 offset:4096\n"
    "ds_read_b128 v[92:95], v1 offset:1024\n"
    "ds_read_b128 v[96:99], v1 offset:5120\n"
    "ds_read_b128 v[116:119], v6 offset:0\n"
    "ds_read_b128 v[120:123], v6 offset:1024\n"
    "v_mov_b64 v[124:125], 0\n"
    "v_mov_b64 v[126:127], 0\n"
    "v_mov_b64 v[128:129], 0\n"
    "v_mov_b64 v[130:131], 0\n"
    "v_mov_b64 v[132:133], 0\n"
    "v_mov_b64 v[134:135], 0\n"
    "v_mov_b64 v[136:137], 0\n"
    "v_mov_b64 v[138:139], 0\n"
    "s_waitcnt lgkmcnt(0)\n"
    "s_branch R0_wait_%=\n"
    "R0_vslow_%=:\n"
    "s_mov_b32 s79, 0\n"
    "R0_vl_%=:\n"
    "s_add_u32 s87, s87, 1\n"
    "buffer_load_dword v22, v9, s[36:39], s67 offen offset:0 sc1\n"
    "buffer_load_dword v24, v9, s[36:39], s67 offen offset:256 sc1\n"
    "s_waitcnt vmcnt(0)\n"
    "v_max_u32 v26, v22, v24\n"
    "v_cmp_eq_u32 vcc, -1, v26\n"
    "s_cbranch_vccz R0_vok_%=\n"
    "s_cmp_lg_u32 s69, 0\n"
    "s_cbranch_scc1 R0_vok_%=\n"
    "s_add_u32 s79, s79, 1\n"
    "s_cmp_lt_u32 s79, 0x40000\n"
    "s_cbranch_scc1 R0_vl_%=\n"
    "s_mov_b32 s69, 1\n"
    "s_branch R0_vok_%=\n"
    "R1_entry_%=:\n"
    "s_waitcnt vmcnt(0)\n"
    "s_barrier\n"
    "ds_read_b128 v[68:71], v2 offset:0\n"
    "ds_read_b128 v[72:75], v2 offset:1024\n"
    "ds_read_b128 v[76:79], v2 offset:2048\n"
    "ds_read_b128 v[80:83], v2 offset:3072\n"
    "ds_read_b128 v[100:103], v2 offset:4096\n"
    "ds_read_b128 v[104:107], v2 offset:5120\n"
    "ds_read_b128 v[108:111], v2 offset:6144\n"
    "ds_read_b128 v[112:115], v2 offset:7168\n"
    "ds_read_b128 v[84:87], v1 offset:0\n"
    "ds_read_b128 v[88:91], v1 offset:4096\n"
    "ds_read_b128 v[92:95], v1 offset:1024\n"
    "ds_read_b128 v[96:99], v1 offset:5120\n"
    "ds_read_b128 v[116:119], v6 offset:0\n"
    "ds_read_b128 v[120:123], v6 offset:1024\n"
    "v_mov_b64 v[124:125], 0\n"
    "v_mov_b64 v[126:127], 0\n"
    "v_mov_b64 v[128:129], 0\n"
    "v_mov_b64 v[130:131], 0\n"
    "v_mov_b64 v[132:133], 0\n"
    "v_mov_b64 v[134:135], 0\n"
    "v_mov_b64 v[136:137], 0\n"
    "v_mov_b64 v[138:139], 0\n"
    "s_waitcnt lgkmcnt(0)\n"
    "R1_wait_%=:\n"
    "s_barrier\n"
    "ds_read_b128 v[28:31], v12 offset:0\n"
    "ds_read_b128 v[32:35], v12 offset:1024\n"
    "ds_read_b128 v[36:39], v12 offset:2048\n"
    "s_waitcnt lgkmcnt(2)\n"
    "v_smfmac_f32_16x16x64_bf16 v[124:127], v[28:31], v[68:75], v7\n"
    "ds_read2st64_b32 v[26:27], v10 offset0:0 offset1:8\n"
    "v_smfmac_f32_16x16x64_bf16 v[128:131], v[28:31], v[76:83], v7\n"
    "ds_read_b128 v[40:43], v12 offset:3072\n"
    "s_waitcnt lgkmcnt(3)\n"
    "v_smfmac_f32_16x16x64_bf16 v[124:127], v[32:35], v[100:107], v7\n"
    "ds_read_b128 v[44:47], v12 offset:4096\n"
    "v_smfmac_f32_16x16x64_bf16 v[128:131], v[32:35], v[108:115], v7\n"
    "ds_read_b128 v[48:51], v12 offset:5120\n"
    "s_waitcnt lgkmcnt(4)\n"
    "v_smfmac_f32_16x16x64_bf16 v[124:127], v[36:39], a[0:7], v7\n"
    "ds_read_b128 v[52:55], v12 offset:6144\n"
    "v_smfmac_f32_16x16x64_bf16 v[128:131], v[36:39], a[8:15], v7\n"
    "ds_read_b128 v[56:59], v12 offset:7168\n"
    "s_waitcnt lgkmcnt(4)\n"
    "v_smfmac_f32_16x16x64_bf16 v[124:127], v[40:43], a[16:23], v7\n"
    "ds_read_b128 v[60:63], v12 offset:8192\n"
    "v_smfmac_f32_16x16x64_bf16 v[128:131], v[40:43], a[24:31], v7\n"
    "ds_read_b128 v[64:67], v12 offset:9216\n"
    "s_waitcnt lgkmcnt(5)\n"
    "v_smfmac_f32_16x16x64_bf16 v[124:127], v[44:47], a[32:39], v7\n"
    "v_max_u32 v26, v26, v27\n"
    "v_smfmac_f32_16x16x64_bf16 v[128:131], v[44:47], a[40:47], v7\n"
    "v_cmp_eq_u32 vcc, -1, v26\n"
    "s_waitcnt lgkmcnt(4)\n"
    "v_smfmac_f32_16x16x64_bf16 v[124:127], v[48:51], a[48:55], v7\n"
    "v_smfmac_f32_16x16x64_bf16 v[128:131], v[48:51], a[56:63], v7\n"
    "s_cbranch_vccnz R1_miss_%=\n"
    "R1_missok_%=:\n"
    "s_waitcnt lgkmcnt(3)\n"
    "v_smfmac_f32_16x16x64_bf16 v[124:127], v[52:55], a[64:71], v7\n"
    "ds_read_b128 v[100:103], v1 offset:2048\n"
    "v_smfmac_f32_16x16x64_bf16 v[128:131], v[52:55], a[72:79], v7\n"
    "s_waitcnt lgkmcnt(3)\n"
    "v_smfmac_f32_16x16x64_bf16 v[124:127], v[56:59], a[80:87], v7\n"
    "ds_read_b128 v[104:107], v1 offset:6144\n"
    "v_smfmac_f32_16x16x64_bf16 v[128:131], v[56:59], a[88:95], v7\n"
    "s_waitcnt lgkmcnt(3)\n"
    "v_smfmac_f32_16x16x64_bf16 v[124:127], v[60:63], a[96:103], v7\n"
    "ds_read_b128 v[108:111], v1 offset:3072\n"
    "v_smfmac_f32_16x16x64_bf16 v[128:131], v[60:63], a[104:111], v7\n"
    "s_waitcnt lgkmcnt(3)\n"
    "v_smfmac_f32_16x16x64_bf16 v[124:127], v[64:67], a[112:119], v7\n"
    "ds_read_b128 v[112:115], v1 offset:7168\n"
    "v_smfmac_f32_16x16x64_bf16 v[128:131], v[64:67], a[120:127], v7\n"
    "v_smfmac_f32_16x16x64_bf16 v[132:135], v[28:31], a[128:135], v7\n"
    "v_smfmac_f32_16x16x64_bf16 v[136:139], v[28:31], a[136:143], v7\n"
    "v_add_f32 v124, v124, v126\n"
    "v_smfmac_f32_16x16x64_bf16 v[132:135], v[32:35], a[144:151], v7\n"
    "v_add_f32 v125, v125, v127\n"
    "v_smfmac_f32_16x16x64_bf16 v[136:139], v[32:35], a[152:159], v7\n"
    "v_cvt_pk_bf16_f32 v124, v124, v125\n"
    "v_smfmac_f32_16x16x64_bf16 v[132:135], v[36:39], a[160:167], v7\n"
    "v_add_f32 v128, v128, v130\n"
    "v_smfmac_f32_16x16x64_bf16 v[136:139], v[36:39], a[168:175], v7\n"
    "v_add_f32 v129, v129, v131\n"
    "v_smfmac_f32_16x16x64_bf16 v[132:135], v[40:43], a[176:183], v7\n"
    "v_cvt_pk_bf16_f32 v128, v128, v129\n"
    "v_smfmac_f32_16x16x64_bf16 v[136:139], v[40:43], a[184:191], v7\n"
    "ds_write2_b32 v11, v124, v128 offset0:0 offset1:64\n"
    "v_smfmac_f32_16x16x64_bf16 v[132:135], v[44:47], a[192:199], v7\n"
    "v_smfmac_f32_16x16x64_bf16 v[136:139], v[44:47], a[200:207], v7\n"
    "v_smfmac_f32_16x16x64_bf16 v[132:135], v[48:51], a[208:215], v7\n"
    "v_smfmac_f32_16x16x64_bf16 v[136:139], v[48:51], a[216:223], v7\n"
    "v_smfmac_f32_16x16x64_bf16 v[132:135], v[52:55], a[224:231], v7\n"
    "s_waitcnt lgkmcnt(0)\n"
    "s_barrier\n"
    "ds_read_b64_tr_b16 v[68:69], v4 offset:0\n"
    "v_smfmac_f32_16x16x64_bf16 v[136:139], v[52:55], a[232:239], v7\n"
    "buffer_load_dword v22, v9, s[36:39], s67 offen offset:0 sc1\n"
    "v_smfmac_f32_16x16x64_bf16 v[132:135], v[56:59], a[240:247], v7\n"
    "ds_read_b64_tr_b16 v[70:71], v4 offset:256\n"
    "v_smfmac_f32_16x16x64_bf16 v[132:135], v[60:63], v[144:151], v7\n"
    "buffer_load_dword v24, v9, s[36:39], s67 offen offset:256 sc1\n"
    "v_smfmac_f32_16x16x64_bf16 v[132:135], v[64:67], v[160:167], v7\n"
    "ds_read_b64_tr_b16 v[72:73], v4 offset:512\n"
    "v_smfmac_f32_16x16x64_bf16 v[136:139], v[56:59], a[248:255], v7\n"
    "ds_read_b64_tr_b16 v[74:75], v4 offset:768\n"
    "v_smfmac_f32_16x16x64_bf16 v[136:139], v[60:63], v[152:159], v7\n"
    "ds_read_b64_tr_b16 v[76:77], v4 offset:1024\n"
    "v_smfmac_f32_16x16x64_bf16 v[136:139], v[64:67], v[168:175], v7\n"
    "ds_read_b64_tr_b16 v[78:79], v4 offset:1280\n"
    "s_waitcnt lgkmcnt(4)\n"
    "v_mfma_f32_16x16x32_bf16 v[124:127], v[84:87], v[68:71], 0\n"
    "v_add_f32 v132, v132, v134\n"
    "v_add_f32 v133, v133, v135\n"
    "v_mfma_f32_16x16x32_bf16 v[128:131], v[88:91], v[68:71], 0\n"
    "v_cvt_pk_bf16_f32 v132, v132, v133\n"
    "ds_read_b64_tr_b16 v[80:81], v4 offset:1536\n"
    "v_add_f32 v136, v136, v138\n"
    "v_add_f32 v137, v137, v139\n"
    "v_cvt_pk_bf16_f32 v136, v136, v137\n"
    "ds_write2_b32 v3, v132, v136 offset0:0 offset1:32\n"
    "s_waitcnt lgkmcnt(4)\n"
    "v_mfma_f32_16x16x32_bf16 v[124:127], v[92:95], v[72:75], v[124:127]\n"
    "ds_read_b64_tr_b16 v[82:83], v4 offset:1792\n"
    "v_mfma_f32_16x16x32_bf16 v[128:131], v[96:99], v[72:75], v[128:131]\n"
    "v_cmp_u_f32 vcc, v68, v68\n"
    "s_waitcnt lgkmcnt(3)\n"
    "v_mfma_f32_16x16x32_bf16 v[124:127], v[100:103], v[76:79], v[124:127]\n"
    "v_mfma_f32_16x16x32_bf16 v[128:131], v[104:107], v[76:79], v[128:131]\n"
    "s_cbranch_vccnz R1_nan_%=\n"
    "R1_nanok_%=:\n"
    "s_waitcnt lgkmcnt(1)\n"
    "s_waitcnt lgkmcnt(0)\n"
    "v_mfma_f32_16x16x32_bf16 v[124:127], v[108:111], v[80:83], v[124:127]\n"
    "s_barrier\n"
    "ds_read_b64_tr_b16 v[52:53], v13 offset:0\n"
    "ds_read_b64_tr_b16 v[54:55], v13 offset:128\n"
    "v_mfma_f32_16x16x32_bf16 v[128:131], v[112:115], v[80:83], v[128:131]\n"
    "ds_read_b64_tr_b16 v[56:57], v13 offset:256\n"
    "ds_read_b64_tr_b16 v[58:59], v13 offset:384\n"
    "v_mov_b32 v15, v15\n"
    "v_mov_b32 v15, v15\n"
    "v_mov_b32 v15, v15\n"
    "v_mov_b32 v15, v15\n"
    "v_mov_b32 v15, v15\n"
    "v_mov_b32 v15, v15\n"
    "v_mov_b32 v15, v15\n"
    "s_waitcnt lgkmcnt(2)\n"
    "v_mfma_f32_16x16x32_bf16 v[140:143], v[52:55], v[68:71], 0\n"
    "v_mfma_f32_16x16x32_bf16 v[60:63], v[52:55], v[72:75], 0\n"
    "v_max3_f32 v138, v124, v125, v126\n"
    "v_max3_f32 v139, v128, v129, v130\n"
    "s_waitcnt lgkmcnt(0)\n"
    "v_mfma_f32_16x16x32_bf16 v[140:143], v[56:59], v[76:79], v[140:143]\n"
    "v_max3_f32 v138, v138, v127, v131\n"
    "v_max_f32 v138, v138, v139\n"
    "v_mfma_f32_16x16x32_bf16 v[60:63], v[56:59], v[80:83], v[60:63]\n"
    "v_mov_b32 v15, v15\n"
    "v_mov_b32 v15, v15\n"
    "v_mov_b32 v15, v15\n"
    "v_mov_b32 v15, v15\n"
    "v_mov_b32 v15, v15\n"
    "v_mov_b32 v15, v15\n"
    "v_mov_b32 v15, v15\n"
    "v_mov_b32 v15, v15\n"
    "v_add_f32 v140, v140, v62\n"
    "v_add_f32 v141, v141, v63\n"
    "v_max3_f32 v134, v138, v140, v141\n"
    "v_max3_f32 v138, v138, v140, v141\n"
    "s_nop 1\n"
    "v_permlane16_swap_b32 v138, v134\n"
    "v_max_f32 v139, v138, v134\n"
    "v_max_f32 v138, v138, v134\n"
    "s_nop 1\n"
    "v_permlane32_swap_b32 v138, v139\n"
    "v_max_f32 v138, v138, v139\n"
    "v_mul_f32 v138, 0xbe0293ee, v138\n"
    "v_pk_fma_f32 v[124:125], v[124:125], s[72:73], v[138:139] op_sel_hi:[1,0,0]\n"
    "v_pk_fma_f32 v[126:127], v[126:127], s[72:73], v[138:139] op_sel_hi:[1,0,0]\n"
    "v_pk_fma_f32 v[128:129], v[128:129], s[72:73], v[138:139] op_sel_hi:[1,0,0]\n"
    "v_pk_fma_f32 v[130:131], v[130:131], s[72:73], v[138:139] op_sel_hi:[1,0,0]\n"
    "v_pk_fma_f32 v[140:141], v[140:141], s[72:73], v[138:139] op_sel_hi:[1,0,0]\n"
    "v_exp_f32 v124, v124\n"
    "v_exp_f32 v125, v125\n"
    "v_exp_f32 v126, v126\n"
    "v_exp_f32 v127, v127\n"
    "v_exp_f32 v128, v128\n"
    "v_exp_f32 v129, v129\n"
    "v_exp_f32 v130, v130\n"
    "v_exp_f32 v131, v131\n"
    "v_cvt_pk_bf16_f32 v112, v124, v125\n"
    "v_cvt_pk_bf16_f32 v113, v126, v127\n"
    "v_cvt_pk_bf16_f32 v114, v128, v129\n"
    "v_cvt_pk_bf16_f32 v115, v130, v131\n"
    "v_exp_f32 v140, v140\n"
    "v_mfma_f32_16x16x32_bf16 v[108:111], v[16:19], v[112:115], 0\n"
    "v_exp_f32 v141, v141\n"
    "v_mfma_f32_16x16x32_bf16 v[100:103], v[116:119], v[112:115], 0\n"
    "v_cvt_pk_bf16_f32 v20, v140, v141\n"
    "s_waitcnt vmcnt(0)\n"
    "v_max_u32 v26, v22, v24\n"
    "v_mfma_f32_16x16x32_bf16 v[104:107], v[120:123], v[112:115], 0\n"
    "v_cmp_eq_u32 vcc, -1, v26\n"
    "v_mov_b32 v15, v15\n"
    "v_mfma_f32_16x16x16_bf16 v[108:111], v[16:17], v[20:21], v[108:111]\n"
    "v_mov_b32 v15, v15\n"
    "s_cbranch_vccnz R1_vslow_%=\n"
    "R1_vok_%=:\n"
    "v_mfma_f32_16x16x16_bf16 v[100:103], v[22:23], v[20:21], v[100:103]\n"
    "v_mfma_f32_16x16x16_bf16 v[104:107], v[24:25], v[20:21], v[104:107]\n"
    "s_nop 1\n"
    "v_rcp_f32 v108, v108\n"
    "s_nop 1\n"
    "v_mul_f32 v100, v100, v108\n"
    "v_mul_f32 v101, v101, v108\n"
    "v_mul_f32 v102, v102, v108\n"
    "v_mul_f32 v103, v103, v108\n"
    "v_cvt_pk_bf16_f32 v100, v100, v101\n"
    "v_cvt_pk_bf16_f32 v101, v102, v103\n"
    "v_pk_mul_f32 v[104:105], v[104:105], v[108:109] op_sel_hi:[1,0]\n"
    "v_pk_mul_f32 v[106:107], v[106:107], v[108:109] op_sel_hi:[1,0]\n"
    "ds_write2_b32 v14, v100, v101 offset0:0 offset1:8\n"
    "v_cvt_pk_bf16_f32 v104, v104, v105\n"
    "v_cvt_pk_bf16_f32 v105, v106, v107\n"
    "ds_write2_b32 v14, v104, v105 offset0:64 offset1:72\n"
    "v_mov_b64 v[44:45], 0\n"
    "v_mov_b64 v[46:47], 0\n"
    "v_mov_b32 v15, v15\n"
    "v_mov_b32 v15, v15\n"
    "v_mov_b32 v15, v15\n"
    "v_mov_b32 v15, v15\n"
    "v_mov_b32 v15, v15\n"
    "v_mov_b32 v15, v15\n"
    "v_mfma_f32_32x32x16_bf16 v[28:43], v[44:47], v[44:47], 0\n"
    "s_waitcnt lgkmcnt(0)\n"
    "s_barrier\n"
    "ds_read_b128 v[68:71], v0 offset:53248\n"
    "ds_read_b128 v[72:75], v0 offset:54272\n"
    "v_mfma_f32_32x32x16_bf16 v[124:139], v[44:47], v[44:47], 0\n"
    "s_waitcnt lgkmcnt(1)\n"
    "v_mov_b32 v15, v15\n"
    "v_mov_b32 v15, v15\n"
    "v_mov_b32 v15, v15\n"
    "v_smfmac_f32_16x16x64_bf16 v[28:31], v[68:71], v[176:183], v7\n"
    "v_smfmac_f32_16x16x64_bf16 v[32:35], v[68:71], v[192:199], v7\n"
    "s_waitcnt lgkmcnt(0)\n"
    "v_smfmac_f32_16x16x64_bf16 v[28:31], v[72:75], v[184:191], v7\n"
    "v_smfmac_f32_16x16x64_bf16 v[32:35], v[72:75], v[200:207], v7\n"
    "v_smfmac_f32_16x16x64_bf16 v[36:39], v[68:71], v[208:215], v7\n"
    "v_smfmac_f32_16x16x64_bf16 v[36:39], v[72:75], v[216:223], v7\n"
    "v_add_f32 v28, v28, v30\n"
    "v_smfmac_f32_16x16x64_bf16 v[40:43], v[68:71], v[224:231], v7\n"
    "v_add_f32 v29, v29, v31\n"
    "v_smfmac_f32_16x16x64_bf16 v[40:43], v[72:75], v[232:239], v7\n"
    "v_cvt_pk_bf16_f32 v28, v28, v29\n"
    "v_smfmac_f32_16x16x64_bf16 v[44:47], v[68:71], v[240:247], v7\n"
    "v_add_f32 v32, v32, v34\n"
    "v_smfmac_f32_16x16x64_bf16 v[44:47], v[72:75], v[248:255], v7\n"
    "v_add_f32 v33, v33, v35\n"
    "v_cvt_pk_bf16_f32 v29, v32, v33\n"
    "v_add_f32 v36, v36, v38\n"
    "v_add_f32 v37, v37, v39\n"
    "v_cvt_pk_bf16_f32 v30, v36, v37\n"
    "v_add_f32 v40, v40, v42\n"
    "v_add_f32 v41, v41, v43\n"
    "v_cvt_pk_bf16_f32 v31, v40, v41\n"
    "buffer_store_dwordx4 v[28:31], v8, s[36:39], s65 offen sc1\n"
    "v_add_f32 v44, v44, v46\n"
    "v_add_f32 v45, v45, v47\n"
    "v_cvt_pk_bf16_f32 v44, v44, v45\n"
    "buffer_store_dword v44, v5, s[36:39], s65 offen sc1\n"
    "s_cmp_eq_u32 s64, 62\n"
    "s_cbranch_scc1 L_exit_%=\n"
    "v_xor_b32 v1, 0x4000, v1\n"
    "v_xor_b32 v6, 0x4000, v6\n"
    "s_bitcmp1_b32 s64, 0\n"
    "s_cselect_b32 s76, 0x4000, 0\n"
    "s_add_u32 m0, s76, 0\n"
    "s_add_u32 s77, s44, 0\n"
    "buffer_load_dwordx4 v0, s[36:39], s77 offen offset:0 sc1 lds\n"
    "buffer_load_dwordx4 v0, s[36:39], s77 offen offset:1024 sc1 lds\n"
    "buffer_load_dwordx4 v0, s[36:39], s77 offen offset:2048 sc1 lds\n"
    "buffer_load_dwordx4 v0, s[36:39], s77 offen offset:3072 sc1 lds\n"
    "s_add_u32 m0, s76, 4096\n"
    "s_add_u32 s77, s44, 4096\n"
    "buffer_load_dwordx4 v0, s[36:39], s77 offen offset:0 sc1 lds\n"
    "buffer_load_dwordx4 v0, s[36:39], s77 offen offset:1024 sc1 lds\n"
    "buffer_load_dwordx4 v0, s[36:39], s77 offen offset:2048 sc1 lds\n"
    "buffer_load_dwordx4 v0, s[36:39], s77 offen offset:3072 sc1 lds\n"
    "s_add_u32 m0, s76, 8192\n"
    "s_add_u32 s77, s45, 0\n"
    "buffer_load_dwordx4 v0, s[36:39], s77 offen offset:0 sc1 lds\n"
    "buffer_load_dwordx4 v0, s[36:39], s77 offen offset:1024 sc1 lds\n"
    "buffer_load_dwordx4 v0, s[36:39], s77 offen offset:2048 sc1 lds\n"
    "buffer_load_dwordx4 v0, s[36:39], s77 offen offset:3072 sc1 lds\n"
    "s_add_u32 m0, s76, 12288\n"
    "s_add_u32 s77, s45, 4096\n"
    "buffer_load_dwordx4 v0, s[36:39], s77 offen offset:0 sc1 lds\n"
    "buffer_load_dwordx4 v0, s[36:39], s77 offen offset:1024 sc1 lds\n"
    "buffer_load_dwordx4 v0, s[36:39], s77 offen offset:2048 sc1 lds\n"
    "buffer_load_dwordx4 v0, s[36:39], s77 offen offset:3072 sc1 lds\n"
    "s_add_u32 s44, s44, 0x40000\n"
    "s_add_u32 s45, s45, 0x40000\n"
    "ds_read_b128 v[68:71], v2 offset:0\n"
    "ds_read_b128 v[72:75], v2 offset:1024\n"
    "ds_read_b128 v[76:79], v2 offset:2048\n"
    "ds_read_b128 v[80:83], v2 offset:3072\n"
    "ds_read_b128 v[100:103], v2 offset:4096\n"
    "ds_read_b128 v[104:107], v2 offset:5120\n"
    "ds_read_b128 v[108:111], v2 offset:6144\n"
    "ds_read_b128 v[112:115], v2 offset:7168\n"
    "ds_read_b128 v[84:87], v1 offset:0\n"
    "ds_read_b128 v[88:91], v1 offset:4096\n"
    "ds_read_b128 v[92:95], v1 offset:1024\n"
    "ds_read_b128 v[96:99], v1 offset:5120\n"
    "ds_read_b128 v[116:119], v6 offset:0\n"
    "ds_read_b128 v[120:123], v6 offset:1024\n"
    "v_mov_b64 v[124:125], 0\n"
    "v_mov_b64 v[126:127], 0\n"
    "v_mov_b64 v[128:129], 0\n"
    "v_mov_b64 v[130:131], 0\n"
    "v_mov_b64 v[132:133], 0\n"
    "v_mov_b64 v[134:135], 0\n"
    "v_mov_b64 v[136:137], 0\n"
    "v_mov_b64 v[138:139], 0\n"
    "s_waitcnt lgkmcnt(0)\n"
    "s_add_u32 s76, s64, 3\n"
    "s_cmp_gt_u32 s47, s76\n"
    "s_cbranch_scc1 R1_kvs_%=\n"
    "s_add_u32 s77, s76, 1\n"
    "s_mul_i32 s78, s76, 43\n"
    "s_lshr_b32 s78, s78, 7\n"
    "s_mul_i32 s78, s78, 3\n"
    "s_sub_u32 s76, s76, s78\n"
    "s_cmp_le_u32 s77, s48\n"
    "s_cselect_b32 s76, s76, 0\n"
    "v_lshlrev_b32 v142, 3, v0\n"
    "s_mov_b32 s79, 0\n"
    "R1_pk_%=:\n"
    "s_mov_b64 exec, 7\n"
    "buffer_load_dword v143, v142, s[36:39], s52 offen sc1\n"
    "s_waitcnt vmcnt(0)\n"
    "s_mov_b64 exec, -1\n"
    "v_readlane_b32 s78, v143, s76\n"
    "v_readlane_b32 s47, v143, 0\n"
    "v_readlane_b32 s94, v143, 1\n"
    "v_readlane_b32 s95, v143, 2\n"
    "s_nop 0\n"
    "s_min_u32 s47, s47, s94\n"
    "s_min_u32 s47, s47, s95\n"
    "s_cmp_ge_u32 s78, s77\n"
    "s_cbranch_scc1 R1_qk_%=\n"
    "s_sleep 2\n"
    "s_cmp_lg_u32 s69, 0\n"
    "s_cbranch_scc1 R1_qk_%=\n"
    "s_add_u32 s79, s79, 1\n"
    "s_cmp_lt_u32 s79, 0x40000\n"
    "s_cbranch_scc1 R1_pk_%=\n"
    "s_mov_b32 s69, 1\n"
    "s_branch R1_qk_%=\n"
    "R1_qk_%=:\n"
    "R1_kvs_%=:\n"
    "s_add_u32 s80, s66, s42\n"
    "s_add_u32 s64, s64, 1\n"
    "s_and_b32 s76, s64, 1\n"
    "s_mul_i32 s76, s76, 0xa0000\n"
    "s_add_u32 s65, s40, s76\n"
    "s_xor_b32 s76, s76, 0xa0000\n"
    "s_add_u32 s71, s40, s76\n"
    "s_and_b32 s76, s64, 3\n"
    "s_lshl_b32 s76, s76, 17\n"
    "s_or_b32 s76, s76, 0x400000\n"
    "s_add_u32 s66, s40, s76\n"
    "s_xor_b32 s76, s76, 0x40000\n"
    "s_add_u32 s68, s40, s76\n"
    "s_add_u32 s67, s66, s60\n"
    "s_mov_b32 s70, 0\n"
    "s_branch R1_wait_%=\n"
    "R1_nan_%=:\n"
    "s_cmp_lg_u32 s70, 0\n"
    "s_cbranch_scc1 R1_nanok_%=\n"
    "s_branch R1_redo_%=\n"
    "R1_miss_%=:\n"
    "s_cmp_lg_u32 s70, 0\n"
    "s_cbranch_scc1 R1_missok_%=\n"
    "R1_redo_%=:\n"
    "buffer_load_dwordx4 v[84:87], v0, s[36:39], s80 offen offset:0 sc1\n"
    "buffer_load_dwordx4 v[88:91], v0, s[36:39], s80 offen offset:1024 sc1\n"
    "s_add_u32 s88, s88, 1\n"
    "s_mov_b32 s79, 0\n"
    "s_mov_b32 s70, 400\n"
    "s_nop 7\n"
    "s_waitcnt lgkmcnt(0)\n"
    "s_barrier\n"
    "v_add_u32 v28, s43, v0\n"
    "s_cmp_eq_u32 s64, 0\n"
    "s_cbranch_scc1 R1_rdone_%=\n"
    "R1_rl_%=:\n"
    "s_waitcnt vmcnt(0)\n"
    "v_max3_u32 v29, v84, v85, v86\n"
    "v_max3_u32 v29, v29, v87, v88\n"
    "v_max3_u32 v29, v29, v89, v90\n"
    "v_max_u32 v29, v29, v91\n"
    "v_cmp_eq_u32 vcc, -1, v29\n"
    "s_cbranch_vccz R1_rw_%=\n"
    "buffer_load_dwordx4 v[84:87], v0, s[36:39], s80 offen offset:0 sc1\n"
    "buffer_load_dwordx4 v[88:91], v0, s[36:39], s80 offen offset:1024 sc1\n"
    "s_cmp_lg_u32 s69, 0\n"
    "s_cbranch_scc1 R1_rdone_%=\n"
    "s_add_u32 s79, s79, 1\n"
    "s_cmp_lt_u32 s79, 0x40000\n"
    "s_cbranch_scc1 R1_rl_%=\n"
    "s_mov_b32 s69, 1\n"
    "s_branch R1_rdone_%=\n"
    "R1_rw_%=:\n"
    "ds_write_b128 v28, v[84:87] offset:0\n"
    "ds_write_b128 v28, v[88:91] offset:1024\n"
    "R1_rdone_%=:\n"
    "s_waitcnt vmcnt(0) lgkmcnt(0)\n"
    "ds_read_b128 v[68:71], v2 offset:0\n"
    "ds_read_b128 v[72:75], v2 offset:1024\n"
    "ds_read_b128 v[76:79], v2 offset:2048\n"
    "ds_read_b128 v[80:83], v2 offset:3072\n"
    "ds_read_b128 v[100:103], v2 offset:4096\n"
    "ds_read_b128 v[104:107], v2 offset:5120\n"
    "ds_read_b128 v[108:111], v2 offset:6144\n"
    "ds_read_b128 v[112:115], v2 offset:7168\n"
    "ds_read_b128 v[84:87], v1 offset:0\n"
    "ds_read_b128 v[88:91], v1 offset:4096\n"
    "ds_read_b128 v[92:95], v1 offset:1024\n"
    "ds_read_b128 v[96:99], v1 offset:5120\n"
    "ds_read_b128 v[116:119], v6 offset:0\n"
    "ds_read_b128 v[120:123], v6 offset:1024\n"
    "v_mov_b64 v[124:125], 0\n"
    "v_mov_b64 v[126:127], 0\n"
    "v_mov_b64 v[128:129], 0\n"
    "v_mov_b64 v[130:131], 0\n"
    "v_mov_b64 v[132:133], 0\n"
    "v_mov_b64 v[134:135], 0\n"
    "v_mov_b64 v[136:137], 0\n"
    "v_mov_b64 v[138:139], 0\n"
    "s_waitcnt lgkmcnt(0)\n"
    "s_branch R1_wait_%=\n"
    "R1_vslow_%=:\n"
    "s_mov_b32 s79, 0\n"
    "R1_vl_%=:\n"
    "s_add_u32 s87, s87, 1\n"
    "buffer_load_dword v22, v9, s[36:39], s67 offen offset:0 sc1\n"
    "buffer_load_dword v24, v9, s[36:39], s67 offen offset:256 sc1\n"
    "s_waitcnt vmcnt(0)\n"
    "v_max_u32 v26, v22, v24\n"
    "v_cmp_eq_u32 vcc, -1, v26\n"
    "s_cbranch_vccz R1_vok_%=\n"
    "s_cmp_lg_u32 s69, 0\n"
    "s_cbranch_scc1 R1_vok_%=\n"
    "s_add_u32 s79, s79, 1\n"
    "s_cmp_lt_u32 s79, 0x40000\n"
    "s_cbranch_scc1 R1_vl_%=\n"
    "s_mov_b32 s69, 1\n"
    "s_branch R1_vok_%=\n"
    "R2_entry_%=:\n"
    "s_waitcnt vmcnt(0)\n"
    "s_barrier\n"
    "ds_read_b128 v[68:71], v2 offset:0\n"
    "ds_read_b128 v[72:75], v2 offset:1024\n"
    "ds_read_b128 v[76:79], v2 offset:2048\n"
    "ds_read_b128 v[80:83], v2 offset:3072\n"
    "ds_read_b128 v[100:103], v2 offset:4096\n"
    "ds_read_b128 v[104:107], v2 offset:5120\n"
    "ds_read_b128 v[108:111], v2 offset:6144\n"
    "ds_read_b128 v[112:115], v2 offset:7168\n"
    "ds_read_b128 v[84:87], v1 offset:0\n"
    "ds_read_b128 v[88:91], v1 offset:4096\n"
    "ds_read_b128 v[92:95], v1 offset:1024\n"
    "ds_read_b128 v[96:99], v1 offset:5120\n"
    "ds_read_b128 v[116:119], v6 offset:0\n"
    "ds_read_b128 v[120:123], v6 offset:1024\n"
    "v_mov_b64 v[124:125], 0\n"
    "v_mov_b64 v[126:127], 0\n"
    "v_mov_b64 v[128:129], 0\n"
    "v_mov_b64 v[130:131], 0\n"
    "v_mov_b64 v[132:133], 0\n"
    "v_mov_b64 v[134:135], 0\n"
    "v_mov_b64 v[136:137], 0\n"
    "v_mov_b64 v[138:139], 0\n"
    "s_waitcnt lgkmcnt(0)\n"
    "R2_wait_%=:\n"
    "s_barrier\n"
    "ds_read_b128 v[28:31], v12 offset:0\n"
    "ds_read_b128 v[32:35], v12 offset:1024\n"
    "ds_read_b128 v[36:39], v12 offset:2048\n"
    "s_waitcnt lgkmcnt(2)\n"
    "v_smfmac_f32_16x16x64_bf16 v[124:127], v[28:31], v[68:75], v7\n"
    "ds_read2st64_b32 v[26:27], v10 offset0:0 offset1:8\n"
    "v_smfmac_f32_16x16x64_bf16 v[128:131], v[28:31], v[76:83], v7\n"
    "ds_read_b128 v[40:43], v12 offset:3072\n"
    "s_waitcnt lgkmcnt(3)\n"
    "v_smfmac_f32_16x16x64_bf16 v[124:127], v[32:35], v[100:107], v7\n"
    "ds_read_b128 v[44:47], v12 offset:4096\n"
    "v_smfmac_f32_16x16x64_bf16 v[128:131], v[32:35], v[108:115], v7\n"
    "ds_read_b128 v[48:51], v12 offset:5120\n"
    "s_waitcnt lgkmcnt(4)\n"
    "v_smfmac_f32_16x16x64_bf16 v[124:127], v[36:39], a[0:7], v7\n"
    "ds_read_b128 v[52:55], v12 offset:6144\n"
    "v_smfmac_f32_16x16x64_bf16 v[128:131], v[36:39], a[8:15], v7\n"
    "ds_read_b128 v[56:59], v12 offset:7168\n"
    "s_waitcnt lgkmcnt(4)\n"
    "v_smfmac_f32_16x16x64_bf16 v[124:127], v[40:43], a[16:23], v7\n"
    "ds_read_b128 v[60:63], v12 offset:8192\n"
    "v_smfmac_f32_16x16x64_bf16 v[128:131], v[40:43], a[24:31], v7\n"
    "ds_read_b128 v[64:67], v12 offset:9216\n"
    "s_waitcnt lgkmcnt(5)\n"
    "v_smfmac_f32_16x16x64_bf16 v[124:127], v[44:47], a[32:39], v7\n"
    "v_max_u32 v26, v26, v27\n"
    "v_smfmac_f32_16x16x64_bf16 v[128:131], v[44:47], a[40:47], v7\n"
    "v_cmp_eq_u32 vcc, -1, v26\n"
    "s_waitcnt lgkmcnt(4)\n"
    "v_smfmac_f32_16x16x64_bf16 v[124:127], v[48:51], a[48:55], v7\n"
    "v_smfmac_f32_16x16x64_bf16 v[128:131], v[48:51], a[56:63], v7\n"
    "s_cbranch_vccnz R2_miss_%=\n"
    "R2_missok_%=:\n"
    "s_waitcnt lgkmcnt(3)\n"
    "v_smfmac_f32_16x16x64_bf16 v[124:127], v[52:55], a[64:71], v7\n"
    "ds_read_b128 v[100:103], v1 offset:2048\n"
    "v_smfmac_f32_16x16x64_bf16 v[128:131], v[52:55], a[72:79], v7\n"
    "s_waitcnt lgkmcnt(3)\n"
    "v_smfmac_f32_16x16x64_bf16 v[124:127], v[56:59], a[80:87], v7\n"
    "ds_read_b128 v[104:107], v1 offset:6144\n"
    "v_smfmac_f32_16x16x64_bf16 v[128:131], v[56:59], a[88:95], v7\n"
    "s_waitcnt lgkmcnt(3)\n"
    "v_smfmac_f32_16x16x64_bf16 v[124:127], v[60:63], a[96:103], v7\n"
    "ds_read_b128 v[108:111], v1 offset:3072\n"
    "v_smfmac_f32_16x16x64_bf16 v[128:131], v[60:63], a[104:111], v7\n"
    "s_waitcnt lgkmcnt(3)\n"
    "v_smfmac_f32_16x16x64_bf16 v[124:127], v[64:67], a[112:119], v7\n"
    "ds_read_b128 v[112:115], v1 offset:7168\n"
    "v_smfmac_f32_16x16x64_bf16 v[128:131], v[64:67], a[120:127], v7\n"
    "v_smfmac_f32_16x16x64_bf16 v[132:135], v[28:31], a[128:135], v7\n"
    "v_smfmac_f32_16x16x64_bf16 v[136:139], v[28:31], a[136:143], v7\n"
    "v_add_f32 v124, v124, v126\n"
    "v_smfmac_f32_16x16x64_bf16 v[132:135], v[32:35], a[144:151], v7\n"
    "v_add_f32 v125, v125, v127\n"
    "v_smfmac_f32_16x16x64_bf16 v[136:139], v[32:35], a[152:159], v7\n"
    "v_cvt_pk_bf16_f32 v124, v124, v125\n"
    "v_smfmac_f32_16x16x64_bf16 v[132:135], v[36:39], a[160:167], v7\n"
    "v_add_f32 v128, v128, v130\n"
    "v_smfmac_f32_16x16x64_bf16 v[136:139], v[36:39], a[168:175], v7\n"
    "v_add_f32 v129, v129, v131\n"
    "v_smfmac_f32_16x16x64_bf16 v[132:135], v[40:43], a[176:183], v7\n"
    "v_cvt_pk_bf16_f32 v128, v128, v129\n"
    "v_smfmac_f32_16x16x64_bf16 v[136:139], v[40:43], a[184:191], v7\n"
    "ds_write2_b32 v11, v124, v128 offset0:0 offset1:64\n"
    "v_smfmac_f32_16x16x64_bf16 v[132:135], v[44:47], a[192:199], v7\n"
    "v_smfmac_f32_16x16x64_bf16 v[136:139], v[44:47], a[200:207], v7\n"
    "v_smfmac_f32_16x16x64_bf16 v[132:135], v[48:51], a[208:215], v7\n"
    "v_smfmac_f32_16x16x64_bf16 v[136:139], v[48:51], a[216:223], v7\n"
    "v_smfmac_f32_16x16x64_bf16 v[132:135], v[52:55], a[224:231], v7\n"
    "s_waitcnt lgkmcnt(0)\n"
    "s_barrier\n"
    "ds_read_b64_tr_b16 v[68:69], v4 offset:0\n"
    "v_smfmac_f32_16x16x64_bf16 v[136:139], v[52:55], a[232:239], v7\n"
    "buffer_load_dword v22, v9, s[36:39], s67 offen offset:0 sc1\n"
    "v_smfmac_f32_16x16x64_bf16 v[132:135], v[56:59], a[240:247], v7\n"
    "ds_read_b64_tr_b16 v[70:71], v4 offset:256\n"
    "v_smfmac_f32_16x16x64_bf16 v[132:135], v[60:63], v[144:151], v7\n"
    "buffer_load_dword v24, v9, s[36:39], s67 offen offset:256 sc1\n"
    "v_smfmac_f32_16x16x64_bf16 v[132:135], v[64:67], v[160:167], v7\n"
    "ds_read_b64_tr_b16 v[72:73], v4 offset:512\n"
    "v_smfmac_f32_16x16x64_bf16 v[136:139], v[56:59], a[248:255], v7\n"
    "ds_read_b64_tr_b16 v[74:75], v4 offset:768\n"
    "v_smfmac_f32_16x16x64_bf16 v[136:139], v[60:63], v[152:159], v7\n"
    "ds_read_b64_tr_b16 v[76:77], v4 offset:1024\n"
    "v_smfmac_f32_16x16x64_bf16 v[136:139], v[64:67], v[168:175], v7\n"
    "ds_read_b64_tr_b16 v[78:79], v4 offset:1280\n"
    "s_waitcnt lgkmcnt(4)\n"
    "v_mfma_f32_16x16x32_bf16 v[124:127], v[84:87], v[68:71], 0\n"
    "v_add_f32 v132, v132, v134\n"
    "v_add_f32 v133, v133, v135\n"
    "v_mfma_f32_16x16x32_bf16 v[128:131], v[88:91], v[68:71], 0\n"
    "v_cvt_pk_bf16_f32 v132, v132, v133\n"
    "ds_read_b64_tr_b16 v[80:81], v4 offset:1536\n"
    "v_add_f32 v136, v136, v138\n"
    "v_add_f32 v137, v137, v139\n"
    "v_cvt_pk_bf16_f32 v136, v136, v137\n"
    "ds_write2_b32 v3, v132, v136 offset0:0 offset1:32\n"
    "s_waitcnt lgkmcnt(4)\n"
    "v_mfma_f32_16x16x32_bf16 v[124:127], v[92:95], v[72:75], v[124:127]\n"
    "ds_read_b64_tr_b16 v[82:83], v4 offset:1792\n"
    "v_mfma_f32_16x16x32_bf16 v[128:131], v[96:99], v[72:75], v[128:131]\n"
    "v_cmp_u_f32 vcc, v68, v68\n"
    "s_waitcnt lgkmcnt(3)\n"
    "v_mfma_f32_16x16x32_bf16 v[124:127], v[100:103], v[76:79], v[124:127]\n"
    "v_mfma_f32_16x16x32_bf16 v[128:131], v[104:107], v[76:79], v[128:131]\n"
    "s_cbranch_vccnz R2_nan_%=\n"
    "R2_nanok_%=:\n"
    "s_waitcnt lgkmcnt(1)\n"
    "s_waitcnt lgkmcnt(0)\n"
    "v_mfma_f32_16x16x32_bf16 v[124:127], v[108:111], v[80:83], v[124:127]\n"
    "s_barrier\n"
    "ds_read_b64_tr_b16 v[52:53], v13 offset:0\n"
    "ds_read_b64_tr_b16 v[54:55], v13 offset:128\n"
    "v_mfma_f32_16x16x32_bf16 v[128:131], v[112:115], v[80:83], v[128:131]\n"
    "ds_read_b64_tr_b16 v[56:57], v13 offset:256\n"
    "ds_read_b64_tr_b16 v[58:59], v13 offset:384\n"
    "v_mov_b32 v15, v15\n"
    "v_mov_b32 v15, v15\n"
    "v_mov_b32 v15, v15\n"
    "v_mov_b32 v15, v15\n"
    "v_mov_b32 v15, v15\n"
    "v_mov_b32 v15, v15\n"
    "v_mov_b32 v15, v15\n"
    "s_waitcnt lgkmcnt(2)\n"
    "v_mfma_f32_16x16x32_bf16 v[140:143], v[52:55], v[68:71], 0\n"
    "v_mfma_f32_16x16x32_bf16 v[60:63], v[52:55], v[72:75], 0\n"
    "v_max3_f32 v138, v124, v125, v126\n"
    "v_max3_f32 v139, v128, v129, v130\n"
    "s_waitcnt lgkmcnt(0)\n"
    "v_mfma_f32_16x16x32_bf16 v[140:143], v[56:59], v[76:79], v[140:143]\n"
    "v_max3_f32 v138, v138, v127, v131\n"
    "v_max_f32 v138, v138, v139\n"
    "v_mfma_f32_16x16x32_bf16 v[60:63], v[56:59], v[80:83], v[60:63]\n"
    "v_mov_b32 v15, v15\n"
    "v_mov_b32 v15, v15\n"
    "v_mov_b32 v15, v15\n"
    "v_mov_b32 v15, v15\n"
    "v_mov_b32 v15, v15\n"
    "v_mov_b32 v15, v15\n"
    "v_mov_b32 v15, v15\n"
    "v_mov_b32 v15, v15\n"
    "v_add_f32 v140, v140, v62\n"
    "v_add_f32 v141, v141, v63\n"
    "v_max3_f32 v134, v138, v140, v141\n"
    "v_max3_f32 v138, v138, v140, v141\n"
    "s_nop 1\n"
    "v_permlane16_swap_b32 v138, v134\n"
    "v_max_f32 v139, v138, v134\n"
    "v_max_f32 v138, v138, v134\n"
    "s_nop 1\n"
    "v_permlane32_swap_b32 v138, v139\n"
    "v_max_f32 v138, v138, v139\n"
    "v_mul_f32 v138, 0xbe0293ee, v138\n"
    "v_pk_fma_f32 v[124:125], v[124:125], s[72:73], v[138:139] op_sel_hi:[1,0,0]\n"
    "v_pk_fma_f32 v[126:127], v[126:127], s[72:73], v[138:139] op_sel_hi:[1,0,0]\n"
    "v_pk_fma_f32 v[128:129], v[128:129], s[72:73], v[138:139] op_sel_hi:[1,0,0]\n"
    "v_pk_fma_f32 v[130:131], v[130:131], s[72:73], v[138:139] op_sel_hi:[1,0,0]\n"
    "v_pk_fma_f32 v[140:141], v[140:141], s[72:73], v[138:139] op_sel_hi:[1,0,0]\n"
    "v_exp_f32 v124, v124\n"
    "v_exp_f32 v125, v125\n"
    "v_exp_f32 v126, v126\n"
    "v_exp_f32 v127, v127\n"
    "v_exp_f32 v128, v128\n"
    "v_exp_f32 v129, v129\n"
    "v_exp_f32 v130, v130\n"
    "v_exp_f32 v131, v131\n"
    "v_cvt_pk_bf16_f32 v112, v124, v125\n"
    "v_cvt_pk_bf16_f32 v113, v126, v127\n"
    "v_cvt_pk_bf16_f32 v114, v128, v129\n"
    "v_cvt_pk_bf16_f32 v115, v130, v131\n"
    "v_exp_f32 v140, v140\n"
    "v_mfma_f32_16x16x32_bf16 v[108:111], v[16:19], v[112:115], 0\n"
    "v_exp_f32 v141, v141\n"
    "v_mfma_f32_16x16x32_bf16 v[100:103], v[116:119], v[112:115], 0\n"
    "v_cvt_pk_bf16_f32 v20, v140, v141\n"
    "s_waitcnt vmcnt(0)\n"
    "v_max_u32 v26, v22, v24\n"
    "v_mfma_f32_16x16x32_bf16 v[104:107], v[120:123], v[112:115], 0\n"
    "v_cmp_eq_u32 vcc, -1, v26\n"
    "v_mov_b32 v15, v15\n"
    "v_mfma_f32_16x16x16_bf16 v[108:111], v[16:17], v[20:21], v[108:111]\n"
    "v_mov_b32 v15, v15\n"
    "s_cbranch_vccnz R2_vslow_%=\n"
    "R2_vok_%=:\n"
    "v_mfma_f32_16x16x16_bf16 v[100:103], v[22:23], v[20:21], v[100:103]\n"
    "v_mfma_f32_16x16x16_bf16 v[104:107], v[24:25], v[20:21], v[104:107]\n"
    "s_nop 1\n"
    "v_rcp_f32 v108, v108\n"
    "s_nop 1\n"
    "v_mul_f32 v100, v100, v108\n"
    "v_mul_f32 v101, v101, v108\n"
    "v_mul_f32 v102, v102, v108\n"
    "v_mul_f32 v103, v103, v108\n"
    "v_cvt_pk_bf16_f32 v100, v100, v101\n"
    "v_cvt_pk_bf16_f32 v101, v102, v103\n"
    "v_pk_mul_f32 v[104:105], v[104:105], v[108:109] op_sel_hi:[1,0]\n"
    "v_pk_mul_f32 v[106:107], v[106:107], v[108:109] op_sel_hi:[1,0]\n"
    "ds_write2_b32 v14, v100, v101 offset0:0 offset1:8\n"
    "v_cvt_pk_bf16_f32 v104, v104, v105\n"
    "v_cvt_pk_bf16_f32 v105, v106, v107\n"
    "ds_write2_b32 v14, v104, v105 offset0:64 offset1:72\n"
    "v_mov_b64 v[44:45], 0\n"
    "v_mov_b64 v[46:47], 0\n"
    "v_mov_b32 v15, v15\n"
    "v_mov_b32 v15, v15\n"
    "v_mov_b32 v15, v15\n"
    "v_mov_b32 v15, v15\n"
    "v_mov_b32 v15, v15\n"
    "v_mov_b32 v15, v15\n"
    "v_mfma_f32_32x32x16_bf16 v[28:43], v[44:47], v[44:47], 0\n"
    "s_waitcnt lgkmcnt(0)\n"
    "s_barrier\n"
    "ds_read_b128 v[68:71], v0 offset:53248\n"
    "ds_read_b128 v[72:75], v0 offset:54272\n"
    "v_mfma_f32_32x32x16_bf16 v[124:139], v[44:47], v[44:47], 0\n"
    "s_waitcnt lgkmcnt(1)\n"
    "v_smfmac_f32_16x16x64_bf16 v[44:47], v[68:71], v[240:247], v7\n"
    "v_smfmac_f32_16x16x64_bf16 v[28:31], v[68:71], v[176:183], v7\n"
    "s_waitcnt lgkmcnt(0)\n"
    "v_smfmac_f32_16x16x64_bf16 v[44:47], v[72:75], v[248:255], v7\n"
    "v_smfmac_f32_16x16x64_bf16 v[28:31], v[72:75], v[184:191], v7\n"
    "v_smfmac_f32_16x16x64_bf16 v[32:35], v[68:71], v[192:199], v7\n"
    "v_smfmac_f32_16x16x64_bf16 v[32:35], v[72:75], v[200:207], v7\n"
    "v_add_f32 v44, v44, v46\n"
    "v_smfmac_f32_16x16x64_bf16 v[36:39], v[68:71], v[208:215], v7\n"
    "v_add_f32 v45, v45, v47\n"
    "v_smfmac_f32_16x16x64_bf16 v[36:39], v[72:75], v[216:223], v7\n"
    "v_cvt_pk_bf16_f32 v44, v44, v45\n"
    "v_smfmac_f32_16x16x64_bf16 v[40:43], v[68:71], v[224:231], v7\n"
    "v_add_f32 v28, v28, v30\n"
    "v_smfmac_f32_16x16x64_bf16 v[40:43], v[72:75], v[232:239], v7\n"
    "buffer_store_dword v44, v5, s[36:39], s65 offen sc1\n"
    "v_add_f32 v29, v29, v31\n"
    "v_cvt_pk_bf16_f32 v28, v28, v29\n"
    "v_add_f32 v32, v32, v34\n"
    "v_add_f32 v33, v33, v35\n"
    "v_cvt_pk_bf16_f32 v29, v32, v33\n"
    "v_add_f32 v36, v36, v38\n"
    "v_add_f32 v37, v37, v39\n"
    "v_cvt_pk_bf16_f32 v30, v36, v37\n"
    "v_add_f32 v40, v40, v42\n"
    "v_add_f32 v41, v41, v43\n"
    "v_cvt_pk_bf16_f32 v31, v40, v41\n"
    "buffer_store_dwordx4 v[28:31], v8, s[36:39], s65 offen sc1\n"
    "s_cmp_eq_u32 s64, 62\n"
    "s_cbranch_scc1 L_exit_%=\n"
    "v_xor_b32 v1, 0x4000, v1\n"
    "v_xor_b32 v6, 0x4000, v6\n"
    "ds_read_b128 v[68:71], v2 offset:0\n"
    "ds_read_b128 v[72:75], v2 offset:1024\n"
    "ds_read_b128 v[76:79], v2 offset:2048\n"
    "ds_read_b128 v[80:83], v2 offset:3072\n"
    "ds_read_b128 v[100:103], v2 offset:4096\n"
    "ds_read_b128 v[104:107], v2 offset:5120\n"
    "ds_read_b128 v[108:111], v2 offset:6144\n"
    "ds_read_b128 v[112:115], v2 offset:7168\n"
    "ds_read_b128 v[84:87], v1 offset:0\n"
    "ds_read_b128 v[88:91], v1 offset:4096\n"
    "ds_read_b128 v[92:95], v1 offset:1024\n"
    "ds_read_b128 v[96:99], v1 offset:5120\n"
    "ds_read_b128 v[116:119], v6 offset:0\n"
    "ds_read_b128 v[120:123], v6 offset:1024\n"
    "v_mov_b64 v[124:125], 0\n"
    "v_mov_b64 v[126:127], 0\n"
    "v_mov_b64 v[128:129], 0\n"
    "v_mov_b64 v[130:131], 0\n"
    "v_mov_b64 v[132:133], 0\n"
    "v_mov_b64 v[134:135], 0\n"
    "v_mov_b64 v[136:137], 0\n"
    "v_mov_b64 v[138:139], 0\n"
    "s_waitcnt lgkmcnt(0)\n"
    "s_add_u32 s80, s66, s42\n"
    "s_add_u32 s64, s64, 1\n"
    "s_and_b32 s76, s64, 1\n"
    "s_mul_i32 s76, s76, 0xa0000\n"
    "s_add_u32 s65, s40, s76\n"
    "s_xor_b32 s76, s76, 0xa0000\n"
    "s_add_u32 s71, s40, s76\n"
    "s_and_b32 s76, s64, 3\n"
    "s_lshl_b32 s76, s76, 17\n"
    "s_or_b32 s76, s76, 0x400000\n"
    "s_add_u32 s66, s40, s76\n"
    "s_xor_b32 s76, s76, 0x40000\n"
    "s_add_u32 s68, s40, s76\n"
    "s_add_u32 s67, s66, s60\n"
    "s_mov_b32 s70, 0\n"
    "s_branch R2_wait_%=\n"
    "R2_nan_%=:\n"
    "s_cmp_lg_u32 s70, 0\n"
    "s_cbranch_scc1 R2_nanok_%=\n"
    "s_branch R2_redo_%=\n"
    "R2_miss_%=:\n"
    "s_cmp_lg_u32 s70, 0\n"
    "s_cbranch_scc1 R2_missok_%=\n"
    "R2_redo_%=:\n"
    "buffer_load_dwordx4 v[84:87], v0, s[36:39], s80 offen offset:0 sc1\n"
    "buffer_load_dwordx4 v[88:91], v0, s[36:39], s80 offen offset:1024 sc1\n"
    "buffer_load_dwordx4 v[92:95], v0, s[36:39], s80 offen offset:2048 sc1\n"
    "s_add_u32 s88, s88, 1\n"
    "s_mov_b32 s79, 0\n"
    "s_mov_b32 s70, 400\n"
    "s_nop 7\n"
    "s_waitcnt lgkmcnt(0)\n"
    "s_barrier\n"
    "v_add_u32 v28, s43, v0\n"
    "s_cmp_eq_u32 s64, 0\n"
    "s_cbranch_scc1 R2_rdone_%=\n"
    "R2_rl_%=:\n"
    "s_waitcnt vmcnt(0)\n"
    "v_max3_u32 v29, v84, v85, v86\n"
    "v_max3_u32 v29, v29, v87, v88\n"
    "v_max3_u32 v29, v29, v89, v90\n"
    "v_max3_u32 v29, v29, v91, v92\n"
    "v_max3_u32 v29, v29, v93, v94\n"
    "v_max_u32 v29, v29, v95\n"
    "v_cmp_eq_u32 vcc, -1, v29\n"
    "s_cbranch_vccz R2_rw_%=\n"
    "buffer_load_dwordx4 v[84:87], v0, s[36:39], s80 offen offset:0 sc1\n"
    "buffer_load_dwordx4 v[88:91], v0, s[36:39], s80 offen offset:1024 sc1\n"
    "buffer_load_dwordx4 v[92:95], v0, s[36:39], s80 offen offset:2048 sc1\n"
    "s_cmp_lg_u32 s69, 0\n"
    "s_cbranch_scc1 R2_rdone_%=\n"
    "s_add_u32 s79, s79, 1\n"
    "s_cmp_lt_u32 s79, 0x40000\n"
    "s_cbranch_scc1 R2_rl_%=\n"
    "s_mov_b32 s69, 1\n"
    "s_branch R2_rdone_%=\n"
    "R2_rw_%=:\n"
    "ds_write_b128 v28, v[84:87] offset:0\n"
    "ds_write_b128 v28, v[88:91] offset:1024\n"
    "ds_write_b128 v28, v[92:95] offset:2048\n"
    "R2_rdone_%=:\n"
    "s_waitcnt vmcnt(0) lgkmcnt(0)\n"
    "ds_read_b128 v[68:71], v2 offset:0\n"
    "ds_read_b128 v[72:75], v2 offset:1024\n"
    "ds_read_b128 v[76:79], v2 offset:2048\n"
    "ds_read_b128 v[80:83], v2 offset:3072\n"
    "ds_read_b128 v[100:103], v2 offset:4096\n"
    "ds_read_b128 v[104:107], v2 offset:5120\n"
    "ds_read_b128 v[108:111], v2 offset:6144\n"
    "ds_read_b128 v[112:115], v2 offset:7168\n"
    "ds_read_b128 v[84:87], v1 offset:0\n"
    "ds_read_b128 v[88:91], v1 offset:4096\n"
    "ds_read_b128 v[92:95], v1 offset:1024\n"
    "ds_read_b128 v[96:99], v1 offset:5120\n"
    "ds_read_b128 v[116:119], v6 offset:0\n"
    "ds_read_b128 v[120:123], v6 offset:1024\n"
    "v_mov_b64 v[124:125], 0\n"
    "v_mov_b64 v[126:127], 0\n"
    "v_mov_b64 v[128:129], 0\n"
    "v_mov_b64 v[130:131], 0\n"
    "v_mov_b64 v[132:133], 0\n"
    "v_mov_b64 v[134:135], 0\n"
    "v_mov_b64 v[136:137], 0\n"
    "v_mov_b64 v[138:139], 0\n"
    "s_waitcnt lgkmcnt(0)\n"
    "s_branch R2_wait_%=\n"
    "R2_vslow_%=:\n"
    "s_mov_b32 s79, 0\n"
    "R2_vl_%=:\n"
    "s_add_u32 s87, s87, 1\n"
    "buffer_load_dword v22, v9, s[36:39], s67 offen offset:0 sc1\n"
    "buffer_load_dword v24, v9, s[36:39], s67 offen offset:256 sc1\n"
    "s_waitcnt vmcnt(0)\n"
    "v_max_u32 v26, v22, v24\n"
    "v_cmp_eq_u32 vcc, -1, v26\n"
    "s_cbranch_vccz R2_vok_%=\n"
    "s_cmp_lg_u32 s69, 0\n"
    "s_cbranch_scc1 R2_vok_%=\n"
    "s_add_u32 s79, s79, 1\n"
    "s_cmp_lt_u32 s79, 0x40000\n"
    "s_cbranch_scc1 R2_vl_%=\n"
    "s_mov_b32 s69, 1\n"
    "s_branch R2_vok_%=\n"
    "R3_entry_%=:\n"
    "s_memrealtime s[76:77]\n"
    "s_waitcnt lgkmcnt(0)\n"
    "R3_cali_%=:\n"
    "s_memrealtime s[78:79]\n"
    "s_memtime s[80:81]\n"
    "s_waitcnt lgkmcnt(0)\n"
    "s_cmp_lg_u32 s78, s76\n"
    "s_cbranch_scc1 R3_caldi_%=\n"
    "s_memrealtime s[78:79]\n"
    "s_memtime s[80:81]\n"
    "s_waitcnt lgkmcnt(0)\n"
    "s_cmp_lg_u32 s78, s76\n"
    "s_cbranch_scc1 R3_caldi_%=\n"
    "s_memrealtime s[78:79]\n"
    "s_memtime s[80:81]\n"
    "s_waitcnt lgkmcnt(0)\n"
    "s_cmp_lg_u32 s78, s76\n"
    "s_cbranch_scc1 R3_caldi_%=\n"
    "s_memrealtime s[78:79]\n"
    "s_memtime s[80:81]\n"
    "s_waitcnt lgkmcnt(0)\n"
    "s_cmp_lg_u32 s78, s76\n"
    "s_cbranch_scc1 R3_caldi_%=\n"
    "s_branch R3_cali_%=\n"
    "R3_caldi_%=:\n"
    "s_lshl_b32 s78, s78, 5\n"
    "s_mov_b32 s82, s78\n"
    "s_mov_b32 s83, s80\n"
    "s_mov_b32 s80, s66\n"
    "s_waitcnt vmcnt(0)\n"
    "s_barrier\n"
    "ds_read_b128 v[68:71], v2 offset:0\n"
    "ds_read_b128 v[72:75], v2 offset:1024\n"
    "ds_read_b128 v[76:79], v2 offset:2048\n"
    "ds_read_b128 v[80:83], v2 offset:3072\n"
    "ds_read_b128 v[100:103], v2 offset:4096\n"
    "ds_read_b128 v[104:107], v2 offset:5120\n"
    "ds_read_b128 v[108:111], v2 offset:6144\n"
    "ds_read_b128 v[112:115], v2 offset:7168\n"
    "ds_read_b128 v[84:87], v1 offset:0\n"
    "ds_read_b128 v[88:91], v1 offset:4096\n"
    "ds_read_b128 v[92:95], v1 offset:1024\n"
    "ds_read_b128 v[96:99], v1 offset:5120\n"
    "ds_read_b128 v[116:119], v6 offset:0\n"
    "ds_read_b128 v[120:123], v6 offset:1024\n"
    "v_mov_b64 v[124:125], 0\n"
    "v_mov_b64 v[126:127], 0\n"
    "v_mov_b64 v[128:129], 0\n"
    "v_mov_b64 v[130:131], 0\n"
    "v_mov_b64 v[132:133], 0\n"
    "v_mov_b64 v[134:135], 0\n"
    "v_mov_b64 v[136:137], 0\n"
    "v_mov_b64 v[138:139], 0\n"
    "s_waitcnt lgkmcnt(0)\n"
    "R3_wait_%=:\n"
    "s_waitcnt vmcnt(0)\n"
    "s_barrier\n"
    "ds_read_b128 v[28:31], v12 offset:0\n"
    "ds_read_b128 v[32:35], v12 offset:1024\n"
    "ds_read_b128 v[36:39], v12 offset:2048\n"
    "s_waitcnt lgkmcnt(2)\n"
    "v_smfmac_f32_16x16x64_bf16 v[124:127], v[28:31], v[68:75], v7\n"
    "ds_read2st64_b32 v[26:27], v10 offset0:0 offset1:8\n"
    "v_smfmac_f32_16x16x64_bf16 v[128:131], v[28:31], v[76:83], v7\n"
    "ds_read_b128 v[40:43], v12 offset:3072\n"
    "s_waitcnt lgkmcnt(3)\n"
    "v_smfmac_f32_16x16x64_bf16 v[124:127], v[32:35], v[100:107], v7\n"
    "ds_read_b128 v[44:47], v12 offset:4096\n"
    "v_smfmac_f32_16x16x64_bf16 v[128:131], v[32:35], v[108:115], v7\n"
    "ds_read_b128 v[48:51], v12 offset:5120\n"
    "s_waitcnt lgkmcnt(4)\n"
    "v_smfmac_f32_16x16x64_bf16 v[124:127], v[36:39], a[0:7], v7\n"
    "ds_read_b128 v[52:55], v12 offset:6144\n"
    "v_smfmac_f32_16x16x64_bf16 v[128:131], v[36:39], a[8:15], v7\n"
    "ds_read_b128 v[56:59], v12 offset:7168\n"
    "s_waitcnt lgkmcnt(4)\n"
    "v_smfmac_f32_16x16x64_bf16 v[124:127], v[40:43], a[16:23], v7\n"
    "ds_read_b128 v[60:63], v12 offset:8192\n"
    "v_smfmac_f32_16x16x64_bf16 v[128:131], v[40:43], a[24:31], v7\n"
    "ds_read_b128 v[64:67], v12 offset:9216\n"
    "s_waitcnt lgkmcnt(5)\n"
    "v_smfmac_f32_16x16x64_bf16 v[124:127], v[44:47], a[32:39], v7\n"
    "v_max_u32 v26, v26, v27\n"
    "v_smfmac_f32_16x16x64_bf16 v[128:131], v[44:47], a[40:47], v7\n"
    "v_cmp_eq_u32 vcc, -1, v26\n"
    "s_waitcnt lgkmcnt(4)\n"
    "v_smfmac_f32_16x16x64_bf16 v[124:127], v[48:51], a[48:55], v7\n"
    "v_smfmac_f32_16x16x64_bf16 v[128:131], v[48:51], a[56:63], v7\n"
    "s_cbranch_vccnz R3_miss_%=\n"
    "R3_missok_%=:\n"
    "s_waitcnt lgkmcnt(3)\n"
    "v_smfmac_f32_16x16x64_bf16 v[124:127], v[52:55], a[64:71], v7\n"
    "ds_read_b128 v[100:103], v1 offset:2048\n"
    "v_smfmac_f32_16x16x64_bf16 v[128:131], v[52:55], a[72:79], v7\n"
    "s_waitcnt lgkmcnt(3)\n"
    "v_smfmac_f32_16x16x64_bf16 v[124:127], v[56:59], a[80:87], v7\n"
    "ds_read_b128 v[104:107], v1 offset:6144\n"
    "v_smfmac_f32_16x16x64_bf16 v[128:131], v[56:59], a[88:95], v7\n"
    "s_waitcnt lgkmcnt(3)\n"
    "v_smfmac_f32_16x16x64_bf16 v[124:127], v[60:63], a[96:103], v7\n"
    "ds_read_b128 v[108:111], v1 offset:3072\n"
    "v_smfmac_f32_16x16x64_bf16 v[128:131], v[60:63], a[104:111], v7\n"
    "s_waitcnt lgkmcnt(3)\n"
    "v_smfmac_f32_16x16x64_bf16 v[124:127], v[64:67], a[112:119], v7\n"
    "ds_read_b128 v[112:115], v1 offset:7168\n"
    "v_smfmac_f32_16x16x64_bf16 v[128:131], v[64:67], a[120:127], v7\n"
    "v_smfmac_f32_16x16x64_bf16 v[132:135], v[28:31], a[128:135], v7\n"
    "v_smfmac_f32_16x16x64_bf16 v[136:139], v[28:31], a[136:143], v7\n"
    "v_add_f32 v124, v124, v126\n"
    "v_smfmac_f32_16x16x64_bf16 v[132:135], v[32:35], a[144:151], v7\n"
    "v_add_f32 v125, v125, v127\n"
    "v_smfmac_f32_16x16x64_bf16 v[136:139], v[32:35], a[152:159], v7\n"
    "v_cvt_pk_bf16_f32 v124, v124, v125\n"
    "v_smfmac_f32_16x16x64_bf16 v[132:135], v[36:39], a[160:167], v7\n"
    "v_add_f32 v128, v128, v130\n"
    "v_smfmac_f32_16x16x64_bf16 v[136:139], v[36:39], a[168:175], v7\n"
    "v_add_f32 v129, v129, v131\n"
    "v_smfmac_f32_16x16x64_bf16 v[132:135], v[40:43], a[176:183], v7\n"
    "v_cvt_pk_bf16_f32 v128, v128, v129\n"
    "v_smfmac_f32_16x16x64_bf16 v[136:139], v[40:43], a[184:191], v7\n"
    "ds_write2_b32 v11, v124, v128 offset0:0 offset1:64\n"
    "v_smfmac_f32_16x16x64_bf16 v[132:135], v[44:47], a[192:199], v7\n"
    "v_smfmac_f32_16x16x64_bf16 v[136:139], v[44:47], a[200:207], v7\n"
    "v_smfmac_f32_16x16x64_bf16 v[132:135], v[48:51], a[208:215], v7\n"
    "v_smfmac_f32_16x16x64_bf16 v[136:139], v[48:51], a[216:223], v7\n"
    "v_smfmac_f32_16x16x64_bf16 v[132:135], v[52:55], a[224:231], v7\n"
    "s_waitcnt lgkmcnt(0)\n"
    "s_barrier\n"
    "ds_read_b64_tr_b16 v[68:69], v4 offset:0\n"
    "v_smfmac_f32_16x16x64_bf16 v[136:139], v[52:55], a[232:239], v7\n"
    "buffer_load_dword v22, v9, s[36:39], s67 offen offset:0 sc1\n"
    "v_smfmac_f32_16x16x64_bf16 v[132:135], v[56:59], a[240:247], v7\n"
    "ds_read_b64_tr_b16 v[70:71], v4 offset:256\n"
    "v_smfmac_f32_16x16x64_bf16 v[132:135], v[60:63], v[144:151], v7\n"
    "buffer_load_dword v24, v9, s[36:39], s67 offen offset:256 sc1\n"
    "v_smfmac_f32_16x16x64_bf16 v[132:135], v[64:67], v[160:167], v7\n"
    "ds_read_b64_tr_b16 v[72:73], v4 offset:512\n"
    "v_smfmac_f32_16x16x64_bf16 v[136:139], v[56:59], a[248:255], v7\n"
    "ds_read_b64_tr_b16 v[74:75], v4 offset:768\n"
    "v_smfmac_f32_16x16x64_bf16 v[136:139], v[60:63], v[152:159], v7\n"
    "ds_read_b64_tr_b16 v[76:77], v4 offset:1024\n"
    "v_smfmac_f32_16x16x64_bf16 v[136:139], v[64:67], v[168:175], v7\n"
    "ds_read_b64_tr_b16 v[78:79], v4 offset:1280\n"
    "s_waitcnt lgkmcnt(4)\n"
    "v_mfma_f32_16x16x32_bf16 v[124:127], v[84:87], v[68:71], 0\n"
    "v_add_f32 v132, v132, v134\n"
    "v_add_f32 v133, v133, v135\n"
    "v_mfma_f32_16x16x32_bf16 v[128:131], v[88:91], v[68:71], 0\n"
    "v_cvt_pk_bf16_f32 v132, v132, v133\n"
    "ds_read_b64_tr_b16 v[80:81], v4 offset:1536\n"
    "v_add_f32 v136, v136, v138\n"
    "v_add_f32 v137, v137, v139\n"
    "v_cvt_pk_bf16_f32 v136, v136, v137\n"
    "ds_write2_b32 v3, v132, v136 offset0:0 offset1:32\n"
    "s_waitcnt lgkmcnt(4)\n"
    "v_mfma_f32_16x16x32_bf16 v[124:127], v[92:95], v[72:75], v[124:127]\n"
    "ds_read_b64_tr_b16 v[82:83], v4 offset:1792\n"
    "v_mfma_f32_16x16x32_bf16 v[128:131], v[96:99], v[72:75], v[128:131]\n"
    "v_cmp_u_f32 vcc, v68, v68\n"
    "s_waitcnt lgkmcnt(3)\n"
    "v_mfma_f32_16x16x32_bf16 v[124:127], v[100:103], v[76:79], v[124:127]\n"
    "v_mfma_f32_16x16x32_bf16 v[128:131], v[104:107], v[76:79], v[128:131]\n"
    "s_cbranch_vccnz R3_nan_%=\n"
    "R3_nanok_%=:\n"
    "s_waitcnt lgkmcnt(1)\n"
    "s_waitcnt lgkmcnt(0)\n"
    "v_mfma_f32_16x16x32_bf16 v[124:127], v[108:111], v[80:83], v[124:127]\n"
    "s_barrier\n"
    "ds_read_b64_tr_b16 v[52:53], v13 offset:0\n"
    "ds_read_b64_tr_b16 v[54:55], v13 offset:128\n"
    "v_mfma_f32_16x16x32_bf16 v[128:131], v[112:115], v[80:83], v[128:131]\n"
    "ds_read_b64_tr_b16 v[56:57], v13 offset:256\n"
    "ds_read_b64_tr_b16 v[58:59], v13 offset:384\n"
    "v_mov_b32 v15, v15\n"
    "v_mov_b32 v15, v15\n"
    "v_mov_b32 v15, v15\n"
    "v_mov_b32 v15, v15\n"
    "v_mov_b32 v15, v15\n"
    "v_mov_b32 v15, v15\n"
    "v_mov_b32 v15, v15\n"
    "s_waitcnt lgkmcnt(2)\n"
    "v_mfma_f32_16x16x32_bf16 v[140:143], v[52:55], v[68:71], 0\n"
    "v_mfma_f32_16x16x32_bf16 v[60:63], v[52:55], v[72:75], 0\n"
    "v_max3_f32 v138, v124, v125, v126\n"
    "v_max3_f32 v139, v128, v129, v130\n"
    "s_waitcnt lgkmcnt(0)\n"
    "v_mfma_f32_16x16x32_bf16 v[140:143], v[56:59], v[76:79], v[140:143]\n"
    "v_max3_f32 v138, v138, v127, v131\n"
    "v_max_f32 v138, v138, v139\n"
    "v_mfma_f32_16x16x32_bf16 v[60:63], v[56:59], v[80:83], v[60:63]\n"
    "v_mov_b32 v15, v15\n"
    "v_mov_b32 v15, v15\n"
    "v_mov_b32 v15, v15\n"
    "v_mov_b32 v15, v15\n"
    "v_mov_b32 v15, v15\n"
    "v_mov_b32 v15, v15\n"
    "v_mov_b32 v15, v15\n"
    "v_mov_b32 v15, v15\n"
    "v_add_f32 v140, v140, v62\n"
    "v_add_f32 v141, v141, v63\n"
    "v_max3_f32 v134, v138, v140, v141\n"
    "v_max3_f32 v138, v138, v140, v141\n"
    "s_nop 1\n"
    "v_permlane16_swap_b32 v138, v134\n"
    "v_max_f32 v139, v138, v134\n"
    "v_max_f32 v138, v138, v134\n"
    "s_nop 1\n"
    "v_permlane32_swap_b32 v138, v139\n"
    "v_max_f32 v138, v138, v139\n"
    "v_mul_f32 v138, 0xbe0293ee, v138\n"
    "v_pk_fma_f32 v[124:125], v[124:125], s[72:73], v[138:139] op_sel_hi:[1,0,0]\n"
    "v_pk_fma_f32 v[126:127], v[126:127], s[72:73], v[138:139] op_sel_hi:[1,0,0]\n"
    "v_pk_fma_f32 v[128:129], v[128:129], s[72:73], v[138:139] op_sel_hi:[1,0,0]\n"
    "v_pk_fma_f32 v[130:131], v[130:131], s[72:73], v[138:139] op_sel_hi:[1,0,0]\n"
    "v_pk_fma_f32 v[140:141], v[140:141], s[72:73], v[138:139] op_sel_hi:[1,0,0]\n"
    "v_exp_f32 v124, v124\n"
    "v_exp_f32 v125, v125\n"
    "v_exp_f32 v126, v126\n"
    "v_exp_f32 v127, v127\n"
    "v_exp_f32 v128, v128\n"
    "v_exp_f32 v129, v129\n"
    "v_exp_f32 v130, v130\n"
    "v_exp_f32 v131, v131\n"
    "v_cvt_pk_bf16_f32 v112, v124, v125\n"
    "v_cvt_pk_bf16_f32 v113, v126, v127\n"
    "v_cvt_pk_bf16_f32 v114, v128, v129\n"
    "v_cvt_pk_bf16_f32 v115, v130, v131\n"
    "v_exp_f32 v140, v140\n"
    "v_mfma_f32_16x16x32_bf16 v[108:111], v[16:19], v[112:115], 0\n"
    "v_exp_f32 v141, v141\n"
    "v_mfma_f32_16x16x32_bf16 v[100:103], v[116:119], v[112:115], 0\n"
    "v_cvt_pk_bf16_f32 v20, v140, v141\n"
    "s_waitcnt vmcnt(0)\n"
    "v_max_u32 v26, v22, v24\n"
    "v_mfma_f32_16x16x32_bf16 v[104:107], v[120:123], v[112:115], 0\n"
    "v_cmp_eq_u32 vcc, -1, v26\n"
    "v_mov_b32 v15, v15\n"
    "v_mfma_f32_16x16x16_bf16 v[108:111], v[16:17], v[20:21], v[108:111]\n"
    "v_mov_b32 v15, v15\n"
    "s_cbranch_vccnz R3_vslow_%=\n"
    "R3_vok_%=:\n"
    "v_mfma_f32_16x16x16_bf16 v[100:103], v[22:23], v[20:21], v[100:103]\n"
    "v_mfma_f32_16x16x16_bf16 v[104:107], v[24:25], v[20:21], v[104:107]\n"
    "s_nop 1\n"
    "v_rcp_f32 v108, v108\n"
    "s_nop 1\n"
    "v_mul_f32 v100, v100, v108\n"
    "v_mul_f32 v101, v101, v108\n"
    "v_mul_f32 v102, v102, v108\n"
    "v_mul_f32 v103, v103, v108\n"
    "v_cvt_pk_bf16_f32 v100, v100, v101\n"
    "v_cvt_pk_bf16_f32 v101, v102, v103\n"
    "v_pk_mul_f32 v[104:105], v[104:105], v[108:109] op_sel_hi:[1,0]\n"
    "v_pk_mul_f32 v[106:107], v[106:107], v[108:109] op_sel_hi:[1,0]\n"
    "ds_write2_b32 v14, v100, v101 offset0:0 offset1:8\n"
    "v_cvt_pk_bf16_f32 v104, v104, v105\n"
    "v_cvt_pk_bf16_f32 v105, v106, v107\n"
    "ds_write2_b32 v14, v104, v105 offset0:64 offset1:72\n"
    "v_mov_b32 v15, v15\n"
    "s_add_u32 s78, s66, 18432\n"
    "buffer_load_dword v142, v0, s[36:39], s78 offen sc1\n"
    "v_mov_b64 v[44:45], 0\n"
    "v_mov_b64 v[46:47], 0\n"
    "v_mov_b32 v15, v15\n"
    "v_mov_b32 v15, v15\n"
    "v_mov_b32 v15, v15\n"
    "v_mfma_f32_32x32x16_bf16 v[28:43], v[44:47], v[44:47], 0\n"
    "s_waitcnt lgkmcnt(0)\n"
    "s_barrier\n"
    "ds_read_b128 v[68:71], v0 offset:53248\n"
    "ds_read_b128 v[72:75], v0 offset:54272\n"
    "v_mfma_f32_32x32x16_bf16 v[124:139], v[44:47], v[44:47], 0\n"
    "s_waitcnt lgkmcnt(1)\n"
    "v_mov_b32 v15, v15\n"
    "v_mov_b32 v15, v15\n"
    "v_mov_b32 v15, v15\n"
    "v_smfmac_f32_16x16x64_bf16 v[28:31], v[68:71], v[176:183], v7\n"
    "v_smfmac_f32_16x16x64_bf16 v[32:35], v[68:71], v[192:199], v7\n"
    "s_waitcnt lgkmcnt(0)\n"
    "v_smfmac_f32_16x16x64_bf16 v[28:31], v[72:75], v[184:191], v7\n"
    "v_smfmac_f32_16x16x64_bf16 v[32:35], v[72:75], v[200:207], v7\n"
    "v_smfmac_f32_16x16x64_bf16 v[36:39], v[68:71], v[208:215], v7\n"
    "v_smfmac_f32_16x16x64_bf16 v[36:39], v[72:75], v[216:223], v7\n"
    "v_add_f32 v28, v28, v30\n"
    "v_smfmac_f32_16x16x64_bf16 v[40:43], v[68:71], v[224:231], v7\n"
    "v_add_f32 v29, v29, v31\n"
    "v_smfmac_f32_16x16x64_bf16 v[40:43], v[72:75], v[232:239], v7\n"
    "v_cvt_pk_bf16_f32 v28, v28, v29\n"
    "v_smfmac_f32_16x16x64_bf16 v[44:47], v[68:71], v[240:247], v7\n"
    "v_add_f32 v32, v32, v34\n"
    "v_smfmac_f32_16x16x64_bf16 v[44:47], v[72:75], v[248:255], v7\n"
    "v_add_f32 v33, v33, v35\n"
    "v_cvt_pk_bf16_f32 v29, v32, v33\n"
    "v_add_f32 v36, v36, v38\n"
    "v_add_f32 v37, v37, v39\n"
    "v_cvt_pk_bf16_f32 v30, v36, v37\n"
    "v_add_f32 v40, v40, v42\n"
    "v_add_f32 v41, v41, v43\n"
    "v_cvt_pk_bf16_f32 v31, v40, v41\n"
    "buffer_store_dwordx4 v[28:31], v8, s[36:39], s65 offen sc1\n"
    "v_add_f32 v44, v44, v46\n"
    "v_add_f32 v45, v45, v47\n"
    "v_cvt_pk_bf16_f32 v44, v44, v45\n"
    "buffer_store_dword v44, v5, s[36:39], s65 offen sc1\n"
    "s_cmp_eq_u32 s64, 62\n"
    "s_cbranch_scc1 L_exit_%=\n"
    "v_xor_b32 v1, 0x4000, v1\n"
    "v_xor_b32 v6, 0x4000, v6\n"
    "ds_read_b128 v[68:71], v2 offset:0\n"
    "ds_read_b128 v[72:75], v2 offset:1024\n"
    "ds_read_b128 v[76:79], v2 offset:2048\n"
    "ds_read_b128 v[80:83], v2 offset:3072\n"
    "ds_read_b128 v[100:103], v2 offset:4096\n"
    "ds_read_b128 v[104:107], v2 offset:5120\n"
    "ds_read_b128 v[108:111], v2 offset:6144\n"
    "ds_read_b128 v[112:115], v2 offset:7168\n"
    "ds_read_b128 v[84:87], v1 offset:0\n"
    "ds_read_b128 v[88:91], v1 offset:4096\n"
    "ds_read_b128 v[92:95], v1 offset:1024\n"
    "ds_read_b128 v[96:99], v1 offset:5120\n"
    "ds_read_b128 v[116:119], v6 offset:0\n"
    "ds_read_b128 v[120:123], v6 offset:1024\n"
    "v_mov_b64 v[124:125], 0\n"
    "v_mov_b64 v[126:127], 0\n"
    "v_mov_b64 v[128:129], 0\n"
    "v_mov_b64 v[130:131], 0\n"
    "v_mov_b64 v[132:133], 0\n"
    "v_mov_b64 v[134:135], 0\n"
    "v_mov_b64 v[136:137], 0\n"
    "v_mov_b64 v[138:139], 0\n"
    "s_waitcnt lgkmcnt(0)\n"
    "s_mov_b32 s79, 0\n"
    "s_add_u32 s78, s66, 18432\n"
    "s_waitcnt vmcnt(2)\n"
    "s_branch R3_tabc_%=\n"
    "R3_tabl_%=:\n"
    "buffer_load_dword v142, v0, s[36:39], s78 offen sc1\n"
    "s_waitcnt vmcnt(0)\n"
    "R3_tabc_%=:\n"
    "v_cmp_eq_u32 vcc, -1, v142\n"
    "s_cbranch_vccz R3_tabok_%=\n"
    "s_add_u32 s89, s89, 1\n"
    "s_sleep 1\n"
    "s_cmp_lg_u32 s69, 0\n"
    "s_cbranch_scc1 R3_tabok_%=\n"
    "s_add_u32 s79, s79, 1\n"
    "s_cmp_lt_u32 s79, 0x40000\n"
    "s_cbranch_scc1 R3_tabl_%=\n"
    "s_mov_b32 s69, 1\n"
    "s_branch R3_tabok_%=\n"
    "R3_tabok_%=:\n"
    "v_subrev_u32 v142, s82, v142\n"
    "s_nop 1\n"
    "v_max_i32_dpp v142, v142, v142 row_shr:1 row_mask:0xf bank_mask:0xf\n"
    "s_nop 1\n"
    "v_max_i32_dpp v142, v142, v142 row_shr:2 row_mask:0xf bank_mask:0xf\n"
    "s_nop 1\n"
    "v_max_i32_dpp v142, v142, v142 row_shr:4 row_mask:0xf bank_mask:0xf\n"
    "s_nop 1\n"
    "v_max_i32_dpp v142, v142, v142 row_shr:8 row_mask:0xf bank_mask:0xf\n"
    "s_nop 1\n"
    "v_max_i32_dpp v142, v142, v142 row_bcast:15 row_mask:0xa bank_mask:0xf\n"
    "s_nop 1\n"
    "v_max_i32_dpp v142, v142, v142 row_bcast:31 row_mask:0xc bank_mask:0xf\n"
    "s_nop 0\n"
    "v_readlane_b32 s86, v142, 63\n"
    "s_nop 1\n"
    "s_add_u32 s86, s86, s82\n"
    "s_add_u32 s86, s86, s58\n"
    "s_add_u32 s86, s86, s59\n"
    "s_memrealtime s[76:77]\n"
    "s_waitcnt lgkmcnt(0)\n"
    "R3_calb_%=:\n"
    "s_memrealtime s[78:79]\n"
    "s_memtime s[80:81]\n"
    "s_waitcnt lgkmcnt(0)\n"
    "s_cmp_lg_u32 s78, s76\n"
    "s_cbranch_scc1 R3_caldb_%=\n"
    "s_memrealtime s[78:79]\n"
    "s_memtime s[80:81]\n"
    "s_waitcnt lgkmcnt(0)\n"
    "s_cmp_lg_u32 s78, s76\n"
    "s_cbranch_scc1 R3_caldb_%=\n"
    "s_memrealtime s[78:79]\n"
    "s_memtime s[80:81]\n"
    "s_waitcnt lgkmcnt(0)\n"
    "s_cmp_lg_u32 s78, s76\n"
    "s_cbranch_scc1 R3_caldb_%=\n"
    "s_memrealtime s[78:79]\n"
    "s_memtime s[80:81]\n"
    "s_waitcnt lgkmcnt(0)\n"
    "s_cmp_lg_u32 s78, s76\n"
    "s_cbranch_scc1 R3_caldb_%=\n"
    "s_branch R3_calb_%=\n"
    "R3_caldb_%=:\n"
    "s_lshl_b32 s78, s78, 5\n"
    "s_sub_u32 s76, s80, s83\n"
    "s_mul_i32 s76, s76, s55\n"
    "s_lshr_b32 s76, s76, 11\n"
    "s_sub_u32 s77, s78, s82\n"
    "s_sub_u32 s76, s77, s76\n"
    "s_ashr_i32 s76, s76, 3\n"
    "s_max_i32 s76, s76, -64\n"
    "s_min_i32 s76, s76, 64\n"
    "s_add_u32 s55, s55, s76\n"
    "s_mov_b32 s82, s78\n"
    "s_mov_b32 s83, s80\n"
    "s_sub_u32 s84, s86, s82\n"
    "s_mul_i32 s84, s84, 3\n"
    "s_ashr_i32 s84, s84, 2\n"
    "s_min_i32 s84, s84, 0x4000\n"
    "s_add_u32 s84, s84, s83\n"
    "s_add_u32 s80, s66, s46\n"
    "s_add_u32 s81, s80, 4096\n"
    "s_mov_b32 m0, 32768\n"
    "R3_wcb_%=:\n"
    "s_memtime s[76:77]\n"
    "s_getpc_b64 s[78:79]\n"
    "R3_wpb_%=:\n"
    "s_add_u32 s78, s78, R3_web_%=-R3_wpb_%=\n"
    "s_addc_u32 s79, s79, 0\n"
    "s_waitcnt lgkmcnt(0)\n"
    "s_sub_u32 s76, s84, s76\n"
    "s_max_i32 s76, s76, 0\n"
    "s_lshr_b32 s76, s76, 3\n"
    "s_cmp_gt_u32 s76, 40\n"
    "s_cbranch_scc1 R3_wcb_%=\n"
    "s_lshl_b32 s76, s76, 2\n"
    "s_sub_u32 s78, s78, s76\n"
    "s_subb_u32 s79, s79, 0\n"
    "s_setpc_b64 s[78:79]\n"
    ".rept 40\n"
    "s_nop 1\n"
    ".endr\n"
    "R3_web_%=:\n"
    "buffer_load_dwordx4 v0, s[36:39], s80 offen offset:0 sc1 lds\n"
    "buffer_load_dwordx4 v0, s[36:39], s80 offen offset:1024 sc1 lds\n"
    "buffer_load_dwordx4 v0, s[36:39], s80 offen offset:2048 sc1 lds\n"
    "buffer_load_dwordx4 v0, s[36:39], s80 offen offset:3072 sc1 lds\n"
    "s_mov_b32 m0, 36864\n"
    "s_nop 0\n"
    "buffer_load_dwordx4 v0, s[36:39], s81 offen offset:0 sc1 lds\n"
    "buffer_load_dwordx4 v0, s[36:39], s81 offen offset:1024 sc1 lds\n"
    "buffer_load_dwordx4 v0, s[36:39], s81 offen offset:2048 sc1 lds\n"
    "buffer_load_dwordx4 v0, s[36:39], s81 offen offset:3072 sc1 lds\n"
    "s_mov_b32 m0, 40960\n"
    "s_add_u32 s80, s80, 16384\n"
    "buffer_load_dwordx4 v0, s[36:39], s80 offen offset:0 sc1 lds\n"
    "buffer_load_dwordx4 v0, s[36:39], s80 offen offset:1024 sc1 lds\n"
    "s_add_u32 s80, s66, s42\n"
    "s_add_u32 s64, s64, 1\n"
    "s_and_b32 s76, s64, 1\n"
    "s_mul_i32 s76, s76, 0xa0000\n"
    "s_add_u32 s65, s40, s76\n"
    "s_xor_b32 s76, s76, 0xa0000\n"
    "s_add_u32 s71, s40, s76\n"
    "s_and_b32 s76, s64, 3\n"
    "s_lshl_b32 s76, s76, 17\n"
    "s_or_b32 s76, s76, 0x400000\n"
    "s_add_u32 s66, s40, s76\n"
    "s_xor_b32 s76, s76, 0x40000\n"
    "s_add_u32 s68, s40, s76\n"
    "s_add_u32 s67, s66, s60\n"
    "s_mov_b32 s70, 0\n"
    "s_branch R3_wait_%=\n"
    "R3_nan_%=:\n"
    "s_cmp_lg_u32 s70, 0\n"
    "s_cbranch_scc1 R3_nanok_%=\n"
    "s_branch R3_redo_%=\n"
    "R3_miss_%=:\n"
    "s_cmp_lg_u32 s70, 0\n"
    "s_cbranch_scc1 R3_missok_%=\n"
    "R3_redo_%=:\n"
    "buffer_load_dwordx4 v[84:87], v0, s[36:39], s80 offen offset:0 sc1\n"
    "buffer_load_dwordx4 v[88:91], v0, s[36:39], s80 offen offset:1024 sc1\n"
    "buffer_load_dwordx4 v[92:95], v0, s[36:39], s80 offen offset:2048 sc1\n"
    "s_add_u32 s88, s88, 1\n"
    "s_mov_b32 s79, 0\n"
    "s_mov_b32 s70, 400\n"
    "s_nop 7\n"
    "s_waitcnt lgkmcnt(0)\n"
    "s_barrier\n"
    "v_add_u32 v28, s43, v0\n"
    "s_cmp_eq_u32 s64, 0\n"
    "s_cbranch_scc1 R3_rdone_%=\n"
    "R3_rl_%=:\n"
    "s_waitcnt vmcnt(0)\n"
    "v_max3_u32 v29, v84, v85, v86\n"
    "v_max3_u32 v29, v29, v87, v88\n"
    "v_max3_u32 v29, v29, v89, v90\n"
    "v_max3_u32 v29, v29, v91, v92\n"
    "v_max3_u32 v29, v29, v93, v94\n"
    "v_max_u32 v29, v29, v95\n"
    "v_cmp_eq_u32 vcc, -1, v29\n"
    "s_cbranch_vccz R3_rw_%=\n"
    "buffer_load_dwordx4 v[84:87], v0, s[36:39], s80 offen offset:0 sc1\n"
    "buffer_load_dwordx4 v[88:91], v0, s[36:39], s80 offen offset:1024 sc1\n"
    "buffer_load_dwordx4 v[92:95], v0, s[36:39], s80 offen offset:2048 sc1\n"
    "s_cmp_lg_u32 s69, 0\n"
    "s_cbranch_scc1 R3_rdone_%=\n"
    "s_add_u32 s79, s79, 1\n"
    "s_cmp_lt_u32 s79, 0x40000\n"
    "s_cbranch_scc1 R3_rl_%=\n"
    "s_mov_b32 s69, 1\n"
    "s_branch R3_rdone_%=\n"
    "R3_rw_%=:\n"
    "ds_write_b128 v28, v[84:87] offset:0\n"
    "ds_write_b128 v28, v[88:91] offset:1024\n"
    "ds_write_b128 v28, v[92:95] offset:2048\n"
    "R3_rdone_%=:\n"
    "s_waitcnt vmcnt(0) lgkmcnt(0)\n"
    "ds_read_b128 v[68:71], v2 offset:0\n"
    "ds_read_b128 v[72:75], v2 offset:1024\n"
    "ds_read_b128 v[76:79], v2 offset:2048\n"
    "ds_read_b128 v[80:83], v2 offset:3072\n"
    "ds_read_b128 v[100:103], v2 offset:4096\n"
    "ds_read_b128 v[104:107], v2 offset:5120\n"
    "ds_read_b128 v[108:111], v2 offset:6144\n"
    "ds_read_b128 v[112:115], v2 offset:7168\n"
    "ds_read_b128 v[84:87], v1 offset:0\n"
    "ds_read_b128 v[88:91], v1 offset:4096\n"
    "ds_read_b128 v[92:95], v1 offset:1024\n"
    "ds_read_b128 v[96:99], v1 offset:5120\n"
    "ds_read_b128 v[116:119], v6 offset:0\n"
    "ds_read_b128 v[120:123], v6 offset:1024\n"
    "v_mov_b64 v[124:125], 0\n"
    "v_mov_b64 v[126:127], 0\n"
    "v_mov_b64 v[128:129], 0\n"
    "v_mov_b64 v[130:131], 0\n"
    "v_mov_b64 v[132:133], 0\n"
    "v_mov_b64 v[134:135], 0\n"
    "v_mov_b64 v[136:137], 0\n"
    "v_mov_b64 v[138:139], 0\n"
    "s_waitcnt lgkmcnt(0)\n"
    "s_branch R3_wait_%=\n"
    "R3_vslow_%=:\n"
    "s_mov_b32 s79, 0\n"
    "R3_vl_%=:\n"
    "s_add_u32 s87, s87, 1\n"
    "buffer_load_dword v22, v9, s[36:39], s67 offen offset:0 sc1\n"
    "buffer_load_dword v24, v9, s[36:39], s67 offen offset:256 sc1\n"
    "s_waitcnt vmcnt(0)\n"
    "v_max_u32 v26, v22, v24\n"
    "v_cmp_eq_u32 vcc, -1, v26\n"
    "s_cbranch_vccz R3_vok_%=\n"
    "s_cmp_lg_u32 s69, 0\n"
    "s_cbranch_scc1 R3_vok_%=\n"
    "s_add_u32 s79, s79, 1\n"
    "s_cmp_lt_u32 s79, 0x40000\n"
    "s_cbranch_scc1 R3_vl_%=\n"
    "s_mov_b32 s69, 1\n"
    "s_branch R3_vok_%=\n"
    "L_exit_%=:\n"
    "s_waitcnt vmcnt(0) lgkmcnt(0)\n"
    "s_endpgm\n"
                :: "{v[0:15]}"(C), "{v[16:23]}"(P), "{s[36:51]}"(SA), "{s[52:67]}"(SB), "{s[68:83]}"(SC), "{s[84:87]}"(rm) : CLOB_R);
            __builtin_unreachable();
        }
    }
    const int xi = xcd - 4;
    if (slot >= 24) {
        { ROLE_BEGIN row_prep(p, xi * 8 + (slot - 24), w, lane_id()); }
        ROLE_BEGIN
        const int l = lane_id(), c = l & 15, g = l >> 4;
        const int h = xi * 8 + (slot - 24), qt = w & 1, eh = w >> 1;
        v16u C = {}, B = {}, SA = {}, SB = {};
        const unsigned stg = w * 36864;
        strip_consts(C, l, stg, 8192, false);
        C[4] = (w == 0 && l == 0) ? comm_off(p.ws) + NB_FO + h * 128 : OOB;
        C[5] = FL_KV + h * 512 + (l < 3 ? l : 0) * 128; C[6] = FL_M + (2 * h + (l & 1)) * 128;
        C[7] = 106496 + g * 128 + (c & 7) * 16; C[8] = 106496 + 2048 + eh * 1024 + c * 16 + 8 * (g & 1);
        C[9] = 81920 + w * 1024 + l * 16; C[10] = 81920 + qt * 4096 + l * 16;
        const unsigned NEG = 0xf149f2cau;
#pragma unroll
        for (int j = 0; j < 4; ++j) { const bool dg = 4 * g + j <= c; B[j] = (qt == 0 && !dg) ? NEG : 0u; B[4 + j] = (qt == 0 || !dg) ? NEG : 0u; B[8 + j] = g >= 2 ? NEG : 0u; }
        put_srd(SA, 0, p.ws, 0, WS_BYTES);
        SA[4] = stg; SA[5] = WS_XNF + w * 10240; SA[6] = (w < 2 ? WS_KF : WS_VF) + h * 8192 + (w & 1) * 4096; SA[7] = eh * 4096;
        SA[9] = xi < 3 ? NHC : 99; SA[8] = (w < 2 ? WS_KM : WS_VM) + h * 2048 + (w & 1) * 1024; SA[10] = comm_off(p.ws) + NB_OF + qt * 262144 + spread((4 * h + 2 * eh) * 1024); SA[11] = w * 10240; SA[12] = w * 4096;
        put_srd(SB, 0, p.Wq, (h * 128 + 32 * w) * 2, D * HD * 2);
        uni(SA); uni(SB);
        asm volatile(
    "s_add_u32 m0, s40, 0\n"
    "s_mov_b32 s72, 0\n"
    "buffer_load_dwordx4 v1, s[52:55], s72 offen lds\n"
    "s_add_u32 m0, s40, 1024\n"
    "s_mov_b32 s72, 131072\n"
    "buffer_load_dwordx4 v1, s[52:55], s72 offen lds\n"
    "s_add_u32 m0, s40, 2048\n"
    "s_mov_b32 s72, 262144\n"
    "buffer_load_dwordx4 v1, s[52:55], s72 offen lds\n"
    "s_add_u32 m0, s40, 3072\n"
    "s_mov_b32 s72, 393216\n"
    "buffer_load_dwordx4 v1, s[52:55], s72 offen lds\n"
    "s_add_u32 m0, s40, 4096\n"
    "s_mov_b32 s72, 524288\n"
    "buffer_load_dwordx4 v1, s[52:55], s72 offen lds\n"
    "s_add_u32 m0, s40, 5120\n"
    "s_mov_b32 s72, 655360\n"
    "buffer_load_dwordx4 v1, s[52:55], s72 offen lds\n"
    "s_add_u32 m0, s40, 6144\n"
    "s_mov_b32 s72, 786432\n"
    "buffer_load_dwordx4 v1, s[52:55], s72 offen lds\n"
    "s_add_u32 m0, s40, 7168\n"
    "s_mov_b32 s72, 917504\n"
    "buffer_load_dwordx4 v1, s[52:55], s72 offen lds\n"
    "s_add_u32 m0, s40, 8192\n"
    "s_mov_b32 s72, 1048576\n"
    "buffer_load_dwordx4 v1, s[52:55], s72 offen lds\n"
    "s_add_u32 m0, s40, 9216\n"
    "s_mov_b32 s72, 1179648\n"
    "buffer_load_dwordx4 v1, s[52:55], s72 offen lds\n"
    "s_add_u32 m0, s40, 10240\n"
    "s_mov_b32 s72, 1310720\n"
    "buffer_load_dwordx4 v1, s[52:55], s72 offen lds\n"
    "s_add_u32 m0, s40, 11264\n"
    "s_mov_b32 s72, 1441792\n"
    "buffer_load_dwordx4 v1, s[52:55], s72 offen lds\n"
    "s_add_u32 m0, s40, 12288\n"
    "s_mov_b32 s72, 1572864\n"
    "buffer_load_dwordx4 v1, s[52:55], s72 offen lds\n"
    "s_add_u32 m0, s40, 13312\n"
    "s_mov_b32 s72, 1703936\n"
    "buffer_load_dwordx4 v1, s[52:55], s72 offen lds\n"
    "s_add_u32 m0, s40, 14336\n"
    "s_mov_b32 s72, 1835008\n"
    "buffer_load_dwordx4 v1, s[52:55], s72 offen lds\n"
    "s_add_u32 m0, s40, 15360\n"
    "s_mov_b32 s72, 1966080\n"
    "buffer_load_dwordx4 v1, s[52:55], s72 offen lds\n"
    "s_add_u32 m0, s40, 16384\n"
    "s_mov_b32 s72, 2097152\n"
    "buffer_load_dwordx4 v1, s[52:55], s72 offen lds\n"
    "s_add_u32 m0, s40, 17408\n"
    "s_mov_b32 s72, 2228224\n"
    "buffer_load_dwordx4 v1, s[52:55], s72 offen lds\n"
    "s_add_u32 m0, s40, 18432\n"
    "s_mov_b32 s72, 2359296\n"
    "buffer_load_dwordx4 v1, s[52:55], s72 offen lds\n"
    "s_add_u32 m0, s40, 19456\n"
    "s_mov_b32 s72, 2490368\n"
    "buffer_load_dwordx4 v1, s[52:55], s72 offen lds\n"
    "s_add_u32 m0, s40, 20480\n"
    "s_mov_b32 s72, 2621440\n"
    "buffer_load_dwordx4 v1, s[52:55], s72 offen lds\n"
    "s_add_u32 m0, s40, 21504\n"
    "s_mov_b32 s72, 2752512\n"
    "buffer_load_dwordx4 v1, s[52:55], s72 offen lds\n"
    "s_add_u32 m0, s40, 22528\n"
    "s_mov_b32 s72, 2883584\n"
    "buffer_load_dwordx4 v1, s[52:55], s72 offen lds\n"
    "s_add_u32 m0, s40, 23552\n"
    "s_mov_b32 s72, 3014656\n"
    "buffer_load_dwordx4 v1, s[52:55], s72 offen lds\n"
    "s_add_u32 m0, s40, 24576\n"
    "s_mov_b32 s72, 3145728\n"
    "buffer_load_dwordx4 v1, s[52:55], s72 offen lds\n"
    "s_add_u32 m0, s40, 25600\n"
    "s_mov_b32 s72, 3276800\n"
    "buffer_load_dwordx4 v1, s[52:55], s72 offen lds\n"
    "s_add_u32 m0, s40, 26624\n"
    "s_mov_b32 s72, 3407872\n"
    "buffer_load_dwordx4 v1, s[52:55], s72 offen lds\n"
    "s_add_u32 m0, s40, 27648\n"
    "s_mov_b32 s72, 3538944\n"
    "buffer_load_dwordx4 v1, s[52:55], s72 offen lds\n"
    "s_add_u32 m0, s40, 28672\n"
    "s_mov_b32 s72, 3670016\n"
    "buffer_load_dwordx4 v1, s[52:55], s72 offen lds\n"
    "s_add_u32 m0, s40, 29696\n"
    "s_mov_b32 s72, 3801088\n"
    "buffer_load_dwordx4 v1, s[52:55], s72 offen lds\n"
    "s_add_u32 m0, s40, 30720\n"
    "s_mov_b32 s72, 3932160\n"
    "buffer_load_dwordx4 v1, s[52:55], s72 offen lds\n"
    "s_add_u32 m0, s40, 31744\n"
    "s_mov_b32 s72, 4063232\n"
    "buffer_load_dwordx4 v1, s[52:55], s72 offen lds\n"
    "s_add_u32 m0, s40, 32768\n"
    "s_mov_b32 s72, 4194304\n"
    "buffer_load_dwordx4 v1, s[52:55], s72 offen lds\n"
    "s_add_u32 m0, s40, 33792\n"
    "s_mov_b32 s72, 4325376\n"
    "buffer_load_dwordx4 v1, s[52:55], s72 offen lds\n"
    "s_add_u32 m0, s40, 34816\n"
    "s_mov_b32 s72, 4456448\n"
    "buffer_load_dwordx4 v1, s[52:55], s72 offen lds\n"
    "s_add_u32 m0, s40, 35840\n"
    "s_mov_b32 s72, 4587520\n"
    "buffer_load_dwordx4 v1, s[52:55], s72 offen lds\n"
    "s_waitcnt vmcnt(32)\n"
    "ds_read_b64_tr_b16 a[0:1], v2 offset:0\n"
    "ds_read_b64_tr_b16 a[2:3], v2 offset:256\n"
    "ds_read_b64_tr_b16 a[4:5], v2 offset:2048\n"
    "ds_read_b64_tr_b16 a[6:7], v2 offset:2304\n"
    "ds_read_b64_tr_b16 a[8:9], v3 offset:0\n"
    "ds_read_b64_tr_b16 a[10:11], v3 offset:256\n"
    "ds_read_b64_tr_b16 a[12:13], v3 offset:2048\n"
    "ds_read_b64_tr_b16 a[14:15], v3 offset:2304\n"
    "s_waitcnt lgkmcnt(0)\n"
    "s_add_u32 m0, s40, 0\n"
    "s_mov_b32 s72, 4718592\n"
    "buffer_load_dwordx4 v1, s[52:55], s72 offen lds\n"
    "s_add_u32 m0, s40, 1024\n"
    "s_mov_b32 s72, 4849664\n"
    "buffer_load_dwordx4 v1, s[52:55], s72 offen lds\n"
    "s_add_u32 m0, s40, 2048\n"
    "s_mov_b32 s72, 4980736\n"
    "buffer_load_dwordx4 v1, s[52:55], s72 offen lds\n"
    "s_add_u32 m0, s40, 3072\n"
    "s_mov_b32 s72, 5111808\n"
    "buffer_load_dwordx4 v1, s[52:55], s72 offen lds\n"
    "s_waitcnt vmcnt(32)\n"
    "ds_read_b64_tr_b16 a[16:17], v2 offset:4096\n"
    "ds_read_b64_tr_b16 a[18:19], v2 offset:4352\n"
    "ds_read_b64_tr_b16 a[20:21], v2 offset:6144\n"
    "ds_read_b64_tr_b16 a[22:23], v2 offset:6400\n"
    "ds_read_b64_tr_b16 a[24:25], v3 offset:4096\n"
    "ds_read_b64_tr_b16 a[26:27], v3 offset:4352\n"
    "ds_read_b64_tr_b16 a[28:29], v3 offset:6144\n"
    "ds_read_b64_tr_b16 a[30:31], v3 offset:6400\n"
    "s_waitcnt vmcnt(28)\n"
    "ds_read_b64_tr_b16 a[32:33], v2 offset:8192\n"
    "ds_read_b64_tr_b16 a[34:35], v2 offset:8448\n"
    "ds_read_b64_tr_b16 a[36:37], v2 offset:10240\n"
    "ds_read_b64_tr_b16 a[38:39], v2 offset:10496\n"
    "ds_read_b64_tr_b16 a[40:41], v3 offset:8192\n"
    "ds_read_b64_tr_b16 a[42:43], v3 offset:8448\n"
    "s_waitcnt lgkmcnt(7)\n"
    "ds_read_b64_tr_b16 a[44:45], v3 offset:10240\n"
    "ds_read_b64_tr_b16 a[46:47], v3 offset:10496\n"
    "s_waitcnt vmcnt(24)\n"
    "ds_read_b64_tr_b16 a[48:49], v2 offset:12288\n"
    "ds_read_b64_tr_b16 a[50:51], v2 offset:12544\n"
    "ds_read_b64_tr_b16 a[52:53], v2 offset:14336\n"
    "ds_read_b64_tr_b16 a[54:55], v2 offset:14592\n"
    "ds_read_b64_tr_b16 a[56:57], v3 offset:12288\n"
    "s_waitcnt lgkmcnt(7)\n"
    "ds_read_b64_tr_b16 a[58:59], v3 offset:12544\n"
    "ds_read_b64_tr_b16 a[60:61], v3 offset:14336\n"
    "ds_read_b64_tr_b16 a[62:63], v3 offset:14592\n"
    "s_waitcnt vmcnt(20)\n"
    "ds_read_b64_tr_b16 a[64:65], v2 offset:16384\n"
    "ds_read_b64_tr_b16 a[66:67], v2 offset:16640\n"
    "ds_read_b64_tr_b16 a[68:69], v2 offset:18432\n"
    "ds_read_b64_tr_b16 a[70:71], v2 offset:18688\n"
    "s_waitcnt lgkmcnt(7)\n"
    "ds_read_b64_tr_b16 a[72:73], v3 offset:16384\n"
    "ds_read_b64_tr_b16 a[74:75], v3 offset:16640\n"
    "ds_read_b64_tr_b16 a[76:77], v3 offset:18432\n"
    "ds_read_b64_tr_b16 a[78:79], v3 offset:18688\n"
    "s_waitcnt vmcnt(16)\n"
    "ds_read_b64_tr_b16 a[80:81], v2 offset:20480\n"
    "ds_read_b64_tr_b16 a[82:83], v2 offset:20736\n"
    "ds_read_b64_tr_b16 a[84:85], v2 offset:22528\n"
    "s_waitcnt lgkmcnt(7)\n"
    "ds_read_b64_tr_b16 a[86:87], v2 offset:22784\n"
    "ds_read_b64_tr_b16 a[88:89], v3 offset:20480\n"
    "ds_read_b64_tr_b16 a[90:91], v3 offset:20736\n"
    "ds_read_b64_tr_b16 a[92:93], v3 offset:22528\n"
    "ds_read_b64_tr_b16 a[94:95], v3 offset:22784\n"
    "s_waitcnt vmcnt(12)\n"
    "ds_read_b64_tr_b16 a[96:97], v2 offset:24576\n"
    "ds_read_b64_tr_b16 a[98:99], v2 offset:24832\n"
    "s_waitcnt lgkmcnt(7)\n"
    "ds_read_b64_tr_b16 a[100:101], v2 offset:26624\n"
    "ds_read_b64_tr_b16 a[102:103], v2 offset:26880\n"
    "ds_read_b64_tr_b16 a[104:105], v3 offset:24576\n"
    "ds_read_b64_tr_b16 a[106:107], v3 offset:24832\n"
    "ds_read_b64_tr_b16 a[108:109], v3 offset:26624\n"
    "ds_read_b64_tr_b16 a[110:111], v3 offset:26880\n"
    "s_waitcnt vmcnt(8)\n"
    "ds_read_b64_tr_b16 a[112:113], v2 offset:28672\n"
    "s_waitcnt lgkmcnt(7)\n"
    "ds_read_b64_tr_b16 a[114:115], v2 offset:28928\n"
    "ds_read_b64_tr_b16 a[116:117], v2 offset:30720\n"
    "ds_read_b64_tr_b16 a[118:119], v2 offset:30976\n"
    "ds_read_b64_tr_b16 a[120:121], v3 offset:28672\n"
    "ds_read_b64_tr_b16 a[122:123], v3 offset:28928\n"
    "ds_read_b64_tr_b16 a[124:125], v3 offset:30720\n"
    "ds_read_b64_tr_b16 a[126:127], v3 offset:30976\n"
    "s_waitcnt vmcnt(4)\n"
    "s_waitcnt lgkmcnt(7)\n"
    "ds_read_b64_tr_b16 a[128:129], v2 offset:32768\n"
    "ds_read_b64_tr_b16 a[130:131], v2 offset:33024\n"
    "ds_read_b64_tr_b16 a[132:133], v2 offset:34816\n"
    "ds_read_b64_tr_b16 a[134:135], v2 offset:35072\n"
    "ds_read_b64_tr_b16 a[136:137], v3 offset:32768\n"
    "ds_read_b64_tr_b16 a[138:139], v3 offset:33024\n"
    "ds_read_b64_tr_b16 a[140:141], v3 offset:34816\n"
    "s_waitcnt lgkmcnt(7)\n"
    "ds_read_b64_tr_b16 a[142:143], v3 offset:35072\n"
    "s_waitcnt vmcnt(0)\n"
    "ds_read_b64_tr_b16 a[144:145], v2 offset:0\n"
    "ds_read_b64_tr_b16 a[146:147], v2 offset:256\n"
    "ds_read_b64_tr_b16 a[148:149], v2 offset:2048\n"
    "ds_read_b64_tr_b16 a[150:151], v2 offset:2304\n"
    "ds_read_b64_tr_b16 a[152:153], v3 offset:0\n"
    "ds_read_b64_tr_b16 a[154:155], v3 offset:256\n"
    "s_waitcnt lgkmcnt(7)\n"
    "ds_read_b64_tr_b16 a[156:157], v3 offset:2048\n"
    "ds_read_b64_tr_b16 a[158:159], v3 offset:2304\n"
    "s_waitcnt lgkmcnt(0)\n"
    "v_mov_b32 v28, 0x3f803f80\n"
    "v_mov_b32 v29, 0x3f803f80\n"
    "v_mov_b32 v30, 0x3f803f80\n"
    "v_mov_b32 v31, 0x3f803f80\n"
    "s_mov_b32 s75, 0\n"
    "s_mov_b32 s71, 0\n"
    "v_add_u32 v210, 90112, v0\n"
    "v_add_u32 v211, s43, v210\n"
    "s_mov_b32 s76, 0\n"
    "s_mov_b32 exec_hi, 1\n"
    "buffer_load_dwordx2 v[32:33], off, s[36:39], 0 sc1\n"
    "RQ_ix_%=:\n"
    "buffer_load_dwordx2 v[34:35], v0, s[36:39], 0 offen offset:1024 sc1\n"
    "s_waitcnt vmcnt(0)\n"
    "v_sub_u32 v34, v34, v32\n"
    "v_sub_u32 v35, v35, v33\n"
    "v_subrev_u32 v34, 0x7f4a7c16, v34\n"
    "v_cmp_ne_u32 vcc, 0x9e3779b9, v35\n"
    "v_cmp_lt_u32 s[94:95], 1, v34\n"
    "s_nop 1\n"
    "s_or_b64 vcc, vcc, s[94:95]\n"
    "s_cbranch_scc0 RQ_jx_%=\n"
    "s_cmp_lg_u32 s75, 0\n"
    "s_cbranch_scc1 RQ_jx_%=\n"
    "s_sleep 4\n"
    "s_add_u32 s76, s76, 1\n"
    "s_cmp_lt_u32 s76, 0x40000\n"
    "s_cbranch_scc1 RQ_ix_%=\n"
    "s_mov_b32 s75, 1\n"
    "RQ_jx_%=:\n"
    "s_mov_b64 exec, -1\n"
    "v_readlane_b32 s72, v34, 32\n"
    "s_nop 1\n"
    "s_and_b32 s72, s72, 1\n"
    "s_xor_b32 s72, s72, 1\n"
    "s_lshl_b32 s72, s72, 13\n"
    "s_add_u32 s46, s46, s72\n"
    "v_add_u32 v4, s72, v4\n"
    "s_barrier\n"
    "s_mov_b32 s69, s47\n"
    "s_add_u32 s70, s47, 40960\n"
    "s_add_u32 m0, s69, 0\n"
    "s_add_u32 s73, s41, 0\n"
    "buffer_load_dwordx4 v0, s[36:39], s73 offen offset:0 sc1 lds\n"
    "buffer_load_dwordx4 v0, s[36:39], s73 offen offset:1024 sc1 lds\n"
    "buffer_load_dwordx4 v0, s[36:39], s73 offen offset:2048 sc1 lds\n"
    "buffer_load_dwordx4 v0, s[36:39], s73 offen offset:3072 sc1 lds\n"
    "s_add_u32 m0, s69, 4096\n"
    "s_add_u32 s73, s41, 4096\n"
    "buffer_load_dwordx4 v0, s[36:39], s73 offen offset:0 sc1 lds\n"
    "buffer_load_dwordx4 v0, s[36:39], s73 offen offset:1024 sc1 lds\n"
    "buffer_load_dwordx4 v0, s[36:39], s73 offen offset:2048 sc1 lds\n"
    "buffer_load_dwordx4 v0, s[36:39], s73 offen offset:3072 sc1 lds\n"
    "s_add_u32 m0, s69, 8192\n"
    "s_add_u32 s73, s41, 8192\n"
    "buffer_load_dwordx4 v0, s[36:39], s73 offen offset:0 sc1 lds\n"
    "buffer_load_dwordx4 v0, s[36:39], s73 offen offset:1024 sc1 lds\n"
    "s_add_u32 s41, s41, 40960\n"
    "s_mov_b32 s68, 0\n"
    "s_add_u32 s74, s68, 0\n"
    "s_cmp_gt_u32 s71, s74\n"
    "s_cbranch_scc1 RQ_kq0_%=\n"
    "s_add_u32 s77, s74, 1\n"
    "s_mul_i32 s72, s74, 43\n"
    "s_lshr_b32 s72, s72, 7\n"
    "s_mul_i32 s72, s72, 3\n"
    "s_sub_u32 s74, s74, s72\n"
    "s_cmp_le_u32 s77, s45\n"
    "s_cselect_b32 s74, s74, 0\n"
    "s_mov_b32 s76, 0\n"
    "RQ_kp0_%=:\n"
    "buffer_load_dword v14, v5, s[36:39], 0 offen sc1\n"
    "s_waitcnt vmcnt(0)\n"
    "v_readlane_b32 s72, v14, s74\n"
    "v_readlane_b32 s71, v14, 0\n"
    "v_readlane_b32 s94, v14, 1\n"
    "v_readlane_b32 s95, v14, 2\n"
    "s_nop 0\n"
    "s_min_u32 s71, s71, s94\n"
    "s_min_u32 s71, s71, s95\n"
    "s_cmp_ge_u32 s72, s77\n"
    "s_cbranch_scc1 RQ_kq0_%=\n"
    "s_cmp_lg_u32 s75, 0\n"
    "s_cbranch_scc1 RQ_kq0_%=\n"
    "s_sleep 2\n"
    "s_add_u32 s76, s76, 1\n"
    "s_cmp_lt_u32 s76, 0x40000\n"
    "s_cbranch_scc1 RQ_kp0_%=\n"
    "s_mov_b32 s75, 1\n"
    "RQ_kq0_%=:\n"
    "s_waitcnt vmcnt(0)\n"
    "s_barrier\n"
    "s_sub_u32 s72, s69, s47\n"
    "v_add_u32 v12, s72, v0\n"
    "ds_read_b128 v[32:35], v12 offset:0\n"
    "ds_read_b128 v[36:39], v12 offset:20480\n"
    "ds_read_b128 v[40:43], v12 offset:1024\n"
    "ds_read_b128 v[44:47], v12 offset:21504\n"
    "ds_read_b128 v[48:51], v12 offset:2048\n"
    "ds_read_b128 v[52:55], v12 offset:22528\n"
    "s_waitcnt lgkmcnt(5)\n"
    "v_mfma_f32_16x16x32_bf16 v[64:67], a[0:3], v[32:35], 0\n"
    "ds_read_b128 v[56:59], v12 offset:3072\n"
    "v_mfma_f32_16x16x32_bf16 v[68:71], a[8:11], v[32:35], 0\n"
    "s_waitcnt lgkmcnt(5)\n"
    "v_mfma_f32_16x16x32_bf16 v[72:75], a[0:3], v[36:39], 0\n"
    "ds_read_b128 v[60:63], v12 offset:23552\n"
    "v_mfma_f32_16x16x32_bf16 v[76:79], a[8:11], v[36:39], 0\n"
    "s_add_u32 m0, s48, 90112\n"
    "s_nop 0\n"
    "buffer_load_dwordx4 v0, s[36:39], s42 offen offset:0 sc1 lds\n"
    "s_waitcnt lgkmcnt(5)\n"
    "v_mfma_f32_16x16x32_bf16 v[64:67], a[4:7], v[40:43], v[64:67]\n"
    "ds_read_b128 v[32:35], v12 offset:4096\n"
    "v_mfma_f32_16x16x32_bf16 v[68:71], a[12:15], v[40:43], v[68:71]\n"
    "s_waitcnt lgkmcnt(5)\n"
    "v_mfma_f32_16x16x32_bf16 v[72:75], a[4:7], v[44:47], v[72:75]\n"
    "ds_read_b128 v[36:39], v12 offset:24576\n"
    "v_mfma_f32_16x16x32_bf16 v[76:79], a[12:15], v[44:47], v[76:79]\n"
    "buffer_load_dwordx4 v0, s[36:39], s42 offen offset:1024 sc1 lds\n"
    "s_waitcnt lgkmcnt(5)\n"
    "v_mfma_f32_16x16x32_bf16 v[64:67], a[16:19], v[48:51], v[64:67]\n"
    "ds_read_b128 v[40:43], v12 offset:5120\n"
    "v_mfma_f32_16x16x32_bf16 v[68:71], a[24:27], v[48:51], v[68:71]\n"
    "s_waitcnt lgkmcnt(5)\n"
    "v_mfma_f32_16x16x32_bf16 v[72:75], a[16:19], v[52:55], v[72:75]\n"
    "ds_read_b128 v[44:47], v12 offset:25600\n"
    "v_mfma_f32_16x16x32_bf16 v[76:79], a[24:27], v[52:55], v[76:79]\n"
    "buffer_load_dwordx4 v0, s[36:39], s42 offen offset:2048 sc1 lds\n"
    "s_waitcnt lgkmcnt(5)\n"
    "v_mfma_f32_16x16x32_bf16 v[64:67], a[20:23], v[56:59], v[64:67]\n"
    "ds_read_b128 v[48:51], v12 offset:6144\n"
    "v_mfma_f32_16x16x32_bf16 v[68:71], a[28:31], v[56:59], v[68:71]\n"
    "s_waitcnt lgkmcnt(5)\n"
    "v_mfma_f32_16x16x32_bf16 v[72:75], a[20:23], v[60:63], v[72:75]\n"
    "ds_read_b128 v[52:55], v12 offset:26624\n"
    "v_mfma_f32_16x16x32_bf16 v[76:79], a[28:31], v[60:63], v[76:79]\n"
    "buffer_load_dwordx4 v0, s[36:39], s42 offen offset:3072 sc1 lds\n"
    "s_waitcnt lgkmcnt(5)\n"
    "v_mfma_f32_16x16x32_bf16 v[64:67], a[32:35], v[32:35], v[64:67]\n"
    "ds_read_b128 v[56:59], v12 offset:7168\n"
    "v_mfma_f32_16x16x32_bf16 v[68:71], a[40:43], v[32:35], v[68:71]\n"
    "s_waitcnt lgkmcnt(5)\n"
    "v_mfma_f32_16x16x32_bf16 v[72:75], a[32:35], v[36:39], v[72:75]\n"
    "ds_read_b128 v[60:63], v12 offset:27648\n"
    "v_mfma_f32_16x16x32_bf16 v[76:79], a[40:43], v[36:39], v[76:79]\n"
    "s_add_u32 m0, s70, 0\n"
    "s_add_u32 s73, s41, 0\n"
    "buffer_load_dwordx4 v0, s[36:39], s73 offen offset:0 sc1 lds\n"
    "s_waitcnt lgkmcnt(5)\n"
    "v_mfma_f32_16x16x32_bf16 v[64:67], a[36:39], v[40:43], v[64:67]\n"
    "ds_read_b128 v[32:35], v12 offset:8192\n"
    "v_mfma_f32_16x16x32_bf16 v[68:71], a[44:47], v[40:43], v[68:71]\n"
    "s_waitcnt lgkmcnt(5)\n"
    "v_mfma_f32_16x16x32_bf16 v[72:75], a[36:39], v[44:47], v[72:75]\n"
    "ds_read_b128 v[36:39], v12 offset:28672\n"
    "v_mfma_f32_16x16x32_bf16 v[76:79], a[44:47], v[44:47], v[76:79]\n"
    "buffer_load_dwordx4 v0, s[36:39], s73 offen offset:1024 sc1 lds\n"
    "s_waitcnt lgkmcnt(5)\n"
    "v_mfma_f32_16x16x32_bf16 v[64:67], a[48:51], v[48:51], v[64:67]\n"
    "ds_read_b128 v[40:43], v12 offset:9216\n"
    "v_mfma_f32_16x16x32_bf16 v[68:71], a[56:59], v[48:51], v[68:71]\n"
    "s_waitcnt lgkmcnt(5)\n"
    "v_mfma_f32_16x16x32_bf16 v[72:75], a[48:51], v[52:55], v[72:75]\n"
    "ds_read_b128 v[44:47], v12 offset:29696\n"
    "v_mfma_f32_16x16x32_bf16 v[76:79], a[56:59], v[52:55], v[76:79]\n"
    "buffer_load_dwordx4 v0, s[36:39], s73 offen offset:2048 sc1 lds\n"
    "s_waitcnt lgkmcnt(5)\n"
    "v_mfma_f32_16x16x32_bf16 v[64:67], a[52:55], v[56:59], v[64:67]\n"
    "ds_read_b128 v[48:51], v12 offset:10240\n"
    "v_mfma_f32_16x16x32_bf16 v[68:71], a[60:63], v[56:59], v[68:71]\n"
    "s_waitcnt lgkmcnt(5)\n"
    "v_mfma_f32_16x16x32_bf16 v[72:75], a[52:55], v[60:63], v[72:75]\n"
    "ds_read_b128 v[52:55], v12 offset:30720\n"
    "v_mfma_f32_16x16x32_bf16 v[76:79], a[60:63], v[60:63], v[76:79]\n"
    "buffer_load_dwordx4 v0, s[36:39], s73 offen offset:3072 sc1 lds\n"
    "s_waitcnt lgkmcnt(5)\n"
    "v_mfma_f32_16x16x32_bf16 v[64:67], a[64:67], v[32:35], v[64:67]\n"
    "ds_read_b128 v[56:59], v12 offset:11264\n"
    "v_mfma_f32_16x16x32_bf16 v[68:71], a[72:75], v[32:35], v[68:71]\n"
    "s_waitcnt lgkmcnt(5)\n"
    "v_mfma_f32_16x16x32_bf16 v[72:75], a[64:67], v[36:39], v[72:75]\n"
    "ds_read_b128 v[60:63], v12 offset:31744\n"
    "v_mfma_f32_16x16x32_bf16 v[76:79], a[72:75], v[36:39], v[76:79]\n"
    "s_add_u32 m0, s70, 4096\n"
    "s_add_u32 s73, s41, 4096\n"
    "buffer_load_dwordx4 v0, s[36:39], s73 offen offset:0 sc1 lds\n"
    "s_waitcnt lgkmcnt(5)\n"
    "v_mfma_f32_16x16x32_bf16 v[64:67], a[68:71], v[40:43], v[64:67]\n"
    "ds_read_b128 v[32:35], v12 offset:12288\n"
    "v_mfma_f32_16x16x32_bf16 v[68:71], a[76:79], v[40:43], v[68:71]\n"
    "s_waitcnt lgkmcnt(5)\n"
    "v_mfma_f32_16x16x32_bf16 v[72:75], a[68:71], v[44:47], v[72:75]\n"
    "ds_read_b128 v[36:39], v12 offset:32768\n"
    "v_mfma_f32_16x16x32_bf16 v[76:79], a[76:79], v[44:47], v[76:79]\n"
    "buffer_load_dwordx4 v0, s[36:39], s73 offen offset:1024 sc1 lds\n"
    "s_waitcnt lgkmcnt(5)\n"
    "v_mfma_f32_16x16x32_bf16 v[64:67], a[80:83], v[48:51], v[64:67]\n"
    "ds_read_b128 v[40:43], v12 offset:13312\n"
    "v_mfma_f32_16x16x32_bf16 v[68:71], a[88:91], v[48:51], v[68:71]\n"
    "s_waitcnt lgkmcnt(5)\n"
    "v_mfma_f32_16x16x32_bf16 v[72:75], a[80:83], v[52:55], v[72:75]\n"
    "ds_read_b128 v[44:47], v12 offset:33792\n"
    "v_mfma_f32_16x16x32_bf16 v[76:79], a[88:91], v[52:55], v[76:79]\n"
    "buffer_load_dwordx4 v0, s[36:39], s73 offen offset:2048 sc1 lds\n"
    "s_waitcnt lgkmcnt(5)\n"
    "v_mfma_f32_16x16x32_bf16 v[64:67], a[84:87], v[56:59], v[64:67]\n"
    "ds_read_b128 v[48:51], v12 offset:14336\n"
    "v_mfma_f32_16x16x32_bf16 v[68:71], a[92:95], v[56:59], v[68:71]\n"
    "s_waitcnt lgkmcnt(5)\n"
    "v_mfma_f32_16x16x32_bf16 v[72:75], a[84:87], v[60:63], v[72:75]\n"
    "ds_read_b128 v[52:55], v12 offset:34816\n"
    "v_mfma_f32_16x16x32_bf16 v[76:79], a[92:95], v[60:63], v[76:79]\n"
    "buffer_load_dwordx4 v0, s[36:39], s73 offen offset:3072 sc1 lds\n"
    "s_waitcnt lgkmcnt(5)\n"
    "v_mfma_f32_16x16x32_bf16 v[64:67], a[96:99], v[32:35], v[64:67]\n"
    "ds_read_b128 v[56:59], v12 offset:15360\n"
    "v_mfma_f32_16x16x32_bf16 v[68:71], a[104:107], v[32:35], v[68:71]\n"
    "s_waitcnt lgkmcnt(5)\n"
    "v_mfma_f32_16x16x32_bf16 v[72:75], a[96:99], v[36:39], v[72:75]\n"
    "ds_read_b128 v[60:63], v12 offset:35840\n"
    "v_mfma_f32_16x16x32_bf16 v[76:79], a[104:107], v[36:39], v[76:79]\n"
    "s_add_u32 m0, s70, 8192\n"
    "s_add_u32 s73, s41, 8192\n"
    "buffer_load_dwordx4 v0, s[36:39], s73 offen offset:0 sc1 lds\n"
    "s_waitcnt lgkmcnt(5)\n"
    "v_mfma_f32_16x16x32_bf16 v[64:67], a[100:103], v[40:43], v[64:67]\n"
    "ds_read_b128 v[32:35], v12 offset:16384\n"
    "v_mfma_f32_16x16x32_bf16 v[68:71], a[108:111], v[40:43], v[68:71]\n"
    "s_waitcnt lgkmcnt(5)\n"
    "v_mfma_f32_16x16x32_bf16 v[72:75], a[100:103], v[44:47], v[72:75]\n"
    "ds_read_b128 v[36:39], v12 offset:36864\n"
    "v_mfma_f32_16x16x32_bf16 v[76:79], a[108:111], v[44:47], v[76:79]\n"
    "buffer_load_dwordx4 v0, s[36:39], s73 offen offset:1024 sc1 lds\n"
    "s_waitcnt lgkmcnt(5)\n"
    "v_mfma_f32_16x16x32_bf16 v[64:67], a[112:115], v[48:51], v[64:67]\n"
    "ds_read_b128 v[40:43], v12 offset:17408\n"
    "v_mfma_f32_16x16x32_bf16 v[68:71], a[120:123], v[48:51], v[68:71]\n"
    "s_waitcnt lgkmcnt(5)\n"
    "v_mfma_f32_16x16x32_bf16 v[72:75], a[112:115], v[52:55], v[72:75]\n"
    "ds_read_b128 v[44:47], v12 offset:37888\n"
    "v_mfma_f32_16x16x32_bf16 v[76:79], a[120:123], v[52:55], v[76:79]\n"
    "s_waitcnt lgkmcnt(5)\n"
    "v_mfma_f32_16x16x32_bf16 v[64:67], a[116:119], v[56:59], v[64:67]\n"
    "ds_read_b128 v[48:51], v12 offset:18432\n"
    "v_mfma_f32_16x16x32_bf16 v[68:71], a[124:127], v[56:59], v[68:71]\n"
    "s_waitcnt lgkmcnt(5)\n"
    "v_mfma_f32_16x16x32_bf16 v[72:75], a[116:119], v[60:63], v[72:75]\n"
    "ds_read_b128 v[52:55], v12 offset:38912\n"
    "v_mfma_f32_16x16x32_bf16 v[76:79], a[124:127], v[60:63], v[76:79]\n"
    "s_waitcnt lgkmcnt(5)\n"
    "v_mfma_f32_16x16x32_bf16 v[64:67], a[128:131], v[32:35], v[64:67]\n"
    "ds_read_b128 v[56:59], v12 offset:19456\n"
    "v_mfma_f32_16x16x32_bf16 v[68:71], a[136:139], v[32:35], v[68:71]\n"
    "s_waitcnt lgkmcnt(5)\n"
    "v_mfma_f32_16x16x32_bf16 v[72:75], a[128:131], v[36:39], v[72:75]\n"
    "ds_read_b128 v[60:63], v12 offset:39936\n"
    "v_mfma_f32_16x16x32_bf16 v[76:79], a[136:139], v[36:39], v[76:79]\n"
    "s_waitcnt lgkmcnt(5)\n"
    "v_mfma_f32_16x16x32_bf16 v[64:67], a[132:135], v[40:43], v[64:67]\n"
    "v_mfma_f32_16x16x32_bf16 v[68:71], a[140:143], v[40:43], v[68:71]\n"
    "s_waitcnt lgkmcnt(4)\n"
    "v_mfma_f32_16x16x32_bf16 v[72:75], a[132:135], v[44:47], v[72:75]\n"
    "v_mfma_f32_16x16x32_bf16 v[76:79], a[140:143], v[44:47], v[76:79]\n"
    "s_waitcnt lgkmcnt(3)\n"
    "v_mfma_f32_16x16x32_bf16 v[64:67], a[144:147], v[48:51], v[64:67]\n"
    "v_mfma_f32_16x16x32_bf16 v[68:71], a[152:155], v[48:51], v[68:71]\n"
    "s_waitcnt lgkmcnt(2)\n"
    "v_mfma_f32_16x16x32_bf16 v[72:75], a[144:147], v[52:55], v[72:75]\n"
    "v_mfma_f32_16x16x32_bf16 v[76:79], a[152:155], v[52:55], v[76:79]\n"
    "s_waitcnt lgkmcnt(1)\n"
    "v_mfma_f32_16x16x32_bf16 v[64:67], a[148:151], v[56:59], v[64:67]\n"
    "v_mfma_f32_16x16x32_bf16 v[68:71], a[156:159], v[56:59], v[68:71]\n"
    "s_waitcnt lgkmcnt(0)\n"
    "v_mfma_f32_16x16x32_bf16 v[72:75], a[148:151], v[60:63], v[72:75]\n"
    "v_mfma_f32_16x16x32_bf16 v[76:79], a[156:159], v[60:63], v[76:79]\n"
    "s_add_u32 s41, s41, 40960\n"
    "s_add_u32 s42, s42, 262144\n"
    "s_mov_b32 s72, s69\n"
    "s_mov_b32 s69, s70\n"
    "s_mov_b32 s70, s72\n"
    "v_cvt_pk_bf16_f32 v64, v64, v65\n"
    "v_cvt_pk_bf16_f32 v65, v66, v67\n"
    "v_cvt_pk_bf16_f32 v66, v68, v69\n"
    "v_cvt_pk_bf16_f32 v67, v70, v71\n"
    "ds_write_b128 v9, v[64:67] offset:0\n"
    "v_cvt_pk_bf16_f32 v72, v72, v73\n"
    "v_cvt_pk_bf16_f32 v73, v74, v75\n"
    "v_cvt_pk_bf16_f32 v74, v76, v77\n"
    "v_cvt_pk_bf16_f32 v75, v78, v79\n"
    "ds_write_b128 v9, v[72:75] offset:4096\n"
    "s_waitcnt vmcnt(0) lgkmcnt(0)\n"
    "s_barrier\n"
    "ds_read_b128 v[128:131], v10 offset:0\n"
    "ds_read_b128 v[80:83], v210 offset:0\n"
    "ds_read_b128 v[96:99], v210 offset:4096\n"
    "ds_read_b128 v[132:135], v10 offset:1024\n"
    "ds_read_b128 v[84:87], v210 offset:1024\n"
    "ds_read_b128 v[100:103], v210 offset:5120\n"
    "ds_read_b128 v[136:139], v10 offset:2048\n"
    "ds_read_b128 v[88:91], v210 offset:2048\n"
    "ds_read_b128 v[104:107], v210 offset:6144\n"
    "ds_read_b128 v[140:143], v10 offset:3072\n"
    "ds_read_b128 v[92:95], v210 offset:3072\n"
    "ds_read_b128 v[108:111], v210 offset:7168\n"
    "s_waitcnt lgkmcnt(9)\n"
    "ds_read_b128 v[112:115], v211 offset:8192\n"
    "v_mfma_f32_16x16x32_bf16 v[168:171], v[80:83], v[128:131], v[16:19]\n"
    "v_mfma_f32_16x16x32_bf16 v[172:175], v[96:99], v[128:131], v[20:23]\n"
    "s_waitcnt lgkmcnt(7)\n"
    "ds_read_b128 v[116:119], v211 offset:9216\n"
    "v_mfma_f32_16x16x32_bf16 v[168:171], v[84:87], v[132:135], v[168:171]\n"
    "v_mfma_f32_16x16x32_bf16 v[172:175], v[100:103], v[132:135], v[172:175]\n"
    "s_waitcnt lgkmcnt(5)\n"
    "ds_read_b128 v[120:123], v211 offset:10240\n"
    "v_mfma_f32_16x16x32_bf16 v[168:171], v[88:91], v[136:139], v[168:171]\n"
    "v_mfma_f32_16x16x32_bf16 v[172:175], v[104:107], v[136:139], v[172:175]\n"
    "s_waitcnt lgkmcnt(3)\n"
    "ds_read_b128 v[124:127], v211 offset:11264\n"
    "v_mfma_f32_16x16x32_bf16 v[168:171], v[92:95], v[140:143], v[168:171]\n"
    "v_mfma_f32_16x16x32_bf16 v[172:175], v[108:111], v[140:143], v[172:175]\n"
    "v_mov_b32 v13, v13\n"
    "v_mov_b32 v13, v13\n"
    "v_mov_b32 v13, v13\n"
    "v_mov_b32 v13, v13\n"
    "v_mov_b32 v13, v13\n"
    "v_mov_b32 v13, v13\n"
    "v_max3_f32 v206, v168, v169, v170\n"
    "s_nop 0\n"
    "v_max3_f32 v206, v206, v171, v172\n"
    "v_max3_f32 v206, v206, v173, v174\n"
    "v_max_f32 v206, v206, v175\n"
    "s_waitcnt lgkmcnt(0)\n"
    "s_barrier\n"
    "v_mov_b32 v212, 0\n"
    "RQ_loop_%=:\n"
    "s_add_u32 s74, s68, 1\n"
    "s_cmp_gt_u32 s71, s74\n"
    "s_cbranch_scc1 RQ_kq1_%=\n"
    "s_add_u32 s77, s74, 1\n"
    "s_mul_i32 s72, s74, 43\n"
    "s_lshr_b32 s72, s72, 7\n"
    "s_mul_i32 s72, s72, 3\n"
    "s_sub_u32 s74, s74, s72\n"
    "s_cmp_le_u32 s77, s45\n"
    "s_cselect_b32 s74, s74, 0\n"
    "s_mov_b32 s76, 0\n"
    "RQ_kp1_%=:\n"
    "buffer_load_dword v14, v5, s[36:39], 0 offen sc1\n"
    "s_waitcnt vmcnt(0)\n"
    "v_readlane_b32 s72, v14, s74\n"
    "v_readlane_b32 s71, v14, 0\n"
    "v_readlane_b32 s94, v14, 1\n"
    "v_readlane_b32 s95, v14, 2\n"
    "s_nop 0\n"
    "s_min_u32 s71, s71, s94\n"
    "s_min_u32 s71, s71, s95\n"
    "s_cmp_ge_u32 s72, s77\n"
    "s_cbranch_scc1 RQ_kq1_%=\n"
    "s_cmp_lg_u32 s75, 0\n"
    "s_cbranch_scc1 RQ_kq1_%=\n"
    "s_sleep 2\n"
    "s_add_u32 s76, s76, 1\n"
    "s_cmp_lt_u32 s76, 0x40000\n"
    "s_cbranch_scc1 RQ_kp1_%=\n"
    "s_mov_b32 s75, 1\n"
    "RQ_kq1_%=:\n"
    "s_add_u32 s77, s68, 1\n"
    "v_cmp_gt_u32 vcc, s77, v212\n"
    "s_cbranch_vccz RQ_qm_%=\n"
    "s_mov_b32 s76, 0\n"
    "RQ_pm_%=:\n"
    "buffer_load_dword v14, v6, s[36:39], 0 offen sc1\n"
    "s_waitcnt vmcnt(0)\n"
    "v_cmp_gt_u32 vcc, s77, v14\n"
    "s_cbranch_vccz RQ_qm_%=\n"
    "s_nop 0\n"
    "s_ff1_i32_b64 s94, vcc\n"
    "s_lshl_b64 exec, 1, s94\n"
    "RQ_rm_%=:\n"
    "s_cmp_lg_u32 s75, 0\n"
    "s_cbranch_scc1 RQ_qm_%=\n"
    "s_sleep 4\n"
    "buffer_load_dword v14, v6, s[36:39], 0 offen sc1\n"
    "s_waitcnt vmcnt(0)\n"
    "v_cmp_gt_u32 vcc, s77, v14\n"
    "s_add_u32 s76, s76, 1\n"
    "s_cmp_lt_u32 s76, 0x40000\n"
    "s_cselect_b32 s75, s75, 1\n"
    "s_cbranch_vccnz RQ_rm_%=\n"
    "s_mov_b64 exec, -1\n"
    "s_branch RQ_pm_%=\n"
    "RQ_qm_%=:\n"
    "s_mov_b64 exec, -1\n"
    "s_lshr_b32 s72, s48, 2\n"
    "s_add_u32 m0, s72, 106496\n"
    "s_nop 0\n"
    "buffer_load_dwordx4 v0, s[36:39], s44 offen sc1 lds\n"
    "s_cmp_eq_u32 s68, 63\n"
    "s_cbranch_scc1 RQ_last_%=\n"
    "s_sub_u32 s72, s69, s47\n"
    "v_add_u32 v12, s72, v0\n"
    "ds_read_b128 v[32:35], v12 offset:0\n"
    "ds_read_b128 v[36:39], v12 offset:20480\n"
    "ds_read_b128 v[40:43], v12 offset:1024\n"
    "ds_read_b128 v[44:47], v12 offset:21504\n"
    "ds_read_b128 v[48:51], v12 offset:2048\n"
    "ds_read_b128 v[52:55], v12 offset:22528\n"
    "s_waitcnt lgkmcnt(5)\n"
    "v_mfma_f32_16x16x32_bf16 v[64:67], a[0:3], v[32:35], 0\n"
    "ds_read_b128 v[56:59], v12 offset:3072\n"
    "v_mfma_f32_16x16x32_bf16 v[68:71], a[8:11], v[32:35], 0\n"
    "s_waitcnt lgkmcnt(5)\n"
    "v_mfma_f32_16x16x32_bf16 v[72:75], a[0:3], v[36:39], 0\n"
    "ds_read_b128 v[60:63], v12 offset:23552\n"
    "v_mfma_f32_16x16x32_bf16 v[76:79], a[8:11], v[36:39], 0\n"
    "s_add_u32 m0, s48, 90112\n"
    "s_nop 0\n"
    "buffer_load_dwordx4 v0, s[36:39], s42 offen offset:0 sc1 lds\n"
    "s_waitcnt lgkmcnt(5)\n"
    "v_mfma_f32_16x16x32_bf16 v[64:67], a[4:7], v[40:43], v[64:67]\n"
    "ds_read_b128 v[32:35], v12 offset:4096\n"
    "v_mfma_f32_16x16x32_bf16 v[68:71], a[12:15], v[40:43], v[68:71]\n"
    "s_waitcnt lgkmcnt(5)\n"
    "v_mfma_f32_16x16x32_bf16 v[72:75], a[4:7], v[44:47], v[72:75]\n"
    "ds_read_b128 v[36:39], v12 offset:24576\n"
    "v_mfma_f32_16x16x32_bf16 v[76:79], a[12:15], v[44:47], v[76:79]\n"
    "buffer_load_dwordx4 v0, s[36:39], s42 offen offset:1024 sc1 lds\n"
    "s_waitcnt lgkmcnt(5)\n"
    "v_mfma_f32_16x16x32_bf16 v[64:67], a[16:19], v[48:51], v[64:67]\n"
    "ds_read_b128 v[40:43], v12 offset:5120\n"
    "v_mfma_f32_16x16x32_bf16 v[68:71], a[24:27], v[48:51], v[68:71]\n"
    "s_waitcnt lgkmcnt(5)\n"
    "v_mfma_f32_16x16x32_bf16 v[72:75], a[16:19], v[52:55], v[72:75]\n"
    "ds_read_b128 v[44:47], v12 offset:25600\n"
    "v_mfma_f32_16x16x32_bf16 v[76:79], a[24:27], v[52:55], v[76:79]\n"
    "buffer_load_dwordx4 v0, s[36:39], s42 offen offset:2048 sc1 lds\n"
    "s_waitcnt lgkmcnt(5)\n"
    "v_mfma_f32_16x16x32_bf16 v[64:67], a[20:23], v[56:59], v[64:67]\n"
    "ds_read_b128 v[48:51], v12 offset:6144\n"
    "v_mfma_f32_16x16x32_bf16 v[68:71], a[28:31], v[56:59], v[68:71]\n"
    "s_waitcnt lgkmcnt(5)\n"
    "v_mfma_f32_16x16x32_bf16 v[72:75], a[20:23], v[60:63], v[72:75]\n"
    "ds_read_b128 v[52:55], v12 offset:26624\n"
    "v_mfma_f32_16x16x32_bf16 v[76:79], a[28:31], v[60:63], v[76:79]\n"
    "buffer_load_dwordx4 v0, s[36:39], s42 offen offset:3072 sc1 lds\n"
    "s_waitcnt lgkmcnt(5)\n"
    "v_mfma_f32_16x16x32_bf16 v[64:67], a[32:35], v[32:35], v[64:67]\n"
    "ds_read_b128 v[56:59], v12 offset:7168\n"
    "v_mfma_f32_16x16x32_bf16 v[68:71], a[40:43], v[32:35], v[68:71]\n"
    "s_waitcnt lgkmcnt(5)\n"
    "v_mfma_f32_16x16x32_bf16 v[72:75], a[32:35], v[36:39], v[72:75]\n"
    "ds_read_b128 v[60:63], v12 offset:27648\n"
    "v_mfma_f32_16x16x32_bf16 v[76:79], a[40:43], v[36:39], v[76:79]\n"
    "s_add_u32 m0, s70, 0\n"
    "s_add_u32 s73, s41, 0\n"
    "buffer_load_dwordx4 v0, s[36:39], s73 offen offset:0 sc1 lds\n"
    "s_waitcnt lgkmcnt(5)\n"
    "v_mfma_f32_16x16x32_bf16 v[64:67], a[36:39], v[40:43], v[64:67]\n"
    "ds_read_b128 v[32:35], v12 offset:8192\n"
    "v_mfma_f32_16x16x32_bf16 v[68:71], a[44:47], v[40:43], v[68:71]\n"
    "s_waitcnt lgkmcnt(5)\n"
    "v_mfma_f32_16x16x32_bf16 v[72:75], a[36:39], v[44:47], v[72:75]\n"
    "ds_read_b128 v[36:39], v12 offset:28672\n"
    "v_mfma_f32_16x16x32_bf16 v[76:79], a[44:47], v[44:47], v[76:79]\n"
    "buffer_load_dwordx4 v0, s[36:39], s73 offen offset:1024 sc1 lds\n"
    "s_waitcnt lgkmcnt(5)\n"
    "v_mfma_f32_16x16x32_bf16 v[64:67], a[48:51], v[48:51], v[64:67]\n"
    "ds_read_b128 v[40:43], v12 offset:9216\n"
    "v_mfma_f32_16x16x32_bf16 v[68:71], a[56:59], v[48:51], v[68:71]\n"
    "s_waitcnt lgkmcnt(5)\n"
    "v_mfma_f32_16x16x32_bf16 v[72:75], a[48:51], v[52:55], v[72:75]\n"
    "ds_read_b128 v[44:47], v12 offset:29696\n"
    "v_mfma_f32_16x16x32_bf16 v[76:79], a[56:59], v[52:55], v[76:79]\n"
    "buffer_load_dwordx4 v0, s[36:39], s73 offen offset:2048 sc1 lds\n"
    "s_waitcnt lgkmcnt(5)\n"
    "v_mfma_f32_16x16x32_bf16 v[64:67], a[52:55], v[56:59], v[64:67]\n"
    "ds_read_b128 v[48:51], v12 offset:10240\n"
    "v_mfma_f32_16x16x32_bf16 v[68:71], a[60:63], v[56:59], v[68:71]\n"
    "s_waitcnt lgkmcnt(5)\n"
    "v_mfma_f32_16x16x32_bf16 v[72:75], a[52:55], v[60:63], v[72:75]\n"
    "ds_read_b128 v[52:55], v12 offset:30720\n"
    "v_mfma_f32_16x16x32_bf16 v[76:79], a[60:63], v[60:63], v[76:79]\n"
    "buffer_load_dwordx4 v0, s[36:39], s73 offen offset:3072 sc1 lds\n"
    "s_waitcnt lgkmcnt(5)\n"
    "v_mfma_f32_16x16x32_bf16 v[64:67], a[64:67], v[32:35], v[64:67]\n"
    "ds_read_b128 v[56:59], v12 offset:11264\n"
    "v_mfma_f32_16x16x32_bf16 v[68:71], a[72:75], v[32:35], v[68:71]\n"
    "s_waitcnt lgkmcnt(5)\n"
    "v_mfma_f32_16x16x32_bf16 v[72:75], a[64:67], v[36:39], v[72:75]\n"
    "ds_read_b128 v[60:63], v12 offset:31744\n"
    "v_mfma_f32_16x16x32_bf16 v[76:79], a[72:75], v[36:39], v[76:79]\n"
    "s_add_u32 m0, s70, 4096\n"
    "s_add_u32 s73, s41, 4096\n"
    "buffer_load_dwordx4 v0, s[36:39], s73 offen offset:0 sc1 lds\n"
    "s_waitcnt lgkmcnt(5)\n"
    "v_mfma_f32_16x16x32_bf16 v[64:67], a[68:71], v[40:43], v[64:67]\n"
    "ds_read_b128 v[32:35], v12 offset:12288\n"
    "v_mfma_f32_16x16x32_bf16 v[68:71], a[76:79], v[40:43], v[68:71]\n"
    "s_waitcnt lgkmcnt(5)\n"
    "v_mfma_f32_16x16x32_bf16 v[72:75], a[68:71], v[44:47], v[72:75]\n"
    "ds_read_b128 v[36:39], v12 offset:32768\n"
    "v_mfma_f32_16x16x32_bf16 v[76:79], a[76:79], v[44:47], v[76:79]\n"
    "buffer_load_dwordx4 v0, s[36:39], s73 offen offset:1024 sc1 lds\n"
    "s_waitcnt lgkmcnt(5)\n"
    "v_mfma_f32_16x16x32_bf16 v[64:67], a[80:83], v[48:51], v[64:67]\n"
    "ds_read_b128 v[40:43], v12 offset:13312\n"
    "s_waitcnt vmcnt(10)\n"
    "s_barrier\n"
    "v_mfma_f32_16x16x32_bf16 v[68:71], a[88:91], v[48:51], v[68:71]\n"
    "ds_read_b128 v[144:147], v7 offset:0\n"
    "ds_read_b128 v[148:151], v7 offset:512\n"
    "s_waitcnt lgkmcnt(7)\n"
    "v_mfma_f32_16x16x32_bf16 v[72:75], a[80:83], v[52:55], v[72:75]\n"
    "ds_read_b128 v[44:47], v12 offset:33792\n"
    "ds_read_b128 v[152:155], v7 offset:1024\n"
    "v_mfma_f32_16x16x32_bf16 v[76:79], a[88:91], v[52:55], v[76:79]\n"
    "buffer_load_dwordx4 v0, s[36:39], s73 offen offset:2048 sc1 lds\n"
    "ds_read_b128 v[156:159], v7 offset:1536\n"
    "ds_read2_b64 v[160:163], v8 offset0:0 offset1:32\n"
    "s_waitcnt lgkmcnt(10)\n"
    "v_mfma_f32_16x16x32_bf16 v[64:67], a[84:87], v[56:59], v[64:67]\n"
    "ds_read_b128 v[48:51], v12 offset:14336\n"
    "ds_read2_b64 v[164:167], v8 offset0:64 offset1:96\n"
    "v_mfma_f32_16x16x32_bf16 v[68:71], a[92:95], v[56:59], v[68:71]\n"
    "s_waitcnt lgkmcnt(11)\n"
    "v_mfma_f32_16x16x32_bf16 v[72:75], a[84:87], v[60:63], v[72:75]\n"
    "ds_read_b128 v[52:55], v12 offset:34816\n"
    "s_waitcnt lgkmcnt(8)\n"
    "v_mfma_f32_16x16x32_bf16 v[176:179], v[144:147], v[128:131], v[24:27]\n"
    "v_mfma_f32_16x16x32_bf16 v[76:79], a[92:95], v[60:63], v[76:79]\n"
    "buffer_load_dwordx4 v0, s[36:39], s73 offen offset:3072 sc1 lds\n"
    "s_waitcnt lgkmcnt(7)\n"
    "v_mfma_f32_16x16x32_bf16 v[176:179], v[148:151], v[132:135], v[176:179]\n"
    "s_waitcnt lgkmcnt(5)\n"
    "v_mfma_f32_16x16x32_bf16 v[176:179], v[152:155], v[136:139], v[176:179]\n"
    "v_mfma_f32_16x16x32_bf16 v[64:67], a[96:99], v[32:35], v[64:67]\n"
    "ds_read_b128 v[56:59], v12 offset:15360\n"
    "s_waitcnt lgkmcnt(5)\n"
    "v_mfma_f32_16x16x32_bf16 v[176:179], v[156:159], v[140:143], v[176:179]\n"
    "v_mfma_f32_16x16x32_bf16 v[68:71], a[104:107], v[32:35], v[68:71]\n"
    "v_mfma_f32_16x16x32_bf16 v[72:75], a[96:99], v[36:39], v[72:75]\n"
    "ds_read_b128 v[60:63], v12 offset:35840\n"
    "v_mfma_f32_16x16x32_bf16 v[76:79], a[104:107], v[36:39], v[76:79]\n"
    "s_add_u32 m0, s70, 8192\n"
    "s_add_u32 s73, s41, 8192\n"
    "buffer_load_dwordx4 v0, s[36:39], s73 offen offset:0 sc1 lds\n"
    "v_max3_f32 v206, v206, v176, v177\n"
    "v_mfma_f32_16x16x32_bf16 v[64:67], a[100:103], v[40:43], v[64:67]\n"
    "ds_read_b128 v[32:35], v12 offset:16384\n"
    "v_max3_f32 v207, v206, v178, v179\n"
    "v_mfma_f32_16x16x32_bf16 v[68:71], a[108:111], v[40:43], v[68:71]\n"
    "v_max3_f32 v206, v206, v178, v179\n"
    "v_mfma_f32_16x16x32_bf16 v[72:75], a[100:103], v[44:47], v[72:75]\n"
    "ds_read_b128 v[36:39], v12 offset:36864\n"
    "v_permlane16_swap_b32 v206, v207\n"
    "v_mfma_f32_16x16x32_bf16 v[76:79], a[108:111], v[44:47], v[76:79]\n"
    "buffer_load_dwordx4 v0, s[36:39], s73 offen offset:1024 sc1 lds\n"
    "v_max_f32 v208, v206, v207\n"
    "v_max_f32 v206, v206, v207\n"
    "s_waitcnt lgkmcnt(6)\n"
    "v_mfma_f32_16x16x32_bf16 v[64:67], a[112:115], v[48:51], v[64:67]\n"
    "ds_read_b128 v[40:43], v12 offset:17408\n"
    "v_mfma_f32_16x16x32_bf16 v[68:71], a[120:123], v[48:51], v[68:71]\n"
    "v_permlane32_swap_b32 v206, v208\n"
    "v_max_f32 v206, v206, v208\n"
    "s_waitcnt lgkmcnt(5)\n"
    "v_mfma_f32_16x16x32_bf16 v[72:75], a[112:115], v[52:55], v[72:75]\n"
    "ds_read_b128 v[44:47], v12 offset:37888\n"
    "v_mul_f32 v206, 0xbe0293ee, v206\n"
    "v_mfma_f32_16x16x32_bf16 v[76:79], a[120:123], v[52:55], v[76:79]\n"
    "v_fmamk_f32 v168, v168, 0x3e0293ee, v206\n"
    "v_fmamk_f32 v169, v169, 0x3e0293ee, v206\n"
    "s_waitcnt lgkmcnt(5)\n"
    "v_mfma_f32_16x16x32_bf16 v[64:67], a[116:119], v[56:59], v[64:67]\n"
    "ds_read_b128 v[48:51], v12 offset:18432\n"
    "v_fmamk_f32 v170, v170, 0x3e0293ee, v206\n"
    "v_mfma_f32_16x16x32_bf16 v[68:71], a[124:127], v[56:59], v[68:71]\n"
    "v_fmamk_f32 v171, v171, 0x3e0293ee, v206\n"
    "v_fmamk_f32 v172, v172, 0x3e0293ee, v206\n"
    "s_waitcnt lgkmcnt(5)\n"
    "v_mfma_f32_16x16x32_bf16 v[72:75], a[116:119], v[60:63], v[72:75]\n"
    "ds_read_b128 v[52:55], v12 offset:38912\n"
    "v_fmamk_f32 v173, v173, 0x3e0293ee, v206\n"
    "v_mfma_f32_16x16x32_bf16 v[76:79], a[124:127], v[60:63], v[76:79]\n"
    "v_fmamk_f32 v174, v174, 0x3e0293ee, v206\n"
    "v_fmamk_f32 v175, v175, 0x3e0293ee, v206\n"
    "s_waitcnt lgkmcnt(5)\n"
    "v_mfma_f32_16x16x32_bf16 v[64:67], a[128:131], v[32:35], v[64:67]\n"
    "ds_read_b128 v[56:59], v12 offset:19456\n"
    "v_fmamk_f32 v176, v176, 0x3e0293ee, v206\n"
    "v_mfma_f32_16x16x32_bf16 v[68:71], a[136:139], v[32:35], v[68:71]\n"
    "v_fmamk_f32 v177, v177, 0x3e0293ee, v206\n"
    "v_fmamk_f32 v178, v178, 0x3e0293ee, v206\n"
    "s_waitcnt lgkmcnt(5)\n"
    "v_mfma_f32_16x16x32_bf16 v[72:75], a[128:131], v[36:39], v[72:75]\n"
    "ds_read_b128 v[60:63], v12 offset:39936\n"
    "v_fmamk_f32 v179, v179, 0x3e0293ee, v206\n"
    "v_mfma_f32_16x16x32_bf16 v[76:79], a[136:139], v[36:39], v[76:79]\n"
    "v_exp_f32 v168, v168\n"
    "v_exp_f32 v169, v169\n"
    "s_waitcnt lgkmcnt(5)\n"
    "v_mfma_f32_16x16x32_bf16 v[64:67], a[132:135], v[40:43], v[64:67]\n"
    "v_exp_f32 v170, v170\n"
    "v_mfma_f32_16x16x32_bf16 v[68:71], a[140:143], v[40:43], v[68:71]\n"
    "v_exp_f32 v171, v171\n"
    "v_exp_f32 v172, v172\n"
    "s_waitcnt lgkmcnt(4)\n"
    "v_mfma_f32_16x16x32_bf16 v[72:75], a[132:135], v[44:47], v[72:75]\n"
    "v_exp_f32 v173, v173\n"
    "v_mfma_f32_16x16x32_bf16 v[76:79], a[140:143], v[44:47], v[76:79]\n"
    "v_exp_f32 v174, v174\n"
    "v_exp_f32 v175, v175\n"
    "s_waitcnt lgkmcnt(3)\n"
    "v_mfma_f32_16x16x32_bf16 v[64:67], a[144:147], v[48:51], v[64:67]\n"
    "v_exp_f32 v176, v176\n"
    "v_mfma_f32_16x16x32_bf16 v[68:71], a[152:155], v[48:51], v[68:71]\n"
    "v_exp_f32 v177, v177\n"
    "v_exp_f32 v178, v178\n"
    "s_waitcnt lgkmcnt(2)\n"
    "v_mfma_f32_16x16x32_bf16 v[72:75], a[144:147], v[52:55], v[72:75]\n"
    "v_exp_f32 v179, v179\n"
    "v_mfma_f32_16x16x32_bf16 v[76:79], a[152:155], v[52:55], v[76:79]\n"
    "v_cvt_pk_bf16_f32 v200, v168, v169\n"
    "v_cvt_pk_bf16_f32 v201, v170, v171\n"
    "s_waitcnt lgkmcnt(1)\n"
    "v_mfma_f32_16x16x32_bf16 v[64:67], a[148:151], v[56:59], v[64:67]\n"
    "v_cvt_pk_bf16_f32 v202, v172, v173\n"
    "v_mfma_f32_16x16x32_bf16 v[68:71], a[156:159], v[56:59], v[68:71]\n"
    "v_cvt_pk_bf16_f32 v203, v174, v175\n"
    "v_cvt_pk_bf16_f32 v204, v176, v177\n"
    "s_waitcnt lgkmcnt(0)\n"
    "v_mfma_f32_16x16x32_bf16 v[72:75], a[148:151], v[60:63], v[72:75]\n"
    "v_cvt_pk_bf16_f32 v205, v178, v179\n"
    "v_mfma_f32_16x16x32_bf16 v[76:79], a[156:159], v[60:63], v[76:79]\n"
    "v_mfma_f32_16x16x32_bf16 v[196:199], v[28:31], v[200:203], 0\n"
    "v_mfma_f32_16x16x32_bf16 v[180:183], v[112:115], v[200:203], 0\n"
    "v_mfma_f32_16x16x32_bf16 v[184:187], v[116:119], v[200:203], 0\n"
    "v_mfma_f32_16x16x32_bf16 v[188:191], v[120:123], v[200:203], 0\n"
    "v_mfma_f32_16x16x32_bf16 v[192:195], v[124:127], v[200:203], 0\n"
    "v_mfma_f32_16x16x16_bf16 v[196:199], v[28:29], v[204:205], v[196:199]\n"
    "v_mfma_f32_16x16x16_bf16 v[180:183], v[160:161], v[204:205], v[180:183]\n"
    "v_mfma_f32_16x16x16_bf16 v[184:187], v[162:163], v[204:205], v[184:187]\n"
    "v_mfma_f32_16x16x16_bf16 v[188:191], v[164:165], v[204:205], v[188:191]\n"
    "v_mfma_f32_16x16x16_bf16 v[192:195], v[166:167], v[204:205], v[192:195]\n"
    "v_rcp_f32 v196, v196\n"
    "s_nop 0\n"
    "v_mul_f32 v180, v180, v196\n"
    "v_mul_f32 v181, v181, v196\n"
    "v_mul_f32 v182, v182, v196\n"
    "v_mul_f32 v183, v183, v196\n"
    "v_mul_f32 v184, v184, v196\n"
    "v_mul_f32 v185, v185, v196\n"
    "v_mul_f32 v186, v186, v196\n"
    "v_mul_f32 v187, v187, v196\n"
    "v_cvt_pk_bf16_f32 v180, v180, v181\n"
    "v_cvt_pk_bf16_f32 v181, v182, v183\n"
    "v_cvt_pk_bf16_f32 v182, v184, v185\n"
    "v_cvt_pk_bf16_f32 v183, v186, v187\n"
    "buffer_store_dwordx4 v[180:183], v0, s[36:39], s46 offen offset:0 sc1\n"
    "v_mul_f32 v188, v188, v196\n"
    "v_mul_f32 v189, v189, v196\n"
    "v_mul_f32 v190, v190, v196\n"
    "v_mul_f32 v191, v191, v196\n"
    "v_mul_f32 v192, v192, v196\n"
    "v_mul_f32 v193, v193, v196\n"
    "v_mul_f32 v194, v194, v196\n"
    "v_mul_f32 v195, v195, v196\n"
    "v_cvt_pk_bf16_f32 v188, v188, v189\n"
    "v_cvt_pk_bf16_f32 v189, v190, v191\n"
    "v_cvt_pk_bf16_f32 v190, v192, v193\n"
    "v_cvt_pk_bf16_f32 v191, v194, v195\n"
    "buffer_store_dwordx4 v[188:191], v0, s[36:39], s46 offen offset:1024 sc1\n"
    "s_add_u32 s41, s41, 40960\n"
    "s_add_u32 s42, s42, 262144\n"
    "s_mov_b32 s72, s69\n"
    "s_mov_b32 s69, s70\n"
    "s_mov_b32 s70, s72\n"
    "v_cvt_pk_bf16_f32 v64, v64, v65\n"
    "v_cvt_pk_bf16_f32 v65, v66, v67\n"
    "v_cvt_pk_bf16_f32 v66, v68, v69\n"
    "v_cvt_pk_bf16_f32 v67, v70, v71\n"
    "ds_write_b128 v9, v[64:67] offset:0\n"
    "v_cvt_pk_bf16_f32 v72, v72, v73\n"
    "v_cvt_pk_bf16_f32 v73, v74, v75\n"
    "v_cvt_pk_bf16_f32 v74, v76, v77\n"
    "v_cvt_pk_bf16_f32 v75, v78, v79\n"
    "ds_write_b128 v9, v[72:75] offset:4096\n"
    "buffer_load_dword v212, v6, s[36:39], 0 offen sc1\n"
    "s_waitcnt vmcnt(3) lgkmcnt(0)\n"
    "s_barrier\n"
    "ds_read_b128 v[128:131], v10 offset:0\n"
    "ds_read_b128 v[80:83], v210 offset:0\n"
    "ds_read_b128 v[96:99], v210 offset:4096\n"
    "ds_read_b128 v[132:135], v10 offset:1024\n"
    "ds_read_b128 v[84:87], v210 offset:1024\n"
    "ds_read_b128 v[100:103], v210 offset:5120\n"
    "ds_read_b128 v[136:139], v10 offset:2048\n"
    "ds_read_b128 v[88:91], v210 offset:2048\n"
    "ds_read_b128 v[104:107], v210 offset:6144\n"
    "ds_read_b128 v[140:143], v10 offset:3072\n"
    "ds_read_b128 v[92:95], v210 offset:3072\n"
    "ds_read_b128 v[108:111], v210 offset:7168\n"
    "s_waitcnt lgkmcnt(9)\n"
    "ds_read_b128 v[112:115], v211 offset:8192\n"
    "v_mfma_f32_16x16x32_bf16 v[168:171], v[80:83], v[128:131], v[16:19]\n"
    "v_mfma_f32_16x16x32_bf16 v[172:175], v[96:99], v[128:131], v[20:23]\n"
    "s_waitcnt lgkmcnt(7)\n"
    "ds_read_b128 v[116:119], v211 offset:9216\n"
    "v_mfma_f32_16x16x32_bf16 v[168:171], v[84:87], v[132:135], v[168:171]\n"
    "v_mfma_f32_16x16x32_bf16 v[172:175], v[100:103], v[132:135], v[172:175]\n"
    "s_waitcnt lgkmcnt(5)\n"
    "ds_read_b128 v[120:123], v211 offset:10240\n"
    "v_mfma_f32_16x16x32_bf16 v[168:171], v[88:91], v[136:139], v[168:171]\n"
    "v_mfma_f32_16x16x32_bf16 v[172:175], v[104:107], v[136:139], v[172:175]\n"
    "s_waitcnt lgkmcnt(3)\n"
    "ds_read_b128 v[124:127], v211 offset:11264\n"
    "v_mfma_f32_16x16x32_bf16 v[168:171], v[92:95], v[140:143], v[168:171]\n"
    "v_mfma_f32_16x16x32_bf16 v[172:175], v[108:111], v[140:143], v[172:175]\n"
    "v_mov_b32 v13, v13\n"
    "v_mov_b32 v13, v13\n"
    "v_mov_b32 v13, v13\n"
    "v_mov_b32 v13, v13\n"
    "v_mov_b32 v13, v13\n"
    "v_mov_b32 v13, v13\n"
    "v_max3_f32 v206, v168, v169, v170\n"
    "s_nop 0\n"
    "v_max3_f32 v206, v206, v171, v172\n"
    "v_max3_f32 v206, v206, v173, v174\n"
    "v_max_f32 v206, v206, v175\n"
    "s_waitcnt lgkmcnt(0)\n"
    "s_and_b32 s72, s68, 3\n"
    "s_cmp_eq_u32 s72, 3\n"
    "s_cselect_b32 s72, 0x200000, 0\n"
    "s_add_u32 s46, s46, s72\n"
    "s_add_u32 s46, s46, 0x80000\n"
    "s_add_u32 s44, s44, 65536\n"
    "s_add_u32 s68, s68, 1\n"
    "v_mov_b32 v13, s68\n"
    "s_waitcnt vmcnt(0)\n"
    "s_barrier\n"
    "buffer_store_dword v13, v4, s[36:39], 0 offen sc1\n"
    "s_branch RQ_loop_%=\n"
    "RQ_last_%=:\n"
    "s_waitcnt vmcnt(0)\n"
    "s_barrier\n"
    "ds_read_b128 v[144:147], v7 offset:0\n"
    "ds_read_b128 v[148:151], v7 offset:512\n"
    "ds_read_b128 v[152:155], v7 offset:1024\n"
    "ds_read_b128 v[156:159], v7 offset:1536\n"
    "ds_read2_b64 v[160:163], v8 offset0:0 offset1:32\n"
    "ds_read2_b64 v[164:167], v8 offset0:64 offset1:96\n"
    "s_waitcnt lgkmcnt(5)\n"
    "v_mfma_f32_16x16x32_bf16 v[176:179], v[144:147], v[128:131], v[24:27]\n"
    "s_waitcnt lgkmcnt(4)\n"
    "v_mfma_f32_16x16x32_bf16 v[176:179], v[148:151], v[132:135], v[176:179]\n"
    "s_waitcnt lgkmcnt(3)\n"
    "v_mfma_f32_16x16x32_bf16 v[176:179], v[152:155], v[136:139], v[176:179]\n"
    "s_waitcnt lgkmcnt(2)\n"
    "v_mfma_f32_16x16x32_bf16 v[176:179], v[156:159], v[140:143], v[176:179]\n"
    "v_mov_b32 v13, v13\n"
    "v_mov_b32 v13, v13\n"
    "v_mov_b32 v13, v13\n"
    "v_mov_b32 v13, v13\n"
    "v_mov_b32 v13, v13\n"
    "v_mov_b32 v13, v13\n"
    "v_mov_b32 v13, v13\n"
    "v_mov_b32 v13, v13\n"
    "v_max3_f32 v206, v206, v176, v177\n"
    "v_max3_f32 v207, v206, v178, v179\n"
    "v_max3_f32 v206, v206, v178, v179\n"
    "s_nop 1\n"
    "v_permlane16_swap_b32 v206, v207\n"
    "v_max_f32 v208, v206, v207\n"
    "v_max_f32 v206, v206, v207\n"
    "s_nop 1\n"
    "v_permlane32_swap_b32 v206, v208\n"
    "v_max_f32 v206, v206, v208\n"
    "v_mul_f32 v206, 0xbe0293ee, v206\n"
    "v_fmamk_f32 v168, v168, 0x3e0293ee, v206\n"
    "v_fmamk_f32 v169, v169, 0x3e0293ee, v206\n"
    "v_fmamk_f32 v170, v170, 0x3e0293ee, v206\n"
    "v_fmamk_f32 v171, v171, 0x3e0293ee, v206\n"
    "v_fmamk_f32 v172, v172, 0x3e0293ee, v206\n"
    "v_fmamk_f32 v173, v173, 0x3e0293ee, v206\n"
    "v_fmamk_f32 v174, v174, 0x3e0293ee, v206\n"
    "v_fmamk_f32 v175, v175, 0x3e0293ee, v206\n"
    "v_fmamk_f32 v176, v176, 0x3e0293ee, v206\n"
    "v_fmamk_f32 v177, v177, 0x3e0293ee, v206\n"
    "v_fmamk_f32 v178, v178, 0x3e0293ee, v206\n"
    "v_fmamk_f32 v179, v179, 0x3e0293ee, v206\n"
    "v_exp_f32 v168, v168\n"
    "v_exp_f32 v169, v169\n"
    "v_exp_f32 v170, v170\n"
    "v_exp_f32 v171, v171\n"
    "v_exp_f32 v172, v172\n"
    "v_exp_f32 v173, v173\n"
    "v_exp_f32 v174, v174\n"
    "v_exp_f32 v175, v175\n"
    "v_exp_f32 v176, v176\n"
    "v_exp_f32 v177, v177\n"
    "v_exp_f32 v178, v178\n"
    "v_exp_f32 v179, v179\n"
    "v_cvt_pk_bf16_f32 v200, v168, v169\n"
    "v_cvt_pk_bf16_f32 v201, v170, v171\n"
    "v_cvt_pk_bf16_f32 v202, v172, v173\n"
    "v_cvt_pk_bf16_f32 v203, v174, v175\n"
    "v_cvt_pk_bf16_f32 v204, v176, v177\n"
    "v_cvt_pk_bf16_f32 v205, v178, v179\n"
    "v_mfma_f32_16x16x32_bf16 v[196:199], v[28:31], v[200:203], 0\n"
    "v_mfma_f32_16x16x32_bf16 v[180:183], v[112:115], v[200:203], 0\n"
    "v_mfma_f32_16x16x32_bf16 v[184:187], v[116:119], v[200:203], 0\n"
    "v_mfma_f32_16x16x32_bf16 v[188:191], v[120:123], v[200:203], 0\n"
    "v_mfma_f32_16x16x32_bf16 v[192:195], v[124:127], v[200:203], 0\n"
    "v_mfma_f32_16x16x16_bf16 v[196:199], v[28:29], v[204:205], v[196:199]\n"
    "s_waitcnt lgkmcnt(0)\n"
    "v_mfma_f32_16x16x16_bf16 v[180:183], v[160:161], v[204:205], v[180:183]\n"
    "v_mfma_f32_16x16x16_bf16 v[184:187], v[162:163], v[204:205], v[184:187]\n"
    "v_mfma_f32_16x16x16_bf16 v[188:191], v[164:165], v[204:205], v[188:191]\n"
    "v_mfma_f32_16x16x16_bf16 v[192:195], v[166:167], v[204:205], v[192:195]\n"
    "v_rcp_f32 v196, v196\n"
    "s_nop 0\n"
    "v_mul_f32 v180, v180, v196\n"
    "v_mul_f32 v181, v181, v196\n"
    "v_mul_f32 v182, v182, v196\n"
    "v_mul_f32 v183, v183, v196\n"
    "v_mul_f32 v184, v184, v196\n"
    "v_mul_f32 v185, v185, v196\n"
    "v_mul_f32 v186, v186, v196\n"
    "v_mul_f32 v187, v187, v196\n"
    "v_cvt_pk_bf16_f32 v180, v180, v181\n"
    "v_cvt_pk_bf16_f32 v181, v182, v183\n"
    "v_cvt_pk_bf16_f32 v182, v184, v185\n"
    "v_cvt_pk_bf16_f32 v183, v186, v187\n"
    "buffer_store_dwordx4 v[180:183], v0, s[36:39], s46 offen offset:0 sc1\n"
    "v_mul_f32 v188, v188, v196\n"
    "v_mul_f32 v189, v189, v196\n"
    "v_mul_f32 v190, v190, v196\n"
    "v_mul_f32 v191, v191, v196\n"
    "v_mul_f32 v192, v192, v196\n"
    "v_mul_f32 v193, v193, v196\n"
    "v_mul_f32 v194, v194, v196\n"
    "v_mul_f32 v195, v195, v196\n"
    "v_cvt_pk_bf16_f32 v188, v188, v189\n"
    "v_cvt_pk_bf16_f32 v189, v190, v191\n"
    "v_cvt_pk_bf16_f32 v190, v192, v193\n"
    "v_cvt_pk_bf16_f32 v191, v194, v195\n"
    "buffer_store_dwordx4 v[188:191], v0, s[36:39], s46 offen offset:1024 sc1\n"
    "v_mov_b32 v13, 64\n"
    "s_waitcnt vmcnt(0)\n"
    "s_barrier\n"
    "buffer_store_dword v13, v4, s[36:39], 0 offen sc1\n"
    "s_waitcnt vmcnt(0)\n"
    "s_endpgm\n"
            :: "{v[0:15]}"(C), "{v[16:31]}"(B), "{s[36:51]}"(SA), "{s[52:67]}"(SB) : CLOB_Q);
        __builtin_unreachable();
    }
    {
        ROLE_BEGIN
        const int l = lane_id();
        const int h = xi * 8 + (slot & 7), rg = slot >> 3;
        v16u C = {}, SA = {}, SB = {};
        const unsigned stg = w * 36864;
        strip_consts(C, l, stg, 8192, false);
        C[4] = (w == 0 && l == 0) ? FL_KV + h * 512 + rg * 128 : OOB;
        put_srd(SA, 0, p.ws, 0, WS_BYTES);
        SA[4] = stg; SA[5] = WS_XNF + rg * 40960 + w * 10240; SA[6] = WS_KF + rg * 262144 + h * 8192 + w * 1024; SA[7] = WS_VF + rg * 262144 + h * 8192 + w * 2048;
        SA[8] = rg; SA[9] = w * 10240; SA[12] = xi < 3 ? NHC : 99; SA[10] = (rg == 0 || xi == 3) ? 64 : NHC; SA[11] = (h * 4 + w) * 1024 - 0x20000;
        put_srd(SB, 8, rg == 0 ? p.w1 : (rg == 1 ? p.w3 : p.w2), 0, D * F * 2);
        put_srd(SB, 0, p.Wk, (h * 128 + 32 * w) * 2, D * HD * 2); put_srd(SB, 4, p.Wv, (h * 128 + 32 * w) * 2, D * HD * 2);
        uni(SA); uni(SB);
        asm volatile(
    "s_add_u32 m0, s40, 0\n"
    "s_mov_b32 s72, 0\n"
    "buffer_load_dwordx4 v1, s[52:55], s72 offen lds\n"
    "s_add_u32 m0, s40, 1024\n"
    "s_mov_b32 s72, 131072\n"
    "buffer_load_dwordx4 v1, s[52:55], s72 offen lds\n"
    "s_add_u32 m0, s40, 2048\n"
    "s_mov_b32 s72, 262144\n"
    "buffer_load_dwordx4 v1, s[52:55], s72 offen lds\n"
    "s_add_u32 m0, s40, 3072\n"
    "s_mov_b32 s72, 393216\n"
    "buffer_load_dwordx4 v1, s[52:55], s72 offen lds\n"
    "s_add_u32 m0, s40, 4096\n"
    "s_mov_b32 s72, 524288\n"
    "buffer_load_dwordx4 v1, s[52:55], s72 offen lds\n"
    "s_add_u32 m0, s40, 5120\n"
    "s_mov_b32 s72, 655360\n"
    "buffer_load_dwordx4 v1, s[52:55], s72 offen lds\n"
    "s_add_u32 m0, s40, 6144\n"
    "s_mov_b32 s72, 786432\n"
    "buffer_load_dwordx4 v1, s[52:55], s72 offen lds\n"
    "s_add_u32 m0, s40, 7168\n"
    "s_mov_b32 s72, 917504\n"
    "buffer_load_dwordx4 v1, s[52:55], s72 offen lds\n"
    "s_add_u32 m0, s40, 8192\n"
    "s_mov_b32 s72, 1048576\n"
    "buffer_load_dwordx4 v1, s[52:55], s72 offen lds\n"
    "s_add_u32 m0, s40, 9216\n"
    "s_mov_b32 s72, 1179648\n"
    "buffer_load_dwordx4 v1, s[52:55], s72 offen lds\n"
    "s_add_u32 m0, s40, 10240\n"
    "s_mov_b32 s72, 1310720\n"
    "buffer_load_dwordx4 v1, s[52:55], s72 offen lds\n"
    "s_add_u32 m0, s40, 11264\n"
    "s_mov_b32 s72, 1441792\n"
    "buffer_load_dwordx4 v1, s[52:55], s72 offen lds\n"
    "s_add_u32 m0, s40, 12288\n"
    "s_mov_b32 s72, 1572864\n"
    "buffer_load_dwordx4 v1, s[52:55], s72 offen lds\n"
    "s_add_u32 m0, s40, 13312\n"
    "s_mov_b32 s72, 1703936\n"
    "buffer_load_dwordx4 v1, s[52:55], s72 offen lds\n"
    "s_add_u32 m0, s40, 14336\n"
    "s_mov_b32 s72, 1835008\n"
    "buffer_load_dwordx4 v1, s[52:55], s72 offen lds\n"
    "s_add_u32 m0, s40, 15360\n"
    "s_mov_b32 s72, 1966080\n"
    "buffer_load_dwordx4 v1, s[52:55], s72 offen lds\n"
    "s_add_u32 m0, s40, 16384\n"
    "s_mov_b32 s72, 2097152\n"
    "buffer_load_dwordx4 v1, s[52:55], s72 offen lds\n"
    "s_add_u32 m0, s40, 17408\n"
    "s_mov_b32 s72, 2228224\n"
    "buffer_load_dwordx4 v1, s[52:55], s72 offen lds\n"
    "s_add_u32 m0, s40, 18432\n"
    "s_mov_b32 s72, 2359296\n"
    "buffer_load_dwordx4 v1, s[52:55], s72 offen lds\n"
    "s_add_u32 m0, s40, 19456\n"
    "s_mov_b32 s72, 2490368\n"
    "buffer_load_dwordx4 v1, s[52:55], s72 offen lds\n"
    "s_add_u32 m0, s40, 20480\n"
    "s_mov_b32 s72, 2621440\n"
    "buffer_load_dwordx4 v1, s[52:55], s72 offen lds\n"
    "s_add_u32 m0, s40, 21504\n"
    "s_mov_b32 s72, 2752512\n"
    "buffer_load_dwordx4 v1, s[52:55], s72 offen lds\n"
    "s_add_u32 m0, s40, 22528\n"
    "s_mov_b32 s72, 2883584\n"
    "buffer_load_dwordx4 v1, s[52:55], s72 offen lds\n"
    "s_add_u32 m0, s40, 23552\n"
    "s_mov_b32 s72, 3014656\n"
    "buffer_load_dwordx4 v1, s[52:55], s72 offen lds\n"
    "s_add_u32 m0, s40, 24576\n"
    "s_mov_b32 s72, 3145728\n"
    "buffer_load_dwordx4 v1, s[52:55], s72 offen lds\n"
    "s_add_u32 m0, s40, 25600\n"
    "s_mov_b32 s72, 3276800\n"
    "buffer_load_dwordx4 v1, s[52:55], s72 offen lds\n"
    "s_add_u32 m0, s40, 26624\n"
    "s_mov_b32 s72, 3407872\n"
    "buffer_load_dwordx4 v1, s[52:55], s72 offen lds\n"
    "s_add_u32 m0, s40, 27648\n"
    "s_mov_b32 s72, 3538944\n"
    "buffer_load_dwordx4 v1, s[52:55], s72 offen lds\n"
    "s_add_u32 m0, s40, 28672\n"
    "s_mov_b32 s72, 3670016\n"
    "buffer_load_dwordx4 v1, s[52:55], s72 offen lds\n"
    "s_add_u32 m0, s40, 29696\n"
    "s_mov_b32 s72, 3801088\n"
    "buffer_load_dwordx4 v1, s[52:55], s72 offen lds\n"
    "s_add_u32 m0, s40, 30720\n"
    "s_mov_b32 s72, 3932160\n"
    "buffer_load_dwordx4 v1, s[52:55], s72 offen lds\n"
    "s_add_u32 m0, s40, 31744\n"
    "s_mov_b32 s72, 4063232\n"
    "buffer_load_dwordx4 v1, s[52:55], s72 offen lds\n"
    "s_add_u32 m0, s40, 32768\n"
    "s_mov_b32 s72, 4194304\n"
    "buffer_load_dwordx4 v1, s[52:55], s72 offen lds\n"
    "s_add_u32 m0, s40, 33792\n"
    "s_mov_b32 s72, 4325376\n"
    "buffer_load_dwordx4 v1, s[52:55], s72 offen lds\n"
    "s_add_u32 m0, s40, 34816\n"
    "s_mov_b32 s72, 4456448\n"
    "buffer_load_dwordx4 v1, s[52:55], s72 offen lds\n"
    "s_add_u32 m0, s40, 35840\n"
    "s_mov_b32 s72, 4587520\n"
    "buffer_load_dwordx4 v1, s[52:55], s72 offen lds\n"
    "s_waitcnt vmcnt(32)\n"
    "ds_read_b64_tr_b16 a[0:1], v2 offset:0\n"
    "ds_read_b64_tr_b16 a[2:3], v2 offset:256\n"
    "ds_read_b64_tr_b16 a[4:5], v2 offset:2048\n"
    "ds_read_b64_tr_b16 a[6:7], v2 offset:2304\n"
    "ds_read_b64_tr_b16 a[8:9], v3 offset:0\n"
    "ds_read_b64_tr_b16 a[10:11], v3 offset:256\n"
    "ds_read_b64_tr_b16 a[12:13], v3 offset:2048\n"
    "ds_read_b64_tr_b16 a[14:15], v3 offset:2304\n"
    "s_waitcnt lgkmcnt(0)\n"
    "s_add_u32 m0, s40, 0\n"
    "s_mov_b32 s72, 4718592\n"
    "buffer_load_dwordx4 v1, s[52:55], s72 offen lds\n"
    "s_add_u32 m0, s40, 1024\n"
    "s_mov_b32 s72, 4849664\n"
    "buffer_load_dwordx4 v1, s[52:55], s72 offen lds\n"
    "s_add_u32 m0, s40, 2048\n"
    "s_mov_b32 s72, 4980736\n"
    "buffer_load_dwordx4 v1, s[52:55], s72 offen lds\n"
    "s_add_u32 m0, s40, 3072\n"
    "s_mov_b32 s72, 5111808\n"
    "buffer_load_dwordx4 v1, s[52:55], s72 offen lds\n"
    "s_waitcnt vmcnt(32)\n"
    "ds_read_b64_tr_b16 a[16:17], v2 offset:4096\n"
    "ds_read_b64_tr_b16 a[18:19], v2 offset:4352\n"
    "ds_read_b64_tr_b16 a[20:21], v2 offset:6144\n"
    "ds_read_b64_tr_b16 a[22:23], v2 offset:6400\n"
    "ds_read_b64_tr_b16 a[24:25], v3 offset:4096\n"
    "ds_read_b64_tr_b16 a[26:27], v3 offset:4352\n"
    "ds_read_b64_tr_b16 a[28:29], v3 offset:6144\n"
    "ds_read_b64_tr_b16 a[30:31], v3 offset:6400\n"
    "s_waitcnt lgkmcnt(0)\n"
    "s_add_u32 m0, s40, 4096\n"
    "s_mov_b32 s72, 0\n"
    "buffer_load_dwordx4 v1, s[56:59], s72 offen lds\n"
    "s_add_u32 m0, s40, 5120\n"
    "s_mov_b32 s72, 131072\n"
    "buffer_load_dwordx4 v1, s[56:59], s72 offen lds\n"
    "s_add_u32 m0, s40, 6144\n"
    "s_mov_b32 s72, 262144\n"
    "buffer_load_dwordx4 v1, s[56:59], s72 offen lds\n"
    "s_add_u32 m0, s40, 7168\n"
    "s_mov_b32 s72, 393216\n"
    "buffer_load_dwordx4 v1, s[56:59], s72 offen lds\n"
    "s_waitcnt vmcnt(32)\n"
    "ds_read_b64_tr_b16 a[32:33], v2 offset:8192\n"
    "ds_read_b64_tr_b16 a[34:35], v2 offset:8448\n"
    "ds_read_b64_tr_b16 a[36:37], v2 offset:10240\n"
    "ds_read_b64_tr_b16 a[38:39], v2 offset:10496\n"
    "ds_read_b64_tr_b16 a[40:41], v3 offset:8192\n"
    "ds_read_b64_tr_b16 a[42:43], v3 offset:8448\n"
    "ds_read_b64_tr_b16 a[44:45], v3 offset:10240\n"
    "ds_read_b64_tr_b16 a[46:47], v3 offset:10496\n"
    "s_waitcnt lgkmcnt(0)\n"
    "s_add_u32 m0, s40, 8192\n"
    "s_mov_b32 s72, 524288\n"
    "buffer_load_dwordx4 v1, s[56:59], s72 offen lds\n"
    "s_add_u32 m0, s40, 9216\n"
    "s_mov_b32 s72, 655360\n"
    "buffer_load_dwordx4 v1, s[56:59], s72 offen lds\n"
    "s_add_u32 m0, s40, 10240\n"
    "s_mov_b32 s72, 786432\n"
    "buffer_load_dwordx4 v1, s[56:59], s72 offen lds\n"
    "s_add_u32 m0, s40, 11264\n"
    "s_mov_b32 s72, 917504\n"
    "buffer_load_dwordx4 v1, s[56:59], s72 offen lds\n"
    "s_waitcnt vmcnt(32)\n"
    "ds_read_b64_tr_b16 a[48:49], v2 offset:12288\n"
    "ds_read_b64_tr_b16 a[50:51], v2 offset:12544\n"
    "ds_read_b64_tr_b16 a[52:53], v2 offset:14336\n"
    "ds_read_b64_tr_b16 a[54:55], v2 offset:14592\n"
    "ds_read_b64_tr_b16 a[56:57], v3 offset:12288\n"
    "ds_read_b64_tr_b16 a[58:59], v3 offset:12544\n"
    "ds_read_b64_tr_b16 a[60:61], v3 offset:14336\n"
    "ds_read_b64_tr_b16 a[62:63], v3 offset:14592\n"
    "s_waitcnt lgkmcnt(0)\n"
    "s_add_u32 m0, s40, 12288\n"
    "s_mov_b32 s72, 1048576\n"
    "buffer_load_dwordx4 v1, s[56:59], s72 offen lds\n"
    "s_add_u32 m0, s40, 13312\n"
    "s_mov_b32 s72, 1179648\n"
    "buffer_load_dwordx4 v1, s[56:59], s72 offen lds\n"
    "s_add_u32 m0, s40, 14336\n"
    "s_mov_b32 s72, 1310720\n"
    "buffer_load_dwordx4 v1, s[56:59], s72 offen lds\n"
    "s_add_u32 m0, s40, 15360\n"
    "s_mov_b32 s72, 1441792\n"
    "buffer_load_dwordx4 v1, s[56:59], s72 offen lds\n"
    "s_waitcnt vmcnt(32)\n"
    "ds_read_b64_tr_b16 a[64:65], v2 offset:16384\n"
    "ds_read_b64_tr_b16 a[66:67], v2 offset:16640\n"
    "ds_read_b64_tr_b16 a[68:69], v2 offset:18432\n"
    "ds_read_b64_tr_b16 a[70:71], v2 offset:18688\n"
    "ds_read_b64_tr_b16 a[72:73], v3 offset:16384\n"
    "ds_read_b64_tr_b16 a[74:75], v3 offset:16640\n"
    "ds_read_b64_tr_b16 a[76:77], v3 offset:18432\n"
    "ds_read_b64_tr_b16 a[78:79], v3 offset:18688\n"
    "s_waitcnt lgkmcnt(0)\n"
    "s_add_u32 m0, s40, 16384\n"
    "s_mov_b32 s72, 1572864\n"
    "buffer_load_dwordx4 v1, s[56:59], s72 offen lds\n"
    "s_add_u32 m0, s40, 17408\n"
    "s_mov_b32 s72, 1703936\n"
    "buffer_load_dwordx4 v1, s[56:59], s72 offen lds\n"
    "s_add_u32 m0, s40, 18432\n"
    "s_mov_b32 s72, 1835008\n"
    "buffer_load_dwordx4 v1, s[56:59], s72 offen lds\n"
    "s_add_u32 m0, s40, 19456\n"
    "s_mov_b32 s72, 1966080\n"
    "buffer_load_dwordx4 v1, s[56:59], s72 offen lds\n"
    "s_waitcnt vmcnt(32)\n"
    "ds_read_b64_tr_b16 a[80:81], v2 offset:20480\n"
    "ds_read_b64_tr_b16 a[82:83], v2 offset:20736\n"
    "ds_read_b64_tr_b16 a[84:85], v2 offset:22528\n"
    "ds_read_b64_tr_b16 a[86:87], v2 offset:22784\n"
    "ds_read_b64_tr_b16 a[88:89], v3 offset:20480\n"
    "ds_read_b64_tr_b16 a[90:91], v3 offset:20736\n"
    "ds_read_b64_tr_b16 a[92:93], v3 offset:22528\n"
    "ds_read_b64_tr_b16 a[94:95], v3 offset:22784\n"
    "s_waitcnt lgkmcnt(0)\n"
    "s_add_u32 m0, s40, 20480\n"
    "s_mov_b32 s72, 2097152\n"
    "buffer_load_dwordx4 v1, s[56:59], s72 offen lds\n"
    "s_add_u32 m0, s40, 21504\n"
    "s_mov_b32 s72, 2228224\n"
    "buffer_load_dwordx4 v1, s[56:59], s72 offen lds\n"
    "s_add_u32 m0, s40, 22528\n"
    "s_mov_b32 s72, 2359296\n"
    "buffer_load_dwordx4 v1, s[56:59], s72 offen lds\n"
    "s_add_u32 m0, s40, 23552\n"
    "s_mov_b32 s72, 2490368\n"
    "buffer_load_dwordx4 v1, s[56:59], s72 offen lds\n"
    "s_waitcnt vmcnt(32)\n"
    "ds_read_b64_tr_b16 a[96:97], v2 offset:24576\n"
    "ds_read_b64_tr_b16 a[98:99], v2 offset:24832\n"
    "ds_read_b64_tr_b16 a[100:101], v2 offset:26624\n"
    "ds_read_b64_tr_b16 a[102:103], v2 offset:26880\n"
    "ds_read_b64_tr_b16 a[104:105], v3 offset:24576\n"
    "ds_read_b64_tr_b16 a[106:107], v3 offset:24832\n"
    "ds_read_b64_tr_b16 a[108:109], v3 offset:26624\n"
    "ds_read_b64_tr_b16 a[110:111], v3 offset:26880\n"
    "s_waitcnt lgkmcnt(0)\n"
    "s_add_u32 m0, s40, 24576\n"
    "s_mov_b32 s72, 2621440\n"
    "buffer_load_dwordx4 v1, s[56:59], s72 offen lds\n"
    "s_add_u32 m0, s40, 25600\n"
    "s_mov_b32 s72, 2752512\n"
    "buffer_load_dwordx4 v1, s[56:59], s72 offen lds\n"
    "s_add_u32 m0, s40, 26624\n"
    "s_mov_b32 s72, 2883584\n"
    "buffer_load_dwordx4 v1, s[56:59], s72 offen lds\n"
    "s_add_u32 m0, s40, 27648\n"
    "s_mov_b32 s72, 3014656\n"
    "buffer_load_dwordx4 v1, s[56:59], s72 offen lds\n"
    "s_waitcnt vmcnt(32)\n"
    "ds_read_b64_tr_b16 a[112:113], v2 offset:28672\n"
    "ds_read_b64_tr_b16 a[114:115], v2 offset:28928\n"
    "ds_read_b64_tr_b16 a[116:117], v2 offset:30720\n"
    "ds_read_b64_tr_b16 a[118:119], v2 offset:30976\n"
    "ds_read_b64_tr_b16 a[120:121], v3 offset:28672\n"
    "ds_read_b64_tr_b16 a[122:123], v3 offset:28928\n"
    "ds_read_b64_tr_b16 a[124:125], v3 offset:30720\n"
    "ds_read_b64_tr_b16 a[126:127], v3 offset:30976\n"
    "s_waitcnt lgkmcnt(0)\n"
    "s_add_u32 m0, s40, 28672\n"
    "s_mov_b32 s72, 3145728\n"
    "buffer_load_dwordx4 v1, s[56:59], s72 offen lds\n"
    "s_add_u32 m0, s40, 29696\n"
    "s_mov_b32 s72, 3276800\n"
    "buffer_load_dwordx4 v1, s[56:59], s72 offen lds\n"
    "s_add_u32 m0, s40, 30720\n"
    "s_mov_b32 s72, 3407872\n"
    "buffer_load_dwordx4 v1, s[56:59], s72 offen lds\n"
    "s_add_u32 m0, s40, 31744\n"
    "s_mov_b32 s72, 3538944\n"
    "buffer_load_dwordx4 v1, s[56:59], s72 offen lds\n"
    "s_waitcnt vmcnt(32)\n"
    "ds_read_b64_tr_b16 a[128:129], v2 offset:32768\n"
    "ds_read_b64_tr_b16 a[130:131], v2 offset:33024\n"
    "ds_read_b64_tr_b16 a[132:133], v2 offset:34816\n"
    "ds_read_b64_tr_b16 a[134:135], v2 offset:35072\n"
    "ds_read_b64_tr_b16 a[136:137], v3 offset:32768\n"
    "ds_read_b64_tr_b16 a[138:139], v3 offset:33024\n"
    "ds_read_b64_tr_b16 a[140:141], v3 offset:34816\n"
    "ds_read_b64_tr_b16 a[142:143], v3 offset:35072\n"
    "s_waitcnt lgkmcnt(0)\n"
    "s_add_u32 m0, s40, 32768\n"
    "s_mov_b32 s72, 3670016\n"
    "buffer_load_dwordx4 v1, s[56:59], s72 offen lds\n"
    "s_add_u32 m0, s40, 33792\n"
    "s_mov_b32 s72, 3801088\n"
    "buffer_load_dwordx4 v1, s[56:59], s72 offen lds\n"
    "s_add_u32 m0, s40, 34816\n"
    "s_mov_b32 s72, 3932160\n"
    "buffer_load_dwordx4 v1, s[56:59], s72 offen lds\n"
    "s_add_u32 m0, s40, 35840\n"
    "s_mov_b32 s72, 4063232\n"
    "buffer_load_dwordx4 v1, s[56:59], s72 offen lds\n"
    "s_waitcnt vmcnt(32)\n"
    "ds_read_b64_tr_b16 a[144:145], v2 offset:0\n"
    "ds_read_b64_tr_b16 a[146:147], v2 offset:256\n"
    "ds_read_b64_tr_b16 a[148:149], v2 offset:2048\n"
    "ds_read_b64_tr_b16 a[150:151], v2 offset:2304\n"
    "ds_read_b64_tr_b16 a[152:153], v3 offset:0\n"
    "ds_read_b64_tr_b16 a[154:155], v3 offset:256\n"
    "ds_read_b64_tr_b16 a[156:157], v3 offset:2048\n"
    "ds_read_b64_tr_b16 a[158:159], v3 offset:2304\n"
    "s_waitcnt lgkmcnt(0)\n"
    "s_add_u32 m0, s40, 0\n"
    "s_mov_b32 s72, 4194304\n"
    "buffer_load_dwordx4 v1, s[56:59], s72 offen lds\n"
    "s_add_u32 m0, s40, 1024\n"
    "s_mov_b32 s72, 4325376\n"
    "buffer_load_dwordx4 v1, s[56:59], s72 offen lds\n"
    "s_add_u32 m0, s40, 2048\n"
    "s_mov_b32 s72, 4456448\n"
    "buffer_load_dwordx4 v1, s[56:59], s72 offen lds\n"
    "s_add_u32 m0, s40, 3072\n"
    "s_mov_b32 s72, 4587520\n"
    "buffer_load_dwordx4 v1, s[56:59], s72 offen lds\n"
    "s_waitcnt vmcnt(32)\n"
    "ds_read_b64_tr_b16 a[160:161], v2 offset:4096\n"
    "ds_read_b64_tr_b16 a[162:163], v2 offset:4352\n"
    "ds_read_b64_tr_b16 a[164:165], v2 offset:6144\n"
    "ds_read_b64_tr_b16 a[166:167], v2 offset:6400\n"
    "ds_read_b64_tr_b16 a[168:169], v3 offset:4096\n"
    "ds_read_b64_tr_b16 a[170:171], v3 offset:4352\n"
    "ds_read_b64_tr_b16 a[172:173], v3 offset:6144\n"
    "ds_read_b64_tr_b16 a[174:175], v3 offset:6400\n"
    "s_waitcnt lgkmcnt(0)\n"
    "s_add_u32 m0, s40, 4096\n"
    "s_mov_b32 s72, 4718592\n"
    "buffer_load_dwordx4 v1, s[56:59], s72 offen lds\n"
    "s_add_u32 m0, s40, 5120\n"
    "s_mov_b32 s72, 4849664\n"
    "buffer_load_dwordx4 v1, s[56:59], s72 offen lds\n"
    "s_add_u32 m0, s40, 6144\n"
    "s_mov_b32 s72, 4980736\n"
    "buffer_load_dwordx4 v1, s[56:59], s72 offen lds\n"
    "s_add_u32 m0, s40, 7168\n"
    "s_mov_b32 s72, 5111808\n"
    "buffer_load_dwordx4 v1, s[56:59], s72 offen lds\n"
    "s_waitcnt vmcnt(32)\n"
    "ds_read_b64_tr_b16 a[176:177], v2 offset:8192\n"
    "ds_read_b64_tr_b16 a[178:179], v2 offset:8448\n"
    "ds_read_b64_tr_b16 a[180:181], v2 offset:10240\n"
    "ds_read_b64_tr_b16 a[182:183], v2 offset:10496\n"
    "ds_read_b64_tr_b16 a[184:185], v3 offset:8192\n"
    "ds_read_b64_tr_b16 a[186:187], v3 offset:8448\n"
    "ds_read_b64_tr_b16 a[188:189], v3 offset:10240\n"
    "ds_read_b64_tr_b16 a[190:191], v3 offset:10496\n"
    "s_waitcnt vmcnt(28)\n"
    "ds_read_b64_tr_b16 a[192:193], v2 offset:12288\n"
    "ds_read_b64_tr_b16 a[194:195], v2 offset:12544\n"
    "ds_read_b64_tr_b16 a[196:197], v2 offset:14336\n"
    "ds_read_b64_tr_b16 a[198:199], v2 offset:14592\n"
    "ds_read_b64_tr_b16 a[200:201], v3 offset:12288\n"
    "ds_read_b64_tr_b16 a[202:203], v3 offset:12544\n"
    "s_waitcnt lgkmcnt(7)\n"
    "ds_read_b64_tr_b16 a[204:205], v3 offset:14336\n"
    "ds_read_b64_tr_b16 a[206:207], v3 offset:14592\n"
    "s_waitcnt vmcnt(24)\n"
    "ds_read_b64_tr_b16 a[208:209], v2 offset:16384\n"
    "ds_read_b64_tr_b16 a[210:211], v2 offset:16640\n"
    "ds_read_b64_tr_b16 a[212:213], v2 offset:18432\n"
    "ds_read_b64_tr_b16 a[214:215], v2 offset:18688\n"
    "ds_read_b64_tr_b16 a[216:217], v3 offset:16384\n"
    "s_waitcnt lgkmcnt(7)\n"
    "ds_read_b64_tr_b16 a[218:219], v3 offset:16640\n"
    "ds_read_b64_tr_b16 a[220:221], v3 offset:18432\n"
    "ds_read_b64_tr_b16 a[222:223], v3 offset:18688\n"
    "s_waitcnt vmcnt(20)\n"
    "ds_read_b64_tr_b16 a[224:225], v2 offset:20480\n"
    "ds_read_b64_tr_b16 a[226:227], v2 offset:20736\n"
    "ds_read_b64_tr_b16 a[228:229], v2 offset:22528\n"
    "ds_read_b64_tr_b16 a[230:231], v2 offset:22784\n"
    "s_waitcnt lgkmcnt(7)\n"
    "ds_read_b64_tr_b16 a[232:233], v3 offset:20480\n"
    "ds_read_b64_tr_b16 a[234:235], v3 offset:20736\n"
    "ds_read_b64_tr_b16 a[236:237], v3 offset:22528\n"
    "ds_read_b64_tr_b16 a[238:239], v3 offset:22784\n"
    "s_waitcnt vmcnt(16)\n"
    "ds_read_b64_tr_b16 a[240:241], v2 offset:24576\n"
    "ds_read_b64_tr_b16 a[242:243], v2 offset:24832\n"
    "ds_read_b64_tr_b16 a[244:245], v2 offset:26624\n"
    "s_waitcnt lgkmcnt(7)\n"
    "ds_read_b64_tr_b16 a[246:247], v2 offset:26880\n"
    "ds_read_b64_tr_b16 a[248:249], v3 offset:24576\n"
    "ds_read_b64_tr_b16 a[250:251], v3 offset:24832\n"
    "ds_read_b64_tr_b16 a[252:253], v3 offset:26624\n"
    "ds_read_b64_tr_b16 a[254:255], v3 offset:26880\n"
    "s_waitcnt vmcnt(12)\n"
    "ds_read_b64_tr_b16 v[192:193], v2 offset:28672\n"
    "ds_read_b64_tr_b16 v[194:195], v2 offset:28928\n"
    "s_waitcnt lgkmcnt(7)\n"
    "ds_read_b64_tr_b16 v[196:197], v2 offset:30720\n"
    "ds_read_b64_tr_b16 v[198:199], v2 offset:30976\n"
    "ds_read_b64_tr_b16 v[200:201], v3 offset:28672\n"
    "ds_read_b64_tr_b16 v[202:203], v3 offset:28928\n"
    "ds_read_b64_tr_b16 v[204:205], v3 offset:30720\n"
    "ds_read_b64_tr_b16 v[206:207], v3 offset:30976\n"
    "s_waitcnt vmcnt(8)\n"
    "ds_read_b64_tr_b16 v[208:209], v2 offset:32768\n"
    "s_waitcnt lgkmcnt(7)\n"
    "ds_read_b64_tr_b16 v[210:211], v2 offset:33024\n"
    "ds_read_b64_tr_b16 v[212:213], v2 offset:34816\n"
    "ds_read_b64_tr_b16 v[214:215], v2 offset:35072\n"
    "ds_read_b64_tr_b16 v[216:217], v3 offset:32768\n"
    "ds_read_b64_tr_b16 v[218:219], v3 offset:33024\n"
    "ds_read_b64_tr_b16 v[220:221], v3 offset:34816\n"
    "ds_read_b64_tr_b16 v[222:223], v3 offset:35072\n"
    "s_waitcnt vmcnt(4)\n"
    "s_waitcnt lgkmcnt(7)\n"
    "ds_read_b64_tr_b16 v[224:225], v2 offset:0\n"
    "ds_read_b64_tr_b16 v[226:227], v2 offset:256\n"
    "ds_read_b64_tr_b16 v[228:229], v2 offset:2048\n"
    "ds_read_b64_tr_b16 v[230:231], v2 offset:2304\n"
    "ds_read_b64_tr_b16 v[232:233], v3 offset:0\n"
    "ds_read_b64_tr_b16 v[234:235], v3 offset:256\n"
    "ds_read_b64_tr_b16 v[236:237], v3 offset:2048\n"
    "s_waitcnt lgkmcnt(7)\n"
    "ds_read_b64_tr_b16 v[238:239], v3 offset:2304\n"
    "s_waitcnt vmcnt(0)\n"
    "ds_read_b64_tr_b16 v[240:241], v2 offset:4096\n"
    "ds_read_b64_tr_b16 v[242:243], v2 offset:4352\n"
    "ds_read_b64_tr_b16 v[244:245], v2 offset:6144\n"
    "ds_read_b64_tr_b16 v[246:247], v2 offset:6400\n"
    "ds_read_b64_tr_b16 v[248:249], v3 offset:4096\n"
    "ds_read_b64_tr_b16 v[250:251], v3 offset:4352\n"
    "s_waitcnt lgkmcnt(7)\n"
    "ds_read_b64_tr_b16 v[252:253], v3 offset:6144\n"
    "ds_read_b64_tr_b16 v[254:255], v3 offset:6400\n"
    "s_waitcnt lgkmcnt(0)\n"
    "s_mov_b32 s73, 0\n"
    "s_mov_b32 s74, 0\n"
    "s_mov_b32 exec_hi, 1\n"
    "buffer_load_dwordx2 v[32:33], off, s[36:39], 0 sc1\n"
    "RK_ix_%=:\n"
    "buffer_load_dwordx2 v[34:35], v0, s[36:39], 0 offen offset:1024 sc1\n"
    "s_waitcnt vmcnt(0)\n"
    "v_sub_u32 v34, v34, v32\n"
    "v_sub_u32 v35, v35, v33\n"
    "v_subrev_u32 v34, 0x7f4a7c16, v34\n"
    "v_cmp_ne_u32 vcc, 0x9e3779b9, v35\n"
    "v_cmp_lt_u32 s[94:95], 1, v34\n"
    "s_nop 1\n"
    "s_or_b64 vcc, vcc, s[94:95]\n"
    "s_cbranch_scc0 RK_jx_%=\n"
    "s_cmp_lg_u32 s73, 0\n"
    "s_cbranch_scc1 RK_jx_%=\n"
    "s_sleep 4\n"
    "s_add_u32 s74, s74, 1\n"
    "s_cmp_lt_u32 s74, 0x40000\n"
    "s_cbranch_scc1 RK_ix_%=\n"
    "s_mov_b32 s73, 1\n"
    "RK_jx_%=:\n"
    "s_mov_b64 exec, -1\n"
    "s_barrier\n"
    "s_mov_b32 s69, s45\n"
    "s_add_u32 s70, s45, 40960\n"
    "s_add_u32 s71, s45, 81920\n"
    "s_add_u32 m0, s69, 0\n"
    "s_add_u32 s73, s41, 0\n"
    "buffer_load_dwordx4 v0, s[36:39], s73 offen offset:0 sc1 lds\n"
    "buffer_load_dwordx4 v0, s[36:39], s73 offen offset:1024 sc1 lds\n"
    "buffer_load_dwordx4 v0, s[36:39], s73 offen offset:2048 sc1 lds\n"
    "buffer_load_dwordx4 v0, s[36:39], s73 offen offset:3072 sc1 lds\n"
    "s_add_u32 m0, s69, 4096\n"
    "s_add_u32 s73, s41, 4096\n"
    "buffer_load_dwordx4 v0, s[36:39], s73 offen offset:0 sc1 lds\n"
    "buffer_load_dwordx4 v0, s[36:39], s73 offen offset:1024 sc1 lds\n"
    "buffer_load_dwordx4 v0, s[36:39], s73 offen offset:2048 sc1 lds\n"
    "buffer_load_dwordx4 v0, s[36:39], s73 offen offset:3072 sc1 lds\n"
    "s_add_u32 m0, s69, 8192\n"
    "s_add_u32 s73, s41, 8192\n"
    "buffer_load_dwordx4 v0, s[36:39], s73 offen offset:0 sc1 lds\n"
    "buffer_load_dwordx4 v0, s[36:39], s73 offen offset:1024 sc1 lds\n"
    "s_add_u32 s41, s41, 122880\n"
    "s_add_u32 m0, s70, 0\n"
    "s_add_u32 s73, s41, 0\n"
    "buffer_load_dwordx4 v0, s[36:39], s73 offen offset:0 sc1 lds\n"
    "buffer_load_dwordx4 v0, s[36:39], s73 offen offset:1024 sc1 lds\n"
    "buffer_load_dwordx4 v0, s[36:39], s73 offen offset:2048 sc1 lds\n"
    "buffer_load_dwordx4 v0, s[36:39], s73 offen offset:3072 sc1 lds\n"
    "s_add_u32 m0, s70, 4096\n"
    "s_add_u32 s73, s41, 4096\n"
    "buffer_load_dwordx4 v0, s[36:39], s73 offen offset:0 sc1 lds\n"
    "buffer_load_dwordx4 v0, s[36:39], s73 offen offset:1024 sc1 lds\n"
    "buffer_load_dwordx4 v0, s[36:39], s73 offen offset:2048 sc1 lds\n"
    "buffer_load_dwordx4 v0, s[36:39], s73 offen offset:3072 sc1 lds\n"
    "s_add_u32 m0, s70, 8192\n"
    "s_add_u32 s73, s41, 8192\n"
    "buffer_load_dwordx4 v0, s[36:39], s73 offen offset:0 sc1 lds\n"
    "buffer_load_dwordx4 v0, s[36:39], s73 offen offset:1024 sc1 lds\n"
    "s_add_u32 s41, s41, 122880\n"
    "s_mov_b32 s68, s44\n"
    "v_mov_b32 v17, 0\n"
    "s_waitcnt vmcnt(10)\n"
    "RK_loop_%=:\n"
    "s_sub_u32 s72, s69, s45\n"
    "v_add_u32 v16, s72, v0\n"
    "s_barrier\n"
    "ds_read_b128 v[32:35], v16 offset:0\n"
    "ds_read_b128 v[36:39], v16 offset:20480\n"
    "ds_read_b128 v[40:43], v16 offset:1024\n"
    "ds_read_b128 v[44:47], v16 offset:21504\n"
    "ds_read_b128 v[48:51], v16 offset:2048\n"
    "ds_read_b128 v[52:55], v16 offset:22528\n"
    "s_waitcnt lgkmcnt(5)\n"
    "v_mfma_f32_16x16x32_bf16 v[64:67], a[0:3], v[32:35], 0\n"
    "ds_read_b128 v[56:59], v16 offset:3072\n"
    "v_mfma_f32_16x16x32_bf16 v[68:71], a[8:11], v[32:35], 0\n"
    "v_mfma_f32_16x16x32_bf16 v[80:83], v[32:35], a[160:163], 0\n"
    "v_mfma_f32_16x16x32_bf16 v[88:91], v[32:35], a[168:171], 0\n"
    "s_waitcnt lgkmcnt(5)\n"
    "v_mfma_f32_16x16x32_bf16 v[72:75], a[0:3], v[36:39], 0\n"
    "ds_read_b128 v[60:63], v16 offset:23552\n"
    "v_mfma_f32_16x16x32_bf16 v[76:79], a[8:11], v[36:39], 0\n"
    "v_mfma_f32_16x16x32_bf16 v[84:87], v[36:39], a[160:163], 0\n"
    "s_add_u32 m0, s71, 0\n"
    "s_add_u32 s73, s41, 0\n"
    "buffer_load_dwordx4 v0, s[36:39], s73 offen offset:0 sc1 lds\n"
    "v_mfma_f32_16x16x32_bf16 v[92:95], v[36:39], a[168:171], 0\n"
    "s_waitcnt lgkmcnt(5)\n"
    "v_mfma_f32_16x16x32_bf16 v[64:67], a[4:7], v[40:43], v[64:67]\n"
    "ds_read_b128 v[32:35], v16 offset:4096\n"
    "v_mfma_f32_16x16x32_bf16 v[68:71], a[12:15], v[40:43], v[68:71]\n"
    "v_mfma_f32_16x16x32_bf16 v[80:83], v[40:43], a[164:167], v[80:83]\n"
    "v_mfma_f32_16x16x32_bf16 v[88:91], v[40:43], a[172:175], v[88:91]\n"
    "s_waitcnt lgkmcnt(5)\n"
    "v_mfma_f32_16x16x32_bf16 v[72:75], a[4:7], v[44:47], v[72:75]\n"
    "ds_read_b128 v[36:39], v16 offset:24576\n"
    "v_mfma_f32_16x16x32_bf16 v[76:79], a[12:15], v[44:47], v[76:79]\n"
    "v_mfma_f32_16x16x32_bf16 v[84:87], v[44:47], a[164:167], v[84:87]\n"
    "v_mfma_f32_16x16x32_bf16 v[92:95], v[44:47], a[172:175], v[92:95]\n"
    "s_waitcnt lgkmcnt(5)\n"
    "v_mfma_f32_16x16x32_bf16 v[64:67], a[16:19], v[48:51], v[64:67]\n"
    "ds_read_b128 v[40:43], v16 offset:5120\n"
    "v_mfma_f32_16x16x32_bf16 v[68:71], a[24:27], v[48:51], v[68:71]\n"
    "v_mfma_f32_16x16x32_bf16 v[80:83], v[48:51], a[176:179], v[80:83]\n"
    "buffer_load_dwordx4 v0, s[36:39], s73 offen offset:1024 sc1 lds\n"
    "v_mfma_f32_16x16x32_bf16 v[88:91], v[48:51], a[184:187], v[88:91]\n"
    "s_waitcnt lgkmcnt(5)\n"
    "v_mfma_f32_16x16x32_bf16 v[72:75], a[16:19], v[52:55], v[72:75]\n"
    "ds_read_b128 v[44:47], v16 offset:25600\n"
    "v_mfma_f32_16x16x32_bf16 v[76:79], a[24:27], v[52:55], v[76:79]\n"
    "v_mfma_f32_16x16x32_bf16 v[84:87], v[52:55], a[176:179], v[84:87]\n"
    "v_mfma_f32_16x16x32_bf16 v[92:95], v[52:55], a[184:187], v[92:95]\n"
    "s_waitcnt lgkmcnt(5)\n"
    "v_mfma_f32_16x16x32_bf16 v[64:67], a[20:23], v[56:59], v[64:67]\n"
    "ds_read_b128 v[48:51], v16 offset:6144\n"
    "v_mfma_f32_16x16x32_bf16 v[68:71], a[28:31], v[56:59], v[68:71]\n"
    "v_mfma_f32_16x16x32_bf16 v[80:83], v[56:59], a[180:183], v[80:83]\n"
    "v_mfma_f32_16x16x32_bf16 v[88:91], v[56:59], a[188:191], v[88:91]\n"
    "s_waitcnt lgkmcnt(5)\n"
    "v_mfma_f32_16x16x32_bf16 v[72:75], a[20:23], v[60:63], v[72:75]\n"
    "ds_read_b128 v[52:55], v16 offset:26624\n"
    "v_mfma_f32_16x16x32_bf16 v[76:79], a[28:31], v[60:63], v[76:79]\n"
    "v_mfma_f32_16x16x32_bf16 v[84:87], v[60:63], a[180:183], v[84:87]\n"
    "buffer_load_dwordx4 v0, s[36:39], s73 offen offset:2048 sc1 lds\n"
    "v_mfma_f32_16x16x32_bf16 v[92:95], v[60:63], a[188:191], v[92:95]\n"
    "s_waitcnt lgkmcnt(5)\n"
    "v_mfma_f32_16x16x32_bf16 v[64:67], a[32:35], v[32:35], v[64:67]\n"
    "ds_read_b128 v[56:59], v16 offset:7168\n"
    "v_mfma_f32_16x16x32_bf16 v[68:71], a[40:43], v[32:35], v[68:71]\n"
    "v_mfma_f32_16x16x32_bf16 v[80:83], v[32:35], a[192:195], v[80:83]\n"
    "v_mfma_f32_16x16x32_bf16 v[88:91], v[32:35], a[200:203], v[88:91]\n"
    "s_waitcnt lgkmcnt(5)\n"
    "v_mfma_f32_16x16x32_bf16 v[72:75], a[32:35], v[36:39], v[72:75]\n"
    "ds_read_b128 v[60:63], v16 offset:27648\n"
    "v_mfma_f32_16x16x32_bf16 v[76:79], a[40:43], v[36:39], v[76:79]\n"
    "v_mfma_f32_16x16x32_bf16 v[84:87], v[36:39], a[192:195], v[84:87]\n"
    "v_mfma_f32_16x16x32_bf16 v[92:95], v[36:39], a[200:203], v[92:95]\n"
    "s_waitcnt lgkmcnt(5)\n"
    "v_mfma_f32_16x16x32_bf16 v[64:67], a[36:39], v[40:43], v[64:67]\n"
    "ds_read_b128 v[32:35], v16 offset:8192\n"
    "v_mfma_f32_16x16x32_bf16 v[68:71], a[44:47], v[40:43], v[68:71]\n"
    "v_mfma_f32_16x16x32_bf16 v[80:83], v[40:43], a[196:199], v[80:83]\n"
    "buffer_load_dwordx4 v0, s[36:39], s73 offen offset:3072 sc1 lds\n"
    "v_mfma_f32_16x16x32_bf16 v[88:91], v[40:43], a[204:207], v[88:91]\n"
    "s_waitcnt lgkmcnt(5)\n"
    "v_mfma_f32_16x16x32_bf16 v[72:75], a[36:39], v[44:47], v[72:75]\n"
    "ds_read_b128 v[36:39], v16 offset:28672\n"
    "v_mfma_f32_16x16x32_bf16 v[76:79], a[44:47], v[44:47], v[76:79]\n"
    "v_mfma_f32_16x16x32_bf16 v[84:87], v[44:47], a[196:199], v[84:87]\n"
    "v_mfma_f32_16x16x32_bf16 v[92:95], v[44:47], a[204:207], v[92:95]\n"
    "s_waitcnt lgkmcnt(5)\n"
    "v_mfma_f32_16x16x32_bf16 v[64:67], a[48:51], v[48:51], v[64:67]\n"
    "ds_read_b128 v[40:43], v16 offset:9216\n"
    "v_mfma_f32_16x16x32_bf16 v[68:71], a[56:59], v[48:51], v[68:71]\n"
    "v_mfma_f32_16x16x32_bf16 v[80:83], v[48:51], a[208:211], v[80:83]\n"
    "v_mfma_f32_16x16x32_bf16 v[88:91], v[48:51], a[216:219], v[88:91]\n"
    "s_waitcnt lgkmcnt(5)\n"
    "v_mfma_f32_16x16x32_bf16 v[72:75], a[48:51], v[52:55], v[72:75]\n"
    "ds_read_b128 v[44:47], v16 offset:29696\n"
    "v_mfma_f32_16x16x32_bf16 v[76:79], a[56:59], v[52:55], v[76:79]\n"
    "v_mfma_f32_16x16x32_bf16 v[84:87], v[52:55], a[208:211], v[84:87]\n"
    "s_add_u32 m0, s71, 4096\n"
    "s_add_u32 s73, s41, 4096\n"
    "buffer_load_dwordx4 v0, s[36:39], s73 offen offset:0 sc1 lds\n"
    "v_mfma_f32_16x16x32_bf16 v[92:95], v[52:55], a[216:219], v[92:95]\n"
    "s_waitcnt lgkmcnt(5)\n"
    "v_mfma_f32_16x16x32_bf16 v[64:67], a[52:55], v[56:59], v[64:67]\n"
    "ds_read_b128 v[48:51], v16 offset:10240\n"
    "v_mfma_f32_16x16x32_bf16 v[68:71], a[60:63], v[56:59], v[68:71]\n"
    "v_mfma_f32_16x16x32_bf16 v[80:83], v[56:59], a[212:215], v[80:83]\n"
    "v_mfma_f32_16x16x32_bf16 v[88:91], v[56:59], a[220:223], v[88:91]\n"
    "s_waitcnt lgkmcnt(5)\n"
    "v_mfma_f32_16x16x32_bf16 v[72:75], a[52:55], v[60:63], v[72:75]\n"
    "ds_read_b128 v[52:55], v16 offset:30720\n"
    "v_mfma_f32_16x16x32_bf16 v[76:79], a[60:63], v[60:63], v[76:79]\n"
    "v_mfma_f32_16x16x32_bf16 v[84:87], v[60:63], a[212:215], v[84:87]\n"
    "v_mfma_f32_16x16x32_bf16 v[92:95], v[60:63], a[220:223], v[92:95]\n"
    "s_waitcnt lgkmcnt(5)\n"
    "v_mfma_f32_16x16x32_bf16 v[64:67], a[64:67], v[32:35], v[64:67]\n"
    "ds_read_b128 v[56:59], v16 offset:11264\n"
    "v_mfma_f32_16x16x32_bf16 v[68:71], a[72:75], v[32:35], v[68:71]\n"
    "v_mfma_f32_16x16x32_bf16 v[80:83], v[32:35], a[224:227], v[80:83]\n"
    "buffer_load_dwordx4 v0, s[36:39], s73 offen offset:1024 sc1 lds\n"
    "v_mfma_f32_16x16x32_bf16 v[88:91], v[32:35], a[232:235], v[88:91]\n"
    "s_waitcnt lgkmcnt(5)\n"
    "v_mfma_f32_16x16x32_bf16 v[72:75], a[64:67], v[36:39], v[72:75]\n"
    "ds_read_b128 v[60:63], v16 offset:31744\n"
    "v_mfma_f32_16x16x32_bf16 v[76:79], a[72:75], v[36:39], v[76:79]\n"
    "v_mfma_f32_16x16x32_bf16 v[84:87], v[36:39], a[224:227], v[84:87]\n"
    "v_mfma_f32_16x16x32_bf16 v[92:95], v[36:39], a[232:235], v[92:95]\n"
    "s_waitcnt lgkmcnt(5)\n"
    "v_mfma_f32_16x16x32_bf16 v[64:67], a[68:71], v[40:43], v[64:67]\n"
    "ds_read_b128 v[32:35], v16 offset:12288\n"
    "v_mfma_f32_16x16x32_bf16 v[68:71], a[76:79], v[40:43], v[68:71]\n"
    "v_mfma_f32_16x16x32_bf16 v[80:83], v[40:43], a[228:231], v[80:83]\n"
    "v_mfma_f32_16x16x32_bf16 v[88:91], v[40:43], a[236:239], v[88:91]\n"
    "s_waitcnt lgkmcnt(5)\n"
    "v_mfma_f32_16x16x32_bf16 v[72:75], a[68:71], v[44:47], v[72:75]\n"
    "ds_read_b128 v[36:39], v16 offset:32768\n"
    "v_mfma_f32_16x16x32_bf16 v[76:79], a[76:79], v[44:47], v[76:79]\n"
    "v_mfma_f32_16x16x32_bf16 v[84:87], v[44:47], a[228:231], v[84:87]\n"
    "buffer_load_dwordx4 v0, s[36:39], s73 offen offset:2048 sc1 lds\n"
    "v_mfma_f32_16x16x32_bf16 v[92:95], v[44:47], a[236:239], v[92:95]\n"
    "s_waitcnt lgkmcnt(5)\n"
    "v_mfma_f32_16x16x32_bf16 v[64:67], a[80:83], v[48:51], v[64:67]\n"
    "ds_read_b128 v[40:43], v16 offset:13312\n"
    "v_mfma_f32_16x16x32_bf16 v[68:71], a[88:91], v[48:51], v[68:71]\n"
    "v_mfma_f32_16x16x32_bf16 v[80:83], v[48:51], a[240:243], v[80:83]\n"
    "v_mfma_f32_16x16x32_bf16 v[88:91], v[48:51], a[248:251], v[88:91]\n"
    "s_waitcnt lgkmcnt(5)\n"
    "v_mfma_f32_16x16x32_bf16 v[72:75], a[80:83], v[52:55], v[72:75]\n"
    "ds_read_b128 v[44:47], v16 offset:33792\n"
    "v_mfma_f32_16x16x32_bf16 v[76:79], a[88:91], v[52:55], v[76:79]\n"
    "v_mfma_f32_16x16x32_bf16 v[84:87], v[52:55], a[240:243], v[84:87]\n"
    "s_waitcnt vmcnt(7)\n"
    "s_barrier\n"
    "buffer_store_dword v17, v4, s[36:39], 0 offen sc1\n"
    "v_mfma_f32_16x16x32_bf16 v[92:95], v[52:55], a[248:251], v[92:95]\n"
    "s_waitcnt lgkmcnt(5)\n"
    "v_mfma_f32_16x16x32_bf16 v[64:67], a[84:87], v[56:59], v[64:67]\n"
    "ds_read_b128 v[48:51], v16 offset:14336\n"
    "v_mfma_f32_16x16x32_bf16 v[68:71], a[92:95], v[56:59], v[68:71]\n"
    "v_mfma_f32_16x16x32_bf16 v[80:83], v[56:59], a[244:247], v[80:83]\n"
    "buffer_load_dwordx4 v0, s[36:39], s73 offen offset:3072 sc1 lds\n"
    "v_mfma_f32_16x16x32_bf16 v[88:91], v[56:59], a[252:255], v[88:91]\n"
    "s_waitcnt lgkmcnt(5)\n"
    "v_mfma_f32_16x16x32_bf16 v[72:75], a[84:87], v[60:63], v[72:75]\n"
    "ds_read_b128 v[52:55], v16 offset:34816\n"
    "v_mfma_f32_16x16x32_bf16 v[76:79], a[92:95], v[60:63], v[76:79]\n"
    "v_mfma_f32_16x16x32_bf16 v[84:87], v[60:63], a[244:247], v[84:87]\n"
    "v_mfma_f32_16x16x32_bf16 v[92:95], v[60:63], a[252:255], v[92:95]\n"
    "s_waitcnt lgkmcnt(5)\n"
    "v_mfma_f32_16x16x32_bf16 v[64:67], a[96:99], v[32:35], v[64:67]\n"
    "ds_read_b128 v[56:59], v16 offset:15360\n"
    "v_mfma_f32_16x16x32_bf16 v[68:71], a[104:107], v[32:35], v[68:71]\n"
    "v_mfma_f32_16x16x32_bf16 v[80:83], v[32:35], v[192:195], v[80:83]\n"
    "v_mfma_f32_16x16x32_bf16 v[88:91], v[32:35], v[200:203], v[88:91]\n"
    "s_waitcnt lgkmcnt(5)\n"
    "v_mfma_f32_16x16x32_bf16 v[72:75], a[96:99], v[36:39], v[72:75]\n"
    "ds_read_b128 v[60:63], v16 offset:35840\n"
    "v_mfma_f32_16x16x32_bf16 v[76:79], a[104:107], v[36:39], v[76:79]\n"
    "v_mfma_f32_16x16x32_bf16 v[84:87], v[36:39], v[192:195], v[84:87]\n"
    "s_add_u32 m0, s71, 8192\n"
    "s_add_u32 s73, s41, 8192\n"
    "buffer_load_dwordx4 v0, s[36:39], s73 offen offset:0 sc1 lds\n"
    "v_mfma_f32_16x16x32_bf16 v[92:95], v[36:39], v[200:203], v[92:95]\n"
    "s_waitcnt lgkmcnt(5)\n"
    "v_mfma_f32_16x16x32_bf16 v[64:67], a[100:103], v[40:43], v[64:67]\n"
    "ds_read_b128 v[32:35], v16 offset:16384\n"
    "v_mfma_f32_16x16x32_bf16 v[68:71], a[108:111], v[40:43], v[68:71]\n"
    "v_mfma_f32_16x16x32_bf16 v[80:83], v[40:43], v[196:199], v[80:83]\n"
    "v_mfma_f32_16x16x32_bf16 v[88:91], v[40:43], v[204:207], v[88:91]\n"
    "s_waitcnt lgkmcnt(5)\n"
    "v_mfma_f32_16x16x32_bf16 v[72:75], a[100:103], v[44:47], v[72:75]\n"
    "ds_read_b128 v[36:39], v16 offset:36864\n"
    "v_mfma_f32_16x16x32_bf16 v[76:79], a[108:111], v[44:47], v[76:79]\n"
    "v_mfma_f32_16x16x32_bf16 v[84:87], v[44:47], v[196:199], v[84:87]\n"
    "v_mfma_f32_16x16x32_bf16 v[92:95], v[44:47], v[204:207], v[92:95]\n"
    "s_waitcnt lgkmcnt(5)\n"
    "v_mfma_f32_16x16x32_bf16 v[64:67], a[112:115], v[48:51], v[64:67]\n"
    "ds_read_b128 v[40:43], v16 offset:17408\n"
    "v_mfma_f32_16x16x32_bf16 v[68:71], a[120:123], v[48:51], v[68:71]\n"
    "v_mfma_f32_16x16x32_bf16 v[80:83], v[48:51], v[208:211], v[80:83]\n"
    "buffer_load_dwordx4 v0, s[36:39], s73 offen offset:1024 sc1 lds\n"
    "v_mfma_f32_16x16x32_bf16 v[88:91], v[48:51], v[216:219], v[88:91]\n"
    "s_waitcnt lgkmcnt(5)\n"
    "v_mfma_f32_16x16x32_bf16 v[72:75], a[112:115], v[52:55], v[72:75]\n"
    "ds_read_b128 v[44:47], v16 offset:37888\n"
    "v_mfma_f32_16x16x32_bf16 v[76:79], a[120:123], v[52:55], v[76:79]\n"
    "v_mfma_f32_16x16x32_bf16 v[84:87], v[52:55], v[208:211], v[84:87]\n"
    "v_mfma_f32_16x16x32_bf16 v[92:95], v[52:55], v[216:219], v[92:95]\n"
    "s_waitcnt lgkmcnt(5)\n"
    "v_mfma_f32_16x16x32_bf16 v[64:67], a[116:119], v[56:59], v[64:67]\n"
    "ds_read_b128 v[48:51], v16 offset:18432\n"
    "v_mfma_f32_16x16x32_bf16 v[68:71], a[124:127], v[56:59], v[68:71]\n"
    "v_mfma_f32_16x16x32_bf16 v[80:83], v[56:59], v[212:215], v[80:83]\n"
    "v_mfma_f32_16x16x32_bf16 v[88:91], v[56:59], v[220:223], v[88:91]\n"
    "s_waitcnt lgkmcnt(5)\n"
    "v_mfma_f32_16x16x32_bf16 v[72:75], a[116:119], v[60:63], v[72:75]\n"
    "ds_read_b128 v[52:55], v16 offset:38912\n"
    "v_mfma_f32_16x16x32_bf16 v[76:79], a[124:127], v[60:63], v[76:79]\n"
    "v_mfma_f32_16x16x32_bf16 v[84:87], v[60:63], v[212:215], v[84:87]\n"
    "v_mfma_f32_16x16x32_bf16 v[92:95], v[60:63], v[220:223], v[92:95]\n"
    "s_waitcnt lgkmcnt(5)\n"
    "v_mfma_f32_16x16x32_bf16 v[64:67], a[128:131], v[32:35], v[64:67]\n"
    "ds_read_b128 v[56:59], v16 offset:19456\n"
    "v_mfma_f32_16x16x32_bf16 v[68:71], a[136:139], v[32:35], v[68:71]\n"
    "v_mfma_f32_16x16x32_bf16 v[80:83], v[32:35], v[224:227], v[80:83]\n"
    "v_mfma_f32_16x16x32_bf16 v[88:91], v[32:35], v[232:235], v[88:91]\n"
    "s_waitcnt lgkmcnt(5)\n"
    "v_mfma_f32_16x16x32_bf16 v[72:75], a[128:131], v[36:39], v[72:75]\n"
    "ds_read_b128 v[60:63], v16 offset:39936\n"
    "v_mfma_f32_16x16x32_bf16 v[76:79], a[136:139], v[36:39], v[76:79]\n"
    "v_mfma_f32_16x16x32_bf16 v[84:87], v[36:39], v[224:227], v[84:87]\n"
    "v_mfma_f32_16x16x32_bf16 v[92:95], v[36:39], v[232:235], v[92:95]\n"
    "s_waitcnt lgkmcnt(5)\n"
    "v_mfma_f32_16x16x32_bf16 v[64:67], a[132:135], v[40:43], v[64:67]\n"
    "v_mfma_f32_16x16x32_bf16 v[68:71], a[140:143], v[40:43], v[68:71]\n"
    "v_mfma_f32_16x16x32_bf16 v[80:83], v[40:43], v[228:231], v[80:83]\n"
    "v_mfma_f32_16x16x32_bf16 v[88:91], v[40:43], v[236:239], v[88:91]\n"
    "s_waitcnt lgkmcnt(4)\n"
    "v_mfma_f32_16x16x32_bf16 v[72:75], a[132:135], v[44:47], v[72:75]\n"
    "v_mfma_f32_16x16x32_bf16 v[76:79], a[140:143], v[44:47], v[76:79]\n"
    "v_mfma_f32_16x16x32_bf16 v[84:87], v[44:47], v[228:231], v[84:87]\n"
    "v_mfma_f32_16x16x32_bf16 v[92:95], v[44:47], v[236:239], v[92:95]\n"
    "s_waitcnt lgkmcnt(3)\n"
    "v_mfma_f32_16x16x32_bf16 v[64:67], a[144:147], v[48:51], v[64:67]\n"
    "v_mfma_f32_16x16x32_bf16 v[68:71], a[152:155], v[48:51], v[68:71]\n"
    "v_mfma_f32_16x16x32_bf16 v[80:83], v[48:51], v[240:243], v[80:83]\n"
    "s_mov_b32 m0, 122880\n"
    "s_add_u32 s47, s47, 0x20000\n"
    "buffer_load_dwordx4 v0, s[60:63], s47 offen lds\n"
    "v_mfma_f32_16x16x32_bf16 v[88:91], v[48:51], v[248:251], v[88:91]\n"
    "s_waitcnt lgkmcnt(2)\n"
    "v_mfma_f32_16x16x32_bf16 v[72:75], a[144:147], v[52:55], v[72:75]\n"
    "v_mfma_f32_16x16x32_bf16 v[76:79], a[152:155], v[52:55], v[76:79]\n"
    "v_mfma_f32_16x16x32_bf16 v[84:87], v[52:55], v[240:243], v[84:87]\n"
    "v_mfma_f32_16x16x32_bf16 v[92:95], v[52:55], v[248:251], v[92:95]\n"
    "s_waitcnt lgkmcnt(1)\n"
    "v_mfma_f32_16x16x32_bf16 v[64:67], a[148:151], v[56:59], v[64:67]\n"
    "v_mfma_f32_16x16x32_bf16 v[68:71], a[156:159], v[56:59], v[68:71]\n"
    "v_mfma_f32_16x16x32_bf16 v[80:83], v[56:59], v[244:247], v[80:83]\n"
    "v_mfma_f32_16x16x32_bf16 v[88:91], v[56:59], v[252:255], v[88:91]\n"
    "s_waitcnt lgkmcnt(0)\n"
    "v_mfma_f32_16x16x32_bf16 v[72:75], a[148:151], v[60:63], v[72:75]\n"
    "v_mfma_f32_16x16x32_bf16 v[76:79], a[156:159], v[60:63], v[76:79]\n"
    "v_mfma_f32_16x16x32_bf16 v[84:87], v[60:63], v[244:247], v[84:87]\n"
    "v_mfma_f32_16x16x32_bf16 v[92:95], v[60:63], v[252:255], v[92:95]\n"
    "v_cvt_pk_bf16_f32 v64, v64, v65\n"
    "v_cvt_pk_bf16_f32 v65, v66, v67\n"
    "v_cvt_pk_bf16_f32 v66, v68, v69\n"
    "v_cvt_pk_bf16_f32 v67, v70, v71\n"
    "buffer_store_dwordx4 v[64:67], v0, s[36:39], s42 offen sc1\n"
    "v_cvt_pk_bf16_f32 v72, v72, v73\n"
    "v_cvt_pk_bf16_f32 v73, v74, v75\n"
    "v_cvt_pk_bf16_f32 v74, v76, v77\n"
    "v_cvt_pk_bf16_f32 v75, v78, v79\n"
    "s_add_u32 s72, s42, 4096\n"
    "buffer_store_dwordx4 v[72:75], v0, s[36:39], s72 offen sc1\n"
    "v_cvt_pk_bf16_f32 v80, v80, v81\n"
    "v_cvt_pk_bf16_f32 v81, v82, v83\n"
    "v_cvt_pk_bf16_f32 v82, v84, v85\n"
    "v_cvt_pk_bf16_f32 v83, v86, v87\n"
    "buffer_store_dwordx4 v[80:83], v0, s[36:39], s43 offen offset:0 sc1\n"
    "v_cvt_pk_bf16_f32 v88, v88, v89\n"
    "v_cvt_pk_bf16_f32 v89, v90, v91\n"
    "v_cvt_pk_bf16_f32 v90, v92, v93\n"
    "v_cvt_pk_bf16_f32 v91, v94, v95\n"
    "buffer_store_dwordx4 v[88:91], v0, s[36:39], s43 offen offset:1024 sc1\n"
    "s_add_u32 s72, s68, 6\n"
    "s_cmp_lt_u32 s72, s48\n"
    "s_cselect_b32 s72, 3, 1\n"
    "s_mul_i32 s72, s72, 40960\n"
    "s_add_u32 s41, s41, s72\n"
    "s_cmp_lt_u32 s68, s48\n"
    "s_cselect_b32 s74, 3, 1\n"
    "s_lshl_b32 s72, s74, 18\n"
    "s_add_u32 s42, s42, s72\n"
    "s_add_u32 s43, s43, s72\n"
    "s_mov_b32 s72, s69\n"
    "s_mov_b32 s69, s70\n"
    "s_mov_b32 s70, s71\n"
    "s_mov_b32 s71, s72\n"
    "s_add_u32 s72, s68, 1\n"
    "v_mov_b32 v17, s72\n"
    "s_cmp_ge_u32 s68, 3\n"
    "s_cbranch_scc1 RK_nf_%=\n"
    "s_waitcnt vmcnt(0)\n"
    "s_barrier\n"
    "buffer_store_dword v17, v4, s[36:39], 0 offen sc1\n"
    "RK_nf_%=:\n"
    "s_add_u32 s68, s68, s74\n"
    "s_cmp_lt_u32 s68, s46\n"
    "s_waitcnt vmcnt(16)\n"
    "s_cbranch_scc1 RK_loop_%=\n"
    "s_waitcnt vmcnt(0)\n"
    "s_barrier\n"
    "v_mov_b32 v17, 0x100\n"
    "buffer_store_dword v17, v4, s[36:39], 0 offen sc1\n"
    "s_waitcnt vmcnt(0)\n"
            : "+{v[0:15]}"(C), "+{s[36:51]}"(SA), "+{s[52:67]}"(SB) :: CLOB_W);
    }
    if (xi < 3 && slot >= 10) {
        ROLE_BEGIN
        const int j = slot - 10, T0 = xi, ro = j & 1;
        const unsigned sd = read_side(p.ws), ne = comm_off(p.ws) + ((sd ^ 1) << 13), no = comm_off(p.ws) + (sd << 13);
        const int l = lane_id(), c = l & 15, g = l >> 4;
        v16u C = {}, SA = {}, SB = {};
        const unsigned stg = w * 36864;
        strip_consts(C, l, stg, 1280, true);
        const int ct = 3 * j + w;
        C[1] = ((l & 7) ^ ((l >> 3) & 6)) < 6 ? (l >> 3) * 1280 + ((l & 7) ^ ((l >> 3) & 6)) * 16 : OOB;
        { const int row = 4 * g + (c >> 2), sw = (row >> 1) & 3; const unsigned bs = stg + row * 128 + (c & 3) * 8; C[2] = bs + (0 ^ sw) * 32; C[3] = bs + (1 ^ sw) * 32; C[7] = bs + (2 ^ sw) * 32; }
        C[4] = (l == 0 && w < 3) ? ne + NB_FH + T0 * 16384 + ct * 128 : OOB;
        C[5] = ne + NB_FO + (8 * w + (l & 7)) * 128; C[6] = c * 1280 + g * 8; C[8] = w * 1024 + l * 16; C[9] = w * 4096 + l * 16; C[10] = l * 8;
        put_srd(SA, 0, p.ws, 0, WS_BYTES);
        SA[4] = stg; SA[5] = ne + NB_OF + w * 65536 + ro * 32768; SA[10] = ne + NB_OF + w * 65536 + (1 - ro) * 32768; SA[6] = T0 * 20480 + ct * 32; SA[7] = no + NB_HF; SA[8] = T0; SA[9] = w < 3 && ct < 40;
        C[10] = hfoff(ct >> 1) + (ct & 1) * 512 + l * 8;
        C[12] = (g & 1) ? OOB : hfoff(20 + (ct >> 3)) + (ct & 7) * 128 + c * 8 + (g >> 1) * 4;
        put_srd(SB, 0, p.Wo, ((w * 1024 + ro * 512) * D + 48 * j) * 2, D * HD * 2); put_srd(SB, 8, p.Wo, ((w * 1024 + (1 - ro) * 512) * D + 48 * j) * 2, D * HD * 2); put_srd(SB, 4, p.x, 0, S * D * 2);
        uni(SA); uni(SB);
        asm volatile(
    "s_barrier\n"
    "s_add_u32 m0, s40, 0\n"
    "s_mov_b32 s68, 0\n"
    "buffer_load_dwordx4 v1, s[52:55], s68 offen lds\n"
    "s_add_u32 m0, s40, 1024\n"
    "s_mov_b32 s68, 10240\n"
    "buffer_load_dwordx4 v1, s[52:55], s68 offen lds\n"
    "s_add_u32 m0, s40, 2048\n"
    "s_mov_b32 s68, 20480\n"
    "buffer_load_dwordx4 v1, s[52:55], s68 offen lds\n"
    "s_add_u32 m0, s40, 3072\n"
    "s_mov_b32 s68, 30720\n"
    "buffer_load_dwordx4 v1, s[52:55], s68 offen lds\n"
    "s_add_u32 m0, s40, 4096\n"
    "s_mov_b32 s68, 40960\n"
    "buffer_load_dwordx4 v1, s[52:55], s68 offen lds\n"
    "s_add_u32 m0, s40, 5120\n"
    "s_mov_b32 s68, 51200\n"
    "buffer_load_dwordx4 v1, s[52:55], s68 offen lds\n"
    "s_add_u32 m0, s40, 6144\n"
    "s_mov_b32 s68, 61440\n"
    "buffer_load_dwordx4 v1, s[52:55], s68 offen lds\n"
    "s_add_u32 m0, s40, 7168\n"
    "s_mov_b32 s68, 71680\n"
    "buffer_load_dwordx4 v1, s[52:55], s68 offen lds\n"
    "s_add_u32 m0, s40, 8192\n"
    "s_mov_b32 s68, 81920\n"
    "buffer_load_dwordx4 v1, s[52:55], s68 offen lds\n"
    "s_add_u32 m0, s40, 9216\n"
    "s_mov_b32 s68, 92160\n"
    "buffer_load_dwordx4 v1, s[52:55], s68 offen lds\n"
    "s_add_u32 m0, s40, 10240\n"
    "s_mov_b32 s68, 102400\n"
    "buffer_load_dwordx4 v1, s[52:55], s68 offen lds\n"
    "s_add_u32 m0, s40, 11264\n"
    "s_mov_b32 s68, 112640\n"
    "buffer_load_dwordx4 v1, s[52:55], s68 offen lds\n"
    "s_add_u32 m0, s40, 12288\n"
    "s_mov_b32 s68, 122880\n"
    "buffer_load_dwordx4 v1, s[52:55], s68 offen lds\n"
    "s_add_u32 m0, s40, 13312\n"
    "s_mov_b32 s68, 133120\n"
    "buffer_load_dwordx4 v1, s[52:55], s68 offen lds\n"
    "s_add_u32 m0, s40, 14336\n"
    "s_mov_b32 s68, 143360\n"
    "buffer_load_dwordx4 v1, s[52:55], s68 offen lds\n"
    "s_add_u32 m0, s40, 15360\n"
    "s_mov_b32 s68, 153600\n"
    "buffer_load_dwordx4 v1, s[52:55], s68 offen lds\n"
    "s_add_u32 m0, s40, 16384\n"
    "s_mov_b32 s68, 163840\n"
    "buffer_load_dwordx4 v1, s[52:55], s68 offen lds\n"
    "s_add_u32 m0, s40, 17408\n"
    "s_mov_b32 s68, 174080\n"
    "buffer_load_dwordx4 v1, s[52:55], s68 offen lds\n"
    "s_add_u32 m0, s40, 18432\n"
    "s_mov_b32 s68, 184320\n"
    "buffer_load_dwordx4 v1, s[52:55], s68 offen lds\n"
    "s_add_u32 m0, s40, 19456\n"
    "s_mov_b32 s68, 194560\n"
    "buffer_load_dwordx4 v1, s[52:55], s68 offen lds\n"
    "s_add_u32 m0, s40, 20480\n"
    "s_mov_b32 s68, 204800\n"
    "buffer_load_dwordx4 v1, s[52:55], s68 offen lds\n"
    "s_add_u32 m0, s40, 21504\n"
    "s_mov_b32 s68, 215040\n"
    "buffer_load_dwordx4 v1, s[52:55], s68 offen lds\n"
    "s_add_u32 m0, s40, 22528\n"
    "s_mov_b32 s68, 225280\n"
    "buffer_load_dwordx4 v1, s[52:55], s68 offen lds\n"
    "s_add_u32 m0, s40, 23552\n"
    "s_mov_b32 s68, 235520\n"
    "buffer_load_dwordx4 v1, s[52:55], s68 offen lds\n"
    "s_add_u32 m0, s40, 24576\n"
    "s_mov_b32 s68, 245760\n"
    "buffer_load_dwordx4 v1, s[52:55], s68 offen lds\n"
    "s_add_u32 m0, s40, 25600\n"
    "s_mov_b32 s68, 256000\n"
    "buffer_load_dwordx4 v1, s[52:55], s68 offen lds\n"
    "s_add_u32 m0, s40, 26624\n"
    "s_mov_b32 s68, 266240\n"
    "buffer_load_dwordx4 v1, s[52:55], s68 offen lds\n"
    "s_add_u32 m0, s40, 27648\n"
    "s_mov_b32 s68, 276480\n"
    "buffer_load_dwordx4 v1, s[52:55], s68 offen lds\n"
    "s_add_u32 m0, s40, 28672\n"
    "s_mov_b32 s68, 286720\n"
    "buffer_load_dwordx4 v1, s[52:55], s68 offen lds\n"
    "s_add_u32 m0, s40, 29696\n"
    "s_mov_b32 s68, 296960\n"
    "buffer_load_dwordx4 v1, s[52:55], s68 offen lds\n"
    "s_add_u32 m0, s40, 30720\n"
    "s_mov_b32 s68, 307200\n"
    "buffer_load_dwordx4 v1, s[52:55], s68 offen lds\n"
    "s_add_u32 m0, s40, 31744\n"
    "s_mov_b32 s68, 317440\n"
    "buffer_load_dwordx4 v1, s[52:55], s68 offen lds\n"
    "s_add_u32 m0, s40, 32768\n"
    "s_mov_b32 s68, 327680\n"
    "buffer_load_dwordx4 v1, s[52:55], s68 offen lds\n"
    "s_add_u32 m0, s40, 33792\n"
    "s_mov_b32 s68, 337920\n"
    "buffer_load_dwordx4 v1, s[52:55], s68 offen lds\n"
    "s_add_u32 m0, s40, 34816\n"
    "s_mov_b32 s68, 348160\n"
    "buffer_load_dwordx4 v1, s[52:55], s68 offen lds\n"
    "s_add_u32 m0, s40, 35840\n"
    "s_mov_b32 s68, 358400\n"
    "buffer_load_dwordx4 v1, s[52:55], s68 offen lds\n"
    "s_waitcnt vmcnt(32)\n"
    "ds_read_b64_tr_b16 a[0:1], v2 offset:0\n"
    "ds_read_b64_tr_b16 a[2:3], v2 offset:2048\n"
    "ds_read_b64_tr_b16 a[128:129], v3 offset:0\n"
    "ds_read_b64_tr_b16 a[130:131], v3 offset:2048\n"
    "ds_read_b64_tr_b16 v[128:129], v7 offset:0\n"
    "ds_read_b64_tr_b16 v[130:131], v7 offset:2048\n"
    "s_waitcnt lgkmcnt(0)\n"
    "s_add_u32 m0, s40, 0\n"
    "s_mov_b32 s68, 368640\n"
    "buffer_load_dwordx4 v1, s[52:55], s68 offen lds\n"
    "s_add_u32 m0, s40, 1024\n"
    "s_mov_b32 s68, 378880\n"
    "buffer_load_dwordx4 v1, s[52:55], s68 offen lds\n"
    "s_add_u32 m0, s40, 2048\n"
    "s_mov_b32 s68, 389120\n"
    "buffer_load_dwordx4 v1, s[52:55], s68 offen lds\n"
    "s_add_u32 m0, s40, 3072\n"
    "s_mov_b32 s68, 399360\n"
    "buffer_load_dwordx4 v1, s[52:55], s68 offen lds\n"
    "s_waitcnt vmcnt(32)\n"
    "ds_read_b64_tr_b16 a[4:5], v2 offset:4096\n"
    "ds_read_b64_tr_b16 a[6:7], v2 offset:6144\n"
    "ds_read_b64_tr_b16 a[132:133], v3 offset:4096\n"
    "ds_read_b64_tr_b16 a[134:135], v3 offset:6144\n"
    "ds_read_b64_tr_b16 v[132:133], v7 offset:4096\n"
    "ds_read_b64_tr_b16 v[134:135], v7 offset:6144\n"
    "s_waitcnt lgkmcnt(0)\n"
    "s_add_u32 m0, s40, 4096\n"
    "s_mov_b32 s68, 409600\n"
    "buffer_load_dwordx4 v1, s[52:55], s68 offen lds\n"
    "s_add_u32 m0, s40, 5120\n"
    "s_mov_b32 s68, 419840\n"
    "buffer_load_dwordx4 v1, s[52:55], s68 offen lds\n"
    "s_add_u32 m0, s40, 6144\n"
    "s_mov_b32 s68, 430080\n"
    "buffer_load_dwordx4 v1, s[52:55], s68 offen lds\n"
    "s_add_u32 m0, s40, 7168\n"
    "s_mov_b32 s68, 440320\n"
    "buffer_load_dwordx4 v1, s[52:55], s68 offen lds\n"
    "s_waitcnt vmcnt(32)\n"
    "ds_read_b64_tr_b16 a[8:9], v2 offset:8192\n"
    "ds_read_b64_tr_b16 a[10:11], v2 offset:10240\n"
    "ds_read_b64_tr_b16 a[136:137], v3 offset:8192\n"
    "ds_read_b64_tr_b16 a[138:139], v3 offset:10240\n"
    "ds_read_b64_tr_b16 v[136:137], v7 offset:8192\n"
    "ds_read_b64_tr_b16 v[138:139], v7 offset:10240\n"
    "s_waitcnt lgkmcnt(0)\n"
    "s_add_u32 m0, s40, 8192\n"
    "s_mov_b32 s68, 450560\n"
    "buffer_load_dwordx4 v1, s[52:55], s68 offen lds\n"
    "s_add_u32 m0, s40, 9216\n"
    "s_mov_b32 s68, 460800\n"
    "buffer_load_dwordx4 v1, s[52:55], s68 offen lds\n"
    "s_add_u32 m0, s40, 10240\n"
    "s_mov_b32 s68, 471040\n"
    "buffer_load_dwordx4 v1, s[52:55], s68 offen lds\n"
    "s_add_u32 m0, s40, 11264\n"
    "s_mov_b32 s68, 481280\n"
    "buffer_load_dwordx4 v1, s[52:55], s68 offen lds\n"
    "s_waitcnt vmcnt(32)\n"
    "ds_read_b64_tr_b16 a[12:13], v2 offset:12288\n"
    "ds_read_b64_tr_b16 a[14:15], v2 offset:14336\n"
    "ds_read_b64_tr_b16 a[140:141], v3 offset:12288\n"
    "ds_read_b64_tr_b16 a[142:143], v3 offset:14336\n"
    "ds_read_b64_tr_b16 v[140:141], v7 offset:12288\n"
    "ds_read_b64_tr_b16 v[142:143], v7 offset:14336\n"
    "s_waitcnt lgkmcnt(0)\n"
    "s_add_u32 m0, s40, 12288\n"
    "s_mov_b32 s68, 491520\n"
    "buffer_load_dwordx4 v1, s[52:55], s68 offen lds\n"
    "s_add_u32 m0, s40, 13312\n"
    "s_mov_b32 s68, 501760\n"
    "buffer_load_dwordx4 v1, s[52:55], s68 offen lds\n"
    "s_add_u32 m0, s40, 14336\n"
    "s_mov_b32 s68, 512000\n"
    "buffer_load_dwordx4 v1, s[52:55], s68 offen lds\n"
    "s_add_u32 m0, s40, 15360\n"
    "s_mov_b32 s68, 522240\n"
    "buffer_load_dwordx4 v1, s[52:55], s68 offen lds\n"
    "s_waitcnt vmcnt(32)\n"
    "ds_read_b64_tr_b16 a[16:17], v2 offset:16384\n"
    "ds_read_b64_tr_b16 a[18:19], v2 offset:18432\n"
    "ds_read_b64_tr_b16 a[144:145], v3 offset:16384\n"
    "ds_read_b64_tr_b16 a[146:147], v3 offset:18432\n"
    "ds_read_b64_tr_b16 v[144:145], v7 offset:16384\n"
    "ds_read_b64_tr_b16 v[146:147], v7 offset:18432\n"
    "s_waitcnt lgkmcnt(0)\n"
    "s_add_u32 m0, s40, 16384\n"
    "s_mov_b32 s68, 532480\n"
    "buffer_load_dwordx4 v1, s[52:55], s68 offen lds\n"
    "s_add_u32 m0, s40, 17408\n"
    "s_mov_b32 s68, 542720\n"
    "buffer_load_dwordx4 v1, s[52:55], s68 offen lds\n"
    "s_add_u32 m0, s40, 18432\n"
    "s_mov_b32 s68, 552960\n"
    "buffer_load_dwordx4 v1, s[52:55], s68 offen lds\n"
    "s_add_u32 m0, s40, 19456\n"
    "s_mov_b32 s68, 563200\n"
    "buffer_load_dwordx4 v1, s[52:55], s68 offen lds\n"
    "s_waitcnt vmcnt(32)\n"
    "ds_read_b64_tr_b16 a[20:21], v2 offset:20480\n"
    "ds_read_b64_tr_b16 a[22:23], v2 offset:22528\n"
    "ds_read_b64_tr_b16 a[148:149], v3 offset:20480\n"
    "ds_read_b64_tr_b16 a[150:151], v3 offset:22528\n"
    "ds_read_b64_tr_b16 v[148:149], v7 offset:20480\n"
    "ds_read_b64_tr_b16 v[150:151], v7 offset:22528\n"
    "s_waitcnt lgkmcnt(0)\n"
    "s_add_u32 m0, s40, 20480\n"
    "s_mov_b32 s68, 573440\n"
    "buffer_load_dwordx4 v1, s[52:55], s68 offen lds\n"
    "s_add_u32 m0, s40, 21504\n"
    "s_mov_b32 s68, 583680\n"
    "buffer_load_dwordx4 v1, s[52:55], s68 offen lds\n"
    "s_add_u32 m0, s40, 22528\n"
    "s_mov_b32 s68, 593920\n"
    "buffer_load_dwordx4 v1, s[52:55], s68 offen lds\n"
    "s_add_u32 m0, s40, 23552\n"
    "s_mov_b32 s68, 604160\n"
    "buffer_load_dwordx4 v1, s[52:55], s68 offen lds\n"
    "s_waitcnt vmcnt(32)\n"
    "ds_read_b64_tr_b16 a[24:25], v2 offset:24576\n"
    "ds_read_b64_tr_b16 a[26:27], v2 offset:26624\n"
    "ds_read_b64_tr_b16 a[152:153], v3 offset:24576\n"
    "ds_read_b64_tr_b16 a[154:155], v3 offset:26624\n"
    "ds_read_b64_tr_b16 v[152:153], v7 offset:24576\n"
    "ds_read_b64_tr_b16 v[154:155], v7 offset:26624\n"
    "s_waitcnt lgkmcnt(0)\n"
    "s_add_u32 m0, s40, 24576\n"
    "s_mov_b32 s68, 614400\n"
    "buffer_load_dwordx4 v1, s[52:55], s68 offen lds\n"
    "s_add_u32 m0, s40, 25600\n"
    "s_mov_b32 s68, 624640\n"
    "buffer_load_dwordx4 v1, s[52:55], s68 offen lds\n"
    "s_add_u32 m0, s40, 26624\n"
    "s_mov_b32 s68, 634880\n"
    "buffer_load_dwordx4 v1, s[52:55], s68 offen lds\n"
    "s_add_u32 m0, s40, 27648\n"
    "s_mov_b32 s68, 645120\n"
    "buffer_load_dwordx4 v1, s[52:55], s68 offen lds\n"
    "s_waitcnt vmcnt(32)\n"
    "ds_read_b64_tr_b16 a[28:29], v2 offset:28672\n"
    "ds_read_b64_tr_b16 a[30:31], v2 offset:30720\n"
    "ds_read_b64_tr_b16 a[156:157], v3 offset:28672\n"
    "ds_read_b64_tr_b16 a[158:159], v3 offset:30720\n"
    "ds_read_b64_tr_b16 v[156:157], v7 offset:28672\n"
    "ds_read_b64_tr_b16 v[158:159], v7 offset:30720\n"
    "s_waitcnt lgkmcnt(0)\n"
    "s_add_u32 m0, s40, 28672\n"
    "s_mov_b32 s68, 0\n"
    "buffer_load_dwordx4 v1, s[60:63], s68 offen lds\n"
    "s_add_u32 m0, s40, 29696\n"
    "s_mov_b32 s68, 10240\n"
    "buffer_load_dwordx4 v1, s[60:63], s68 offen lds\n"
    "s_add_u32 m0, s40, 30720\n"
    "s_mov_b32 s68, 20480\n"
    "buffer_load_dwordx4 v1, s[60:63], s68 offen lds\n"
    "s_add_u32 m0, s40, 31744\n"
    "s_mov_b32 s68, 30720\n"
    "buffer_load_dwordx4 v1, s[60:63], s68 offen lds\n"
    "s_waitcnt vmcnt(32)\n"
    "ds_read_b64_tr_b16 a[32:33], v2 offset:32768\n"
    "ds_read_b64_tr_b16 a[34:35], v2 offset:34816\n"
    "ds_read_b64_tr_b16 a[160:161], v3 offset:32768\n"
    "ds_read_b64_tr_b16 a[162:163], v3 offset:34816\n"
    "ds_read_b64_tr_b16 v[160:161], v7 offset:32768\n"
    "ds_read_b64_tr_b16 v[162:163], v7 offset:34816\n"
    "s_waitcnt lgkmcnt(0)\n"
    "s_add_u32 m0, s40, 32768\n"
    "s_mov_b32 s68, 40960\n"
    "buffer_load_dwordx4 v1, s[60:63], s68 offen lds\n"
    "s_add_u32 m0, s40, 33792\n"
    "s_mov_b32 s68, 51200\n"
    "buffer_load_dwordx4 v1, s[60:63], s68 offen lds\n"
    "s_add_u32 m0, s40, 34816\n"
    "s_mov_b32 s68, 61440\n"
    "buffer_load_dwordx4 v1, s[60:63], s68 offen lds\n"
    "s_add_u32 m0, s40, 35840\n"
    "s_mov_b32 s68, 71680\n"
    "buffer_load_dwordx4 v1, s[60:63], s68 offen lds\n"
    "s_waitcnt vmcnt(32)\n"
    "ds_read_b64_tr_b16 a[36:37], v2 offset:0\n"
    "ds_read_b64_tr_b16 a[38:39], v2 offset:2048\n"
    "ds_read_b64_tr_b16 a[164:165], v3 offset:0\n"
    "ds_read_b64_tr_b16 a[166:167], v3 offset:2048\n"
    "ds_read_b64_tr_b16 v[164:165], v7 offset:0\n"
    "ds_read_b64_tr_b16 v[166:167], v7 offset:2048\n"
    "s_waitcnt lgkmcnt(0)\n"
    "s_add_u32 m0, s40, 0\n"
    "s_mov_b32 s68, 81920\n"
    "buffer_load_dwordx4 v1, s[60:63], s68 offen lds\n"
    "s_add_u32 m0, s40, 1024\n"
    "s_mov_b32 s68, 92160\n"
    "buffer_load_dwordx4 v1, s[60:63], s68 offen lds\n"
    "s_add_u32 m0, s40, 2048\n"
    "s_mov_b32 s68, 102400\n"
    "buffer_load_dwordx4 v1, s[60:63], s68 offen lds\n"
    "s_add_u32 m0, s40, 3072\n"
    "s_mov_b32 s68, 112640\n"
    "buffer_load_dwordx4 v1, s[60:63], s68 offen lds\n"
    "s_waitcnt vmcnt(32)\n"
    "ds_read_b64_tr_b16 a[40:41], v2 offset:4096\n"
    "ds_read_b64_tr_b16 a[42:43], v2 offset:6144\n"
    "ds_read_b64_tr_b16 a[168:169], v3 offset:4096\n"
    "ds_read_b64_tr_b16 a[170:171], v3 offset:6144\n"
    "ds_read_b64_tr_b16 v[168:169], v7 offset:4096\n"
    "ds_read_b64_tr_b16 v[170:171], v7 offset:6144\n"
    "s_waitcnt lgkmcnt(0)\n"
    "s_add_u32 m0, s40, 4096\n"
    "s_mov_b32 s68, 122880\n"
    "buffer_load_dwordx4 v1, s[60:63], s68 offen lds\n"
    "s_add_u32 m0, s40, 5120\n"
    "s_mov_b32 s68, 133120\n"
    "buffer_load_dwordx4 v1, s[60:63], s68 offen lds\n"
    "s_add_u32 m0, s40, 6144\n"
    "s_mov_b32 s68, 143360\n"
    "buffer_load_dwordx4 v1, s[60:63], s68 offen lds\n"
    "s_add_u32 m0, s40, 7168\n"
    "s_mov_b32 s68, 153600\n"
    "buffer_load_dwordx4 v1, s[60:63], s68 offen lds\n"
    "s_waitcnt vmcnt(32)\n"
    "ds_read_b64_tr_b16 a[44:45], v2 offset:8192\n"
    "ds_read_b64_tr_b16 a[46:47], v2 offset:10240\n"
    "ds_read_b64_tr_b16 a[172:173], v3 offset:8192\n"
    "ds_read_b64_tr_b16 a[174:175], v3 offset:10240\n"
    "ds_read_b64_tr_b16 v[172:173], v7 offset:8192\n"
    "ds_read_b64_tr_b16 v[174:175], v7 offset:10240\n"
    "s_waitcnt lgkmcnt(0)\n"
    "s_add_u32 m0, s40, 8192\n"
    "s_mov_b32 s68, 163840\n"
    "buffer_load_dwordx4 v1, s[60:63], s68 offen lds\n"
    "s_add_u32 m0, s40, 9216\n"
    "s_mov_b32 s68, 174080\n"
    "buffer_load_dwordx4 v1, s[60:63], s68 offen lds\n"
    "s_add_u32 m0, s40, 10240\n"
    "s_mov_b32 s68, 184320\n"
    "buffer_load_dwordx4 v1, s[60:63], s68 offen lds\n"
    "s_add_u32 m0, s40, 11264\n"
    "s_mov_b32 s68, 194560\n"
    "buffer_load_dwordx4 v1, s[60:63], s68 offen lds\n"
    "s_waitcnt vmcnt(32)\n"
    "ds_read_b64_tr_b16 a[48:49], v2 offset:12288\n"
    "ds_read_b64_tr_b16 a[50:51], v2 offset:14336\n"
    "ds_read_b64_tr_b16 a[176:177], v3 offset:12288\n"
    "ds_read_b64_tr_b16 a[178:179], v3 offset:14336\n"
    "ds_read_b64_tr_b16 v[176:177], v7 offset:12288\n"
    "ds_read_b64_tr_b16 v[178:179], v7 offset:14336\n"
    "s_waitcnt lgkmcnt(0)\n"
    "s_add_u32 m0, s40, 12288\n"
    "s_mov_b32 s68, 204800\n"
    "buffer_load_dwordx4 v1, s[60:63], s68 offen lds\n"
    "s_add_u32 m0, s40, 13312\n"
    "s_mov_b32 s68, 215040\n"
    "buffer_load_dwordx4 v1, s[60:63], s68 offen lds\n"
    "s_add_u32 m0, s40, 14336\n"
    "s_mov_b32 s68, 225280\n"
    "buffer_load_dwordx4 v1, s[60:63], s68 offen lds\n"
    "s_add_u32 m0, s40, 15360\n"
    "s_mov_b32 s68, 235520\n"
    "buffer_load_dwordx4 v1, s[60:63], s68 offen lds\n"
    "s_waitcnt vmcnt(32)\n"
    "ds_read_b64_tr_b16 a[52:53], v2 offset:16384\n"
    "ds_read_b64_tr_b16 a[54:55], v2 offset:18432\n"
    "ds_read_b64_tr_b16 a[180:181], v3 offset:16384\n"
    "ds_read_b64_tr_b16 a[182:183], v3 offset:18432\n"
    "ds_read_b64_tr_b16 v[180:181], v7 offset:16384\n"
    "ds_read_b64_tr_b16 v[182:183], v7 offset:18432\n"
    "s_waitcnt lgkmcnt(0)\n"
    "s_add_u32 m0, s40, 16384\n"
    "s_mov_b32 s68, 245760\n"
    "buffer_load_dwordx4 v1, s[60:63], s68 offen lds\n"
    "s_add_u32 m0, s40, 17408\n"
    "s_mov_b32 s68, 256000\n"
    "buffer_load_dwordx4 v1, s[60:63], s68 offen lds\n"
    "s_add_u32 m0, s40, 18432\n"
    "s_mov_b32 s68, 266240\n"
    "buffer_load_dwordx4 v1, s[60:63], s68 offen lds\n"
    "s_add_u32 m0, s40, 19456\n"
    "s_mov_b32 s68, 276480\n"
    "buffer_load_dwordx4 v1, s[60:63], s68 offen lds\n"
    "s_waitcnt vmcnt(32)\n"
    "ds_read_b64_tr_b16 a[56:57], v2 offset:20480\n"
    "ds_read_b64_tr_b16 a[58:59], v2 offset:22528\n"
    "ds_read_b64_tr_b16 a[184:185], v3 offset:20480\n"
    "ds_read_b64_tr_b16 a[186:187], v3 offset:22528\n"
    "ds_read_b64_tr_b16 v[184:185], v7 offset:20480\n"
    "ds_read_b64_tr_b16 v[186:187], v7 offset:22528\n"
    "s_waitcnt lgkmcnt(0)\n"
    "s_add_u32 m0, s40, 20480\n"
    "s_mov_b32 s68, 286720\n"
    "buffer_load_dwordx4 v1, s[60:63], s68 offen lds\n"
    "s_add_u32 m0, s40, 21504\n"
    "s_mov_b32 s68, 296960\n"
    "buffer_load_dwordx4 v1, s[60:63], s68 offen lds\n"
    "s_add_u32 m0, s40, 22528\n"
    "s_mov_b32 s68, 307200\n"
    "buffer_load_dwordx4 v1, s[60:63], s68 offen lds\n"
    "s_add_u32 m0, s40, 23552\n"
    "s_mov_b32 s68, 317440\n"
    "buffer_load_dwordx4 v1, s[60:63], s68 offen lds\n"
    "s_waitcnt vmcnt(32)\n"
    "ds_read_b64_tr_b16 a[60:61], v2 offset:24576\n"
    "ds_read_b64_tr_b16 a[62:63], v2 offset:26624\n"
    "ds_read_b64_tr_b16 a[188:189], v3 offset:24576\n"
    "ds_read_b64_tr_b16 a[190:191], v3 offset:26624\n"
    "ds_read_b64_tr_b16 v[188:189], v7 offset:24576\n"
    "ds_read_b64_tr_b16 v[190:191], v7 offset:26624\n"
    "s_waitcnt lgkmcnt(0)\n"
    "s_add_u32 m0, s40, 24576\n"
    "s_mov_b32 s68, 327680\n"
    "buffer_load_dwordx4 v1, s[60:63], s68 offen lds\n"
    "s_add_u32 m0, s40, 25600\n"
    "s_mov_b32 s68, 337920\n"
    "buffer_load_dwordx4 v1, s[60:63], s68 offen lds\n"
    "s_add_u32 m0, s40, 26624\n"
    "s_mov_b32 s68, 348160\n"
    "buffer_load_dwordx4 v1, s[60:63], s68 offen lds\n"
    "s_add_u32 m0, s40, 27648\n"
    "s_mov_b32 s68, 358400\n"
    "buffer_load_dwordx4 v1, s[60:63], s68 offen lds\n"
    "s_waitcnt vmcnt(32)\n"
    "ds_read_b64_tr_b16 a[64:65], v2 offset:28672\n"
    "ds_read_b64_tr_b16 a[66:67], v2 offset:30720\n"
    "ds_read_b64_tr_b16 a[192:193], v3 offset:28672\n"
    "ds_read_b64_tr_b16 a[194:195], v3 offset:30720\n"
    "ds_read_b64_tr_b16 v[192:193], v7 offset:28672\n"
    "ds_read_b64_tr_b16 v[194:195], v7 offset:30720\n"
    "s_waitcnt lgkmcnt(0)\n"
    "s_add_u32 m0, s40, 28672\n"
    "s_mov_b32 s68, 368640\n"
    "buffer_load_dwordx4 v1, s[60:63], s68 offen lds\n"
    "s_add_u32 m0, s40, 29696\n"
    "s_mov_b32 s68, 378880\n"
    "buffer_load_dwordx4 v1, s[60:63], s68 offen lds\n"
    "s_add_u32 m0, s40, 30720\n"
    "s_mov_b32 s68, 389120\n"
    "buffer_load_dwordx4 v1, s[60:63], s68 offen lds\n"
    "s_add_u32 m0, s40, 31744\n"
    "s_mov_b32 s68, 399360\n"
    "buffer_load_dwordx4 v1, s[60:63], s68 offen lds\n"
    "s_waitcnt vmcnt(32)\n"
    "ds_read_b64_tr_b16 a[68:69], v2 offset:32768\n"
    "ds_read_b64_tr_b16 a[70:71], v2 offset:34816\n"
    "ds_read_b64_tr_b16 a[196:197], v3 offset:32768\n"
    "ds_read_b64_tr_b16 a[198:199], v3 offset:34816\n"
    "ds_read_b64_tr_b16 v[196:197], v7 offset:32768\n"
    "ds_read_b64_tr_b16 v[198:199], v7 offset:34816\n"
    "s_waitcnt lgkmcnt(0)\n"
    "s_add_u32 m0, s40, 32768\n"
    "s_mov_b32 s68, 409600\n"
    "buffer_load_dwordx4 v1, s[60:63], s68 offen lds\n"
    "s_add_u32 m0, s40, 33792\n"
    "s_mov_b32 s68, 419840\n"
    "buffer_load_dwordx4 v1, s[60:63], s68 offen lds\n"
    "s_add_u32 m0, s40, 34816\n"
    "s_mov_b32 s68, 430080\n"
    "buffer_load_dwordx4 v1, s[60:63], s68 offen lds\n"
    "s_add_u32 m0, s40, 35840\n"
    "s_mov_b32 s68, 440320\n"
    "buffer_load_dwordx4 v1, s[60:63], s68 offen lds\n"
    "s_waitcnt vmcnt(32)\n"
    "ds_read_b64_tr_b16 a[72:73], v2 offset:0\n"
    "ds_read_b64_tr_b16 a[74:75], v2 offset:2048\n"
    "ds_read_b64_tr_b16 a[200:201], v3 offset:0\n"
    "ds_read_b64_tr_b16 a[202:203], v3 offset:2048\n"
    "ds_read_b64_tr_b16 v[200:201], v7 offset:0\n"
    "ds_read_b64_tr_b16 v[202:203], v7 offset:2048\n"
    "s_waitcnt lgkmcnt(0)\n"
    "s_add_u32 m0, s40, 0\n"
    "s_mov_b32 s68, 450560\n"
    "buffer_load_dwordx4 v1, s[60:63], s68 offen lds\n"
    "s_add_u32 m0, s40, 1024\n"
    "s_mov_b32 s68, 460800\n"
    "buffer_load_dwordx4 v1, s[60:63], s68 offen lds\n"
    "s_add_u32 m0, s40, 2048\n"
    "s_mov_b32 s68, 471040\n"
    "buffer_load_dwordx4 v1, s[60:63], s68 offen lds\n"
    "s_add_u32 m0, s40, 3072\n"
    "s_mov_b32 s68, 481280\n"
    "buffer_load_dwordx4 v1, s[60:63], s68 offen lds\n"
    "s_waitcnt vmcnt(32)\n"
    "ds_read_b64_tr_b16 a[76:77], v2 offset:4096\n"
    "ds_read_b64_tr_b16 a[78:79], v2 offset:6144\n"
    "ds_read_b64_tr_b16 a[204:205], v3 offset:4096\n"
    "ds_read_b64_tr_b16 a[206:207], v3 offset:6144\n"
    "ds_read_b64_tr_b16 v[204:205], v7 offset:4096\n"
    "ds_read_b64_tr_b16 v[206:207], v7 offset:6144\n"
    "s_waitcnt lgkmcnt(0)\n"
    "s_add_u32 m0, s40, 4096\n"
    "s_mov_b32 s68, 491520\n"
    "buffer_load_dwordx4 v1, s[60:63], s68 offen lds\n"
    "s_add_u32 m0, s40, 5120\n"
    "s_mov_b32 s68, 501760\n"
    "buffer_load_dwordx4 v1, s[60:63], s68 offen lds\n"
    "s_add_u32 m0, s40, 6144\n"
    "s_mov_b32 s68, 512000\n"
    "buffer_load_dwordx4 v1, s[60:63], s68 offen lds\n"
    "s_add_u32 m0, s40, 7168\n"
    "s_mov_b32 s68, 522240\n"
    "buffer_load_dwordx4 v1, s[60:63], s68 offen lds\n"
    "s_waitcnt vmcnt(32)\n"
    "ds_read_b64_tr_b16 a[80:81], v2 offset:8192\n"
    "ds_read_b64_tr_b16 a[82:83], v2 offset:10240\n"
    "ds_read_b64_tr_b16 a[208:209], v3 offset:8192\n"
    "ds_read_b64_tr_b16 a[210:211], v3 offset:10240\n"
    "ds_read_b64_tr_b16 v[208:209], v7 offset:8192\n"
    "ds_read_b64_tr_b16 v[210:211], v7 offset:10240\n"
    "s_waitcnt lgkmcnt(0)\n"
    "s_add_u32 m0, s40, 8192\n"
    "s_mov_b32 s68, 532480\n"
    "buffer_load_dwordx4 v1, s[60:63], s68 offen lds\n"
    "s_add_u32 m0, s40, 9216\n"
    "s_mov_b32 s68, 542720\n"
    "buffer_load_dwordx4 v1, s[60:63], s68 offen lds\n"
    "s_add_u32 m0, s40, 10240\n"
    "s_mov_b32 s68, 552960\n"
    "buffer_load_dwordx4 v1, s[60:63], s68 offen lds\n"
    "s_add_u32 m0, s40, 11264\n"
    "s_mov_b32 s68, 563200\n"
    "buffer_load_dwordx4 v1, s[60:63], s68 offen lds\n"
    "s_waitcnt vmcnt(32)\n"
    "ds_read_b64_tr_b16 a[84:85], v2 offset:12288\n"
    "ds_read_b64_tr_b16 a[86:87], v2 offset:14336\n"
    "ds_read_b64_tr_b16 a[212:213], v3 offset:12288\n"
    "ds_read_b64_tr_b16 a[214:215], v3 offset:14336\n"
    "ds_read_b64_tr_b16 v[212:213], v7 offset:12288\n"
    "ds_read_b64_tr_b16 v[214:215], v7 offset:14336\n"
    "s_waitcnt lgkmcnt(0)\n"
    "s_add_u32 m0, s40, 12288\n"
    "s_mov_b32 s68, 573440\n"
    "buffer_load_dwordx4 v1, s[60:63], s68 offen lds\n"
    "s_add_u32 m0, s40, 13312\n"
    "s_mov_b32 s68, 583680\n"
    "buffer_load_dwordx4 v1, s[60:63], s68 offen lds\n"
    "s_add_u32 m0, s40, 14336\n"
    "s_mov_b32 s68, 593920\n"
    "buffer_load_dwordx4 v1, s[60:63], s68 offen lds\n"
    "s_add_u32 m0, s40, 15360\n"
    "s_mov_b32 s68, 604160\n"
    "buffer_load_dwordx4 v1, s[60:63], s68 offen lds\n"
    "s_waitcnt vmcnt(32)\n"
    "ds_read_b64_tr_b16 a[88:89], v2 offset:16384\n"
    "ds_read_b64_tr_b16 a[90:91], v2 offset:18432\n"
    "ds_read_b64_tr_b16 a[216:217], v3 offset:16384\n"
    "ds_read_b64_tr_b16 a[218:219], v3 offset:18432\n"
    "ds_read_b64_tr_b16 v[216:217], v7 offset:16384\n"
    "ds_read_b64_tr_b16 v[218:219], v7 offset:18432\n"
    "s_waitcnt lgkmcnt(0)\n"
    "s_add_u32 m0, s40, 16384\n"
    "s_mov_b32 s68, 614400\n"
    "buffer_load_dwordx4 v1, s[60:63], s68 offen lds\n"
    "s_add_u32 m0, s40, 17408\n"
    "s_mov_b32 s68, 624640\n"
    "buffer_load_dwordx4 v1, s[60:63], s68 offen lds\n"
    "s_add_u32 m0, s40, 18432\n"
    "s_mov_b32 s68, 634880\n"
    "buffer_load_dwordx4 v1, s[60:63], s68 offen lds\n"
    "s_add_u32 m0, s40, 19456\n"
    "s_mov_b32 s68, 645120\n"
    "buffer_load_dwordx4 v1, s[60:63], s68 offen lds\n"
    "s_waitcnt vmcnt(32)\n"
    "ds_read_b64_tr_b16 a[92:93], v2 offset:20480\n"
    "ds_read_b64_tr_b16 a[94:95], v2 offset:22528\n"
    "ds_read_b64_tr_b16 a[220:221], v3 offset:20480\n"
    "ds_read_b64_tr_b16 a[222:223], v3 offset:22528\n"
    "ds_read_b64_tr_b16 v[220:221], v7 offset:20480\n"
    "ds_read_b64_tr_b16 v[222:223], v7 offset:22528\n"
    "s_waitcnt vmcnt(28)\n"
    "ds_read_b64_tr_b16 a[96:97], v2 offset:24576\n"
    "ds_read_b64_tr_b16 a[98:99], v2 offset:26624\n"
    "ds_read_b64_tr_b16 a[224:225], v3 offset:24576\n"
    "ds_read_b64_tr_b16 a[226:227], v3 offset:26624\n"
    "ds_read_b64_tr_b16 v[224:225], v7 offset:24576\n"
    "ds_read_b64_tr_b16 v[226:227], v7 offset:26624\n"
    "s_waitcnt vmcnt(24)\n"
    "ds_read_b64_tr_b16 a[100:101], v2 offset:28672\n"
    "ds_read_b64_tr_b16 a[102:103], v2 offset:30720\n"
    "s_waitcnt lgkmcnt(7)\n"
    "ds_read_b64_tr_b16 a[228:229], v3 offset:28672\n"
    "ds_read_b64_tr_b16 a[230:231], v3 offset:30720\n"
    "ds_read_b64_tr_b16 v[228:229], v7 offset:28672\n"
    "ds_read_b64_tr_b16 v[230:231], v7 offset:30720\n"
    "s_waitcnt vmcnt(20)\n"
    "ds_read_b64_tr_b16 a[104:105], v2 offset:32768\n"
    "ds_read_b64_tr_b16 a[106:107], v2 offset:34816\n"
    "ds_read_b64_tr_b16 a[232:233], v3 offset:32768\n"
    "s_waitcnt lgkmcnt(7)\n"
    "ds_read_b64_tr_b16 a[234:235], v3 offset:34816\n"
    "ds_read_b64_tr_b16 v[232:233], v7 offset:32768\n"
    "ds_read_b64_tr_b16 v[234:235], v7 offset:34816\n"
    "s_waitcnt vmcnt(16)\n"
    "ds_read_b64_tr_b16 a[108:109], v2 offset:0\n"
    "ds_read_b64_tr_b16 a[110:111], v2 offset:2048\n"
    "ds_read_b64_tr_b16 a[236:237], v3 offset:0\n"
    "ds_read_b64_tr_b16 a[238:239], v3 offset:2048\n"
    "s_waitcnt lgkmcnt(7)\n"
    "ds_read_b64_tr_b16 v[236:237], v7 offset:0\n"
    "ds_read_b64_tr_b16 v[238:239], v7 offset:2048\n"
    "s_waitcnt vmcnt(12)\n"
    "ds_read_b64_tr_b16 a[112:113], v2 offset:4096\n"
    "ds_read_b64_tr_b16 a[114:115], v2 offset:6144\n"
    "ds_read_b64_tr_b16 a[240:241], v3 offset:4096\n"
    "ds_read_b64_tr_b16 a[242:243], v3 offset:6144\n"
    "ds_read_b64_tr_b16 v[240:241], v7 offset:4096\n"
    "s_waitcnt lgkmcnt(7)\n"
    "ds_read_b64_tr_b16 v[242:243], v7 offset:6144\n"
    "s_waitcnt vmcnt(8)\n"
    "ds_read_b64_tr_b16 a[116:117], v2 offset:8192\n"
    "ds_read_b64_tr_b16 a[118:119], v2 offset:10240\n"
    "ds_read_b64_tr_b16 a[244:245], v3 offset:8192\n"
    "ds_read_b64_tr_b16 a[246:247], v3 offset:10240\n"
    "ds_read_b64_tr_b16 v[244:245], v7 offset:8192\n"
    "ds_read_b64_tr_b16 v[246:247], v7 offset:10240\n"
    "s_waitcnt vmcnt(4)\n"
    "s_waitcnt lgkmcnt(7)\n"
    "ds_read_b64_tr_b16 a[120:121], v2 offset:12288\n"
    "ds_read_b64_tr_b16 a[122:123], v2 offset:14336\n"
    "ds_read_b64_tr_b16 a[248:249], v3 offset:12288\n"
    "ds_read_b64_tr_b16 a[250:251], v3 offset:14336\n"
    "ds_read_b64_tr_b16 v[248:249], v7 offset:12288\n"
    "ds_read_b64_tr_b16 v[250:251], v7 offset:14336\n"
    "s_waitcnt vmcnt(0)\n"
    "ds_read_b64_tr_b16 a[124:125], v2 offset:16384\n"
    "s_waitcnt lgkmcnt(7)\n"
    "ds_read_b64_tr_b16 a[126:127], v2 offset:18432\n"
    "ds_read_b64_tr_b16 a[252:253], v3 offset:16384\n"
    "ds_read_b64_tr_b16 a[254:255], v3 offset:18432\n"
    "ds_read_b64_tr_b16 v[252:253], v7 offset:16384\n"
    "ds_read_b64_tr_b16 v[254:255], v7 offset:18432\n"
    "s_waitcnt lgkmcnt(0)\n"
    "s_mov_b32 s71, 0\n"
    "s_mov_b32 s74, 0\n"
    "v_mov_b32 v2, v12\n"
    "v_mov_b32 v12, 0\n"
    "s_barrier\n"
    "RG_loop_%=:\n"
    "s_add_u32 s68, s44, 0\n"
    "s_lshr_b32 s75, s68, 3\n"
    "s_lshl_b32 s75, s75, 21\n"
    "s_lshl_b32 s68, s68, 18\n"
    "s_add_u32 s75, s75, s68\n"
    "s_add_u32 s68, s44, 3\n"
    "s_lshr_b32 s76, s68, 3\n"
    "s_lshl_b32 s76, s76, 21\n"
    "s_lshl_b32 s68, s68, 18\n"
    "s_add_u32 s76, s76, s68\n"
    "s_add_u32 s68, s44, 0\n"
    "s_lshr_b32 s77, s68, 5\n"
    "s_lshl_b32 s77, s77, 21\n"
    "s_lshl_b32 s68, s68, 16\n"
    "s_add_u32 s77, s77, s68\n"
    "s_add_u32 s76, s76, s41\n"
    "s_add_u32 s69, s75, s41\n"
    "s_add_u32 s75, s75, s46\n"
    "s_add_u32 s77, s77, s43\n"
    "s_cmp_lg_u32 s74, 0\n"
    "s_cbranch_scc1 RG_main_%=\n"
    "s_add_u32 s73, s44, 2\n"
    "s_lshr_b32 s73, s73, 1\n"
    "s_mov_b32 s72, 0\n"
    "RG_pa_%=:\n"
    "buffer_load_dword v13, v5, s[36:39], 0 offen sc1\n"
    "s_waitcnt vmcnt(0)\n"
    "v_cmp_gt_u32 vcc, s73, v13\n"
    "s_cbranch_vccz RG_qa_%=\n"
    "s_nop 0\n"
    "s_ff1_i32_b64 s94, vcc\n"
    "s_lshl_b64 exec, 1, s94\n"
    "RG_ra_%=:\n"
    "s_cmp_lg_u32 s71, 0\n"
    "s_cbranch_scc1 RG_qa_%=\n"
    "s_sleep 4\n"
    "buffer_load_dword v13, v5, s[36:39], 0 offen sc1\n"
    "s_waitcnt vmcnt(0)\n"
    "v_cmp_gt_u32 vcc, s73, v13\n"
    "s_add_u32 s72, s72, 1\n"
    "s_cmp_lt_u32 s72, 0x40000\n"
    "s_cselect_b32 s71, s71, 1\n"
    "s_cbranch_vccnz RG_ra_%=\n"
    "s_mov_b64 exec, -1\n"
    "s_branch RG_pa_%=\n"
    "RG_qa_%=:\n"
    "s_mov_b64 exec, -1\n"
    "s_add_u32 s68, s69, 0\n"
    "buffer_load_dwordx4 v[48:51], v0, s[36:39], s68 offen offset:0 sc1\n"
    "buffer_load_dwordx4 v[52:55], v0, s[36:39], s68 offen offset:1024 sc1\n"
    "buffer_load_dwordx4 v[56:59], v0, s[36:39], s68 offen offset:2048 sc1\n"
    "buffer_load_dwordx4 v[60:63], v0, s[36:39], s68 offen offset:3072 sc1\n"
    "s_add_u32 s68, s69, 4096\n"
    "buffer_load_dwordx4 v[64:67], v0, s[36:39], s68 offen offset:0 sc1\n"
    "buffer_load_dwordx4 v[68:71], v0, s[36:39], s68 offen offset:1024 sc1\n"
    "buffer_load_dwordx4 v[72:75], v0, s[36:39], s68 offen offset:2048 sc1\n"
    "buffer_load_dwordx4 v[76:79], v0, s[36:39], s68 offen offset:3072 sc1\n"
    "s_add_u32 s68, s69, 16384\n"
    "buffer_load_dwordx4 v[80:83], v0, s[36:39], s68 offen offset:0 sc1\n"
    "buffer_load_dwordx4 v[84:87], v0, s[36:39], s68 offen offset:1024 sc1\n"
    "buffer_load_dwordx4 v[88:91], v0, s[36:39], s68 offen offset:2048 sc1\n"
    "buffer_load_dwordx4 v[92:95], v0, s[36:39], s68 offen offset:3072 sc1\n"
    "s_add_u32 s68, s69, 20480\n"
    "buffer_load_dwordx4 v[96:99], v0, s[36:39], s68 offen offset:0 sc1\n"
    "buffer_load_dwordx4 v[100:103], v0, s[36:39], s68 offen offset:1024 sc1\n"
    "buffer_load_dwordx4 v[104:107], v0, s[36:39], s68 offen offset:2048 sc1\n"
    "buffer_load_dwordx4 v[108:111], v0, s[36:39], s68 offen offset:3072 sc1\n"
    "RG_main_%=:\n"
    "s_add_u32 s73, s44, 5\n"
    "s_lshr_b32 s73, s73, 1\n"
    "v_cmp_gt_u32 vcc, s73, v13\n"
    "s_add_u32 s73, s44, 8\n"
    "s_lshr_b32 s73, s73, 1\n"
    "v_cmp_gt_u32 s[94:95], s73, v13\n"
    "v_mov_b32 v14, 0x7ffffff0\n"
    "s_nop 0\n"
    "v_cndmask_b32 v14, v14, v5, s[94:95]\n"
    "buffer_load_dword v14, v14, s[36:39], 0 offen sc1\n"
    "buffer_load_dwordx2 v[32:33], v6, s[56:59], s42 offen\n"
    "s_cmp_eq_u64 vcc, 0\n"
    "s_cselect_b32 s74, 1, 0\n"
    "s_cmp_ge_u32 s44, 125\n"
    "s_cselect_b32 s74, 0, s74\n"
    "s_cmp_lg_u32 s74, 0\n"
    "s_cselect_b32 s70, 0, 0x7ffffff0\n"
    "v_or_b32 v34, s70, v0\n"
    "v_or_b32 v35, s70, v7\n"
    "s_waitcnt vmcnt(17)\n"
    "v_mfma_f32_16x16x32_bf16 v[16:19], a[0:3], v[48:51], 0\n"
    "v_mfma_f32_16x16x32_bf16 v[20:23], a[128:131], v[48:51], 0\n"
    "v_mfma_f32_16x16x32_bf16 v[24:27], v[128:131], v[48:51], 0\n"
    "s_add_u32 s68, s75, 0\n"
    "buffer_load_dwordx4 v[48:51], v0, s[36:39], s68 offen offset:0 sc1\n"
    "s_waitcnt vmcnt(17)\n"
    "v_mfma_f32_16x16x32_bf16 v[16:19], a[4:7], v[52:55], v[16:19]\n"
    "v_mfma_f32_16x16x32_bf16 v[20:23], a[132:135], v[52:55], v[20:23]\n"
    "v_mfma_f32_16x16x32_bf16 v[24:27], v[132:135], v[52:55], v[24:27]\n"
    "buffer_load_dwordx4 v[52:55], v0, s[36:39], s68 offen offset:1024 sc1\n"
    "s_waitcnt vmcnt(17)\n"
    "v_mfma_f32_16x16x32_bf16 v[16:19], a[8:11], v[56:59], v[16:19]\n"
    "v_mfma_f32_16x16x32_bf16 v[20:23], a[136:139], v[56:59], v[20:23]\n"
    "v_mfma_f32_16x16x32_bf16 v[24:27], v[136:139], v[56:59], v[24:27]\n"
    "buffer_load_dwordx4 v[56:59], v0, s[36:39], s68 offen offset:2048 sc1\n"
    "s_waitcnt vmcnt(17)\n"
    "v_mfma_f32_16x16x32_bf16 v[16:19], a[12:15], v[60:63], v[16:19]\n"
    "v_mfma_f32_16x16x32_bf16 v[20:23], a[140:143], v[60:63], v[20:23]\n"
    "v_mfma_f32_16x16x32_bf16 v[24:27], v[140:143], v[60:63], v[24:27]\n"
    "buffer_load_dwordx4 v[60:63], v0, s[36:39], s68 offen offset:3072 sc1\n"
    "s_waitcnt vmcnt(17)\n"
    "v_mfma_f32_16x16x32_bf16 v[16:19], a[16:19], v[64:67], v[16:19]\n"
    "v_mfma_f32_16x16x32_bf16 v[20:23], a[144:147], v[64:67], v[20:23]\n"
    "v_mfma_f32_16x16x32_bf16 v[24:27], v[144:147], v[64:67], v[24:27]\n"
    "s_add_u32 s68, s75, 4096\n"
    "buffer_load_dwordx4 v[64:67], v0, s[36:39], s68 offen offset:0 sc1\n"
    "s_waitcnt vmcnt(17)\n"
    "v_mfma_f32_16x16x32_bf16 v[16:19], a[20:23], v[68:71], v[16:19]\n"
    "v_mfma_f32_16x16x32_bf16 v[20:23], a[148:151], v[68:71], v[20:23]\n"
    "v_mfma_f32_16x16x32_bf16 v[24:27], v[148:151], v[68:71], v[24:27]\n"
    "buffer_load_dwordx4 v[68:71], v0, s[36:39], s68 offen offset:1024 sc1\n"
    "s_waitcnt vmcnt(17)\n"
    "v_mfma_f32_16x16x32_bf16 v[16:19], a[24:27], v[72:75], v[16:19]\n"
    "v_mfma_f32_16x16x32_bf16 v[20:23], a[152:155], v[72:75], v[20:23]\n"
    "v_mfma_f32_16x16x32_bf16 v[24:27], v[152:155], v[72:75], v[24:27]\n"
    "buffer_load_dwordx4 v[72:75], v0, s[36:39], s68 offen offset:2048 sc1\n"
    "s_waitcnt vmcnt(17)\n"
    "v_mfma_f32_16x16x32_bf16 v[16:19], a[28:31], v[76:79], v[16:19]\n"
    "v_mfma_f32_16x16x32_bf16 v[20:23], a[156:159], v[76:79], v[20:23]\n"
    "v_mfma_f32_16x16x32_bf16 v[24:27], v[156:159], v[76:79], v[24:27]\n"
    "buffer_load_dwordx4 v[76:79], v0, s[36:39], s68 offen offset:3072 sc1\n"
    "s_waitcnt vmcnt(17)\n"
    "v_mfma_f32_16x16x32_bf16 v[16:19], a[32:35], v[80:83], v[16:19]\n"
    "v_mfma_f32_16x16x32_bf16 v[20:23], a[160:163], v[80:83], v[20:23]\n"
    "v_mfma_f32_16x16x32_bf16 v[24:27], v[160:163], v[80:83], v[24:27]\n"
    "s_add_u32 s68, s75, 16384\n"
    "buffer_load_dwordx4 v[80:83], v0, s[36:39], s68 offen offset:0 sc1\n"
    "s_waitcnt vmcnt(17)\n"
    "v_mfma_f32_16x16x32_bf16 v[16:19], a[36:39], v[84:87], v[16:19]\n"
    "v_mfma_f32_16x16x32_bf16 v[20:23], a[164:167], v[84:87], v[20:23]\n"
    "v_mfma_f32_16x16x32_bf16 v[24:27], v[164:167], v[84:87], v[24:27]\n"
    "buffer_load_dwordx4 v[84:87], v0, s[36:39], s68 offen offset:1024 sc1\n"
    "s_waitcnt vmcnt(17)\n"
    "v_mfma_f32_16x16x32_bf16 v[16:19], a[40:43], v[88:91], v[16:19]\n"
    "v_mfma_f32_16x16x32_bf16 v[20:23], a[168:171], v[88:91], v[20:23]\n"
    "v_mfma_f32_16x16x32_bf16 v[24:27], v[168:171], v[88:91], v[24:27]\n"
    "buffer_load_dwordx4 v[88:91], v0, s[36:39], s68 offen offset:2048 sc1\n"
    "s_waitcnt vmcnt(17)\n"
    "v_mfma_f32_16x16x32_bf16 v[16:19], a[44:47], v[92:95], v[16:19]\n"
    "v_mfma_f32_16x16x32_bf16 v[20:23], a[172:175], v[92:95], v[20:23]\n"
    "v_mfma_f32_16x16x32_bf16 v[24:27], v[172:175], v[92:95], v[24:27]\n"
    "buffer_load_dwordx4 v[92:95], v0, s[36:39], s68 offen offset:3072 sc1\n"
    "s_waitcnt vmcnt(17)\n"
    "v_mfma_f32_16x16x32_bf16 v[16:19], a[48:51], v[96:99], v[16:19]\n"
    "v_mfma_f32_16x16x32_bf16 v[20:23], a[176:179], v[96:99], v[20:23]\n"
    "v_mfma_f32_16x16x32_bf16 v[24:27], v[176:179], v[96:99], v[24:27]\n"
    "s_add_u32 s68, s75, 20480\n"
    "buffer_load_dwordx4 v[96:99], v0, s[36:39], s68 offen offset:0 sc1\n"
    "s_waitcnt vmcnt(17)\n"
    "v_mfma_f32_16x16x32_bf16 v[16:19], a[52:55], v[100:103], v[16:19]\n"
    "v_mfma_f32_16x16x32_bf16 v[20:23], a[180:183], v[100:103], v[20:23]\n"
    "v_mfma_f32_16x16x32_bf16 v[24:27], v[180:183], v[100:103], v[24:27]\n"
    "buffer_load_dwordx4 v[100:103], v0, s[36:39], s68 offen offset:1024 sc1\n"
    "s_waitcnt vmcnt(17)\n"
    "v_mfma_f32_16x16x32_bf16 v[16:19], a[56:59], v[104:107], v[16:19]\n"
    "v_mfma_f32_16x16x32_bf16 v[20:23], a[184:187], v[104:107], v[20:23]\n"
    "v_mfma_f32_16x16x32_bf16 v[24:27], v[184:187], v[104:107], v[24:27]\n"
    "buffer_load_dwordx4 v[104:107], v0, s[36:39], s68 offen offset:2048 sc1\n"
    "s_waitcnt vmcnt(17)\n"
    "v_mfma_f32_16x16x32_bf16 v[16:19], a[60:63], v[108:111], v[16:19]\n"
    "v_mfma_f32_16x16x32_bf16 v[20:23], a[188:191], v[108:111], v[20:23]\n"
    "v_mfma_f32_16x16x32_bf16 v[24:27], v[188:191], v[108:111], v[24:27]\n"
    "buffer_load_dwordx4 v[108:111], v0, s[36:39], s68 offen offset:3072 sc1\n"
    "s_waitcnt vmcnt(15)\n"
    "buffer_store_dword v12, v4, s[36:39], 0 offen sc1\n"
    "v_mfma_f32_16x16x32_bf16 v[16:19], a[64:67], v[48:51], v[16:19]\n"
    "v_mfma_f32_16x16x32_bf16 v[20:23], a[192:195], v[48:51], v[20:23]\n"
    "v_mfma_f32_16x16x32_bf16 v[24:27], v[192:195], v[48:51], v[24:27]\n"
    "s_add_u32 s68, s76, 0\n"
    "buffer_load_dwordx4 v[48:51], v34, s[36:39], s68 offen offset:0 sc1\n"
    "s_waitcnt vmcnt(16)\n"
    "v_mfma_f32_16x16x32_bf16 v[16:19], a[68:71], v[52:55], v[16:19]\n"
    "v_mfma_f32_16x16x32_bf16 v[20:23], a[196:199], v[52:55], v[20:23]\n"
    "v_mfma_f32_16x16x32_bf16 v[24:27], v[196:199], v[52:55], v[24:27]\n"
    "buffer_load_dwordx4 v[52:55], v34, s[36:39], s68 offen offset:1024 sc1\n"
    "s_waitcnt vmcnt(16)\n"
    "v_mfma_f32_16x16x32_bf16 v[16:19], a[72:75], v[56:59], v[16:19]\n"
    "v_mfma_f32_16x16x32_bf16 v[20:23], a[200:203], v[56:59], v[20:23]\n"
    "v_mfma_f32_16x16x32_bf16 v[24:27], v[200:203], v[56:59], v[24:27]\n"
    "buffer_load_dwordx4 v[56:59], v34, s[36:39], s68 offen offset:2048 sc1\n"
    "s_waitcnt vmcnt(16)\n"
    "v_mfma_f32_16x16x32_bf16 v[16:19], a[76:79], v[60:63], v[16:19]\n"
    "v_mfma_f32_16x16x32_bf16 v[20:23], a[204:207], v[60:63], v[20:23]\n"
    "v_mfma_f32_16x16x32_bf16 v[24:27], v[204:207], v[60:63], v[24:27]\n"
    "buffer_load_dwordx4 v[60:63], v34, s[36:39], s68 offen offset:3072 sc1\n"
    "s_waitcnt vmcnt(16)\n"
    "v_mfma_f32_16x16x32_bf16 v[16:19], a[80:83], v[64:67], v[16:19]\n"
    "v_mfma_f32_16x16x32_bf16 v[20:23], a[208:211], v[64:67], v[20:23]\n"
    "v_mfma_f32_16x16x32_bf16 v[24:27], v[208:211], v[64:67], v[24:27]\n"
    "s_add_u32 s68, s76, 4096\n"
    "buffer_load_dwordx4 v[64:67], v34, s[36:39], s68 offen offset:0 sc1\n"
    "s_waitcnt vmcnt(16)\n"
    "v_mfma_f32_16x16x32_bf16 v[16:19], a[84:87], v[68:71], v[16:19]\n"
    "v_mfma_f32_16x16x32_bf16 v[20:23], a[212:215], v[68:71], v[20:23]\n"
    "v_mfma_f32_16x16x32_bf16 v[24:27], v[212:215], v[68:71], v[24:27]\n"
    "buffer_load_dwordx4 v[68:71], v34, s[36:39], s68 offen offset:1024 sc1\n"
    "s_waitcnt vmcnt(16)\n"
    "v_mfma_f32_16x16x32_bf16 v[16:19], a[88:91], v[72:75], v[16:19]\n"
    "v_mfma_f32_16x16x32_bf16 v[20:23], a[216:219], v[72:75], v[20:23]\n"
    "v_mfma_f32_16x16x32_bf16 v[24:27], v[216:219], v[72:75], v[24:27]\n"
    "buffer_load_dwordx4 v[72:75], v34, s[36:39], s68 offen offset:2048 sc1\n"
    "s_waitcnt vmcnt(16)\n"
    "v_mfma_f32_16x16x32_bf16 v[16:19], a[92:95], v[76:79], v[16:19]\n"
    "v_mfma_f32_16x16x32_bf16 v[20:23], a[220:223], v[76:79], v[20:23]\n"
    "v_mfma_f32_16x16x32_bf16 v[24:27], v[220:223], v[76:79], v[24:27]\n"
    "buffer_load_dwordx4 v[76:79], v34, s[36:39], s68 offen offset:3072 sc1\n"
    "s_waitcnt vmcnt(16)\n"
    "v_mfma_f32_16x16x32_bf16 v[16:19], a[96:99], v[80:83], v[16:19]\n"
    "v_mfma_f32_16x16x32_bf16 v[20:23], a[224:227], v[80:83], v[20:23]\n"
    "v_mfma_f32_16x16x32_bf16 v[24:27], v[224:227], v[80:83], v[24:27]\n"
    "s_add_u32 s68, s76, 16384\n"
    "buffer_load_dwordx4 v[80:83], v34, s[36:39], s68 offen offset:0 sc1\n"
    "s_waitcnt vmcnt(16)\n"
    "v_mfma_f32_16x16x32_bf16 v[16:19], a[100:103], v[84:87], v[16:19]\n"
    "v_mfma_f32_16x16x32_bf16 v[20:23], a[228:231], v[84:87], v[20:23]\n"
    "v_mfma_f32_16x16x32_bf16 v[24:27], v[228:231], v[84:87], v[24:27]\n"
    "buffer_load_dwordx4 v[84:87], v34, s[36:39], s68 offen offset:1024 sc1\n"
    "s_waitcnt vmcnt(16)\n"
    "v_mfma_f32_16x16x32_bf16 v[16:19], a[104:107], v[88:91], v[16:19]\n"
    "v_mfma_f32_16x16x32_bf16 v[20:23], a[232:235], v[88:91], v[20:23]\n"
    "v_mfma_f32_16x16x32_bf16 v[24:27], v[232:235], v[88:91], v[24:27]\n"
    "buffer_load_dwordx4 v[88:91], v34, s[36:39], s68 offen offset:2048 sc1\n"
    "s_waitcnt vmcnt(16)\n"
    "v_mfma_f32_16x16x32_bf16 v[16:19], a[108:111], v[92:95], v[16:19]\n"
    "v_mfma_f32_16x16x32_bf16 v[20:23], a[236:239], v[92:95], v[20:23]\n"
    "v_mfma_f32_16x16x32_bf16 v[24:27], v[236:239], v[92:95], v[24:27]\n"
    "buffer_load_dwordx4 v[92:95], v34, s[36:39], s68 offen offset:3072 sc1\n"
    "s_waitcnt vmcnt(16)\n"
    "v_mfma_f32_16x16x32_bf16 v[16:19], a[112:115], v[96:99], v[16:19]\n"
    "v_mfma_f32_16x16x32_bf16 v[20:23], a[240:243], v[96:99], v[20:23]\n"
    "v_mfma_f32_16x16x32_bf16 v[24:27], v[240:243], v[96:99], v[24:27]\n"
    "s_add_u32 s68, s76, 20480\n"
    "buffer_load_dwordx4 v[96:99], v34, s[36:39], s68 offen offset:0 sc1\n"
    "s_waitcnt vmcnt(16)\n"
    "v_mfma_f32_16x16x32_bf16 v[16:19], a[116:119], v[100:103], v[16:19]\n"
    "v_mfma_f32_16x16x32_bf16 v[20:23], a[244:247], v[100:103], v[20:23]\n"
    "v_mfma_f32_16x16x32_bf16 v[24:27], v[244:247], v[100:103], v[24:27]\n"
    "buffer_load_dwordx4 v[100:103], v34, s[36:39], s68 offen offset:1024 sc1\n"
    "s_waitcnt vmcnt(16)\n"
    "v_mfma_f32_16x16x32_bf16 v[16:19], a[120:123], v[104:107], v[16:19]\n"
    "v_mfma_f32_16x16x32_bf16 v[20:23], a[248:251], v[104:107], v[20:23]\n"
    "v_mfma_f32_16x16x32_bf16 v[24:27], v[248:251], v[104:107], v[24:27]\n"
    "buffer_load_dwordx4 v[104:107], v34, s[36:39], s68 offen offset:2048 sc1\n"
    "s_waitcnt vmcnt(16)\n"
    "v_mfma_f32_16x16x32_bf16 v[16:19], a[124:127], v[108:111], v[16:19]\n"
    "v_mfma_f32_16x16x32_bf16 v[20:23], a[252:255], v[108:111], v[20:23]\n"
    "v_mfma_f32_16x16x32_bf16 v[24:27], v[252:255], v[108:111], v[24:27]\n"
    "buffer_load_dwordx4 v[108:111], v34, s[36:39], s68 offen offset:3072 sc1\n"
    "s_nop 2\n"
    "ds_write_b128 v8, v[16:19] offset:0\n"
    "s_nop 0\n"
    "ds_write_b128 v8, v[20:23] offset:4096\n"
    "s_nop 0\n"
    "ds_write_b128 v8, v[24:27] offset:8192\n"
    "v_max_u32 v13, v13, v14\n"
    "s_waitcnt lgkmcnt(0)\n"
    "s_barrier\n"
    "s_cmp_eq_u32 s45, 0\n"
    "s_cbranch_scc1 RG_next_%=\n"
    "ds_read_b128 v[16:19], v9 offset:0\n"
    "ds_read_b128 v[20:23], v9 offset:1024\n"
    "ds_read_b128 v[24:27], v9 offset:2048\n"
    "ds_read_b128 v[28:31], v9 offset:3072\n"
    "v_lshlrev_b32 v36, 16, v32\n"
    "v_and_b32 v37, 0xffff0000, v32\n"
    "v_lshlrev_b32 v38, 16, v33\n"
    "v_and_b32 v39, 0xffff0000, v33\n"
    "s_waitcnt lgkmcnt(2)\n"
    "v_pk_add_f32 v[16:17], v[16:17], v[20:21]\n"
    "v_pk_add_f32 v[18:19], v[18:19], v[22:23]\n"
    "s_waitcnt lgkmcnt(0)\n"
    "v_pk_add_f32 v[24:25], v[24:25], v[28:29]\n"
    "v_pk_add_f32 v[26:27], v[26:27], v[30:31]\n"
    "v_pk_add_f32 v[16:17], v[16:17], v[24:25]\n"
    "v_pk_add_f32 v[18:19], v[18:19], v[26:27]\n"
    "v_pk_add_f32 v[16:17], v[16:17], v[36:37]\n"
    "v_pk_add_f32 v[18:19], v[18:19], v[38:39]\n"
    "v_cvt_pk_bf16_f32 v36, v16, v17\n"
    "v_cvt_pk_bf16_f32 v37, v18, v19\n"
    "v_pk_mul_f32 v[20:21], v[16:17], v[16:17]\n"
    "v_pk_add_f32 v[22:23], v[16:17], v[18:19]\n"
    "v_pk_fma_f32 v[20:21], v[18:19], v[18:19], v[20:21]\n"
    "v_add_f32 v24, v22, v23\n"
    "v_add_f32 v25, v20, v21\n"
    "s_nop 1\n"
    "v_permlane32_swap_b32 v24, v25\n"
    "v_add_f32 v26, v24, v25\n"
    "v_add_f32 v24, v24, v25\n"
    "s_nop 1\n"
    "v_permlane16_swap_b32 v24, v26\n"
    "v_add_f32 v24, v24, v26\n"
    "buffer_store_dword v24, v2, s[36:39], s77 offen sc1\n"
    "buffer_store_dwordx2 v[36:37], v10, s[36:39], s77 offen sc1\n"
    "s_add_u32 s68, s44, 1\n"
    "v_mov_b32 v12, s68\n"
    "s_cmp_lg_u32 s74, 0\n"
    "s_cbranch_scc1 RG_next_%=\n"
    "s_waitcnt vmcnt(0)\n"
    "buffer_store_dword v12, v4, s[36:39], 0 offen sc1\n"
    "RG_next_%=:\n"
    "v_xor_b32 v8, 0x4000, v8\n"
    "v_xor_b32 v9, 0x4000, v9\n"
    "s_add_u32 s42, s42, 61440\n"
    "s_add_u32 s44, s44, 3\n"
    "s_cmp_lt_u32 s44, 128\n"
    "s_cbranch_scc1 RG_loop_%=\n"
    "s_waitcnt vmcnt(0)\n"
    "s_endpgm\n"
            :: "{v[0:15]}"(C), "{s[36:51]}"(SA), "{s[52:67]}"(SB) : CLOB_W);
        __builtin_unreachable();
    }
    if (xi == 2 && slot >= 2 && slot < 8) return;
    if (xi == 0 || (xi < 3 && slot < 8)) {
        ROLE_BEGIN
        const int l = lane_id(), c = l & 15, g = l >> 4;
        v16u C = {}, SA = {}, SB = {};
        const unsigned stg = w * 36864;
        strip_consts(C, l, stg, 1280, true);
        const int k = xi == 2 ? 8 + slot : slot, T0 = xi != 0, ct = 4 * k + w;
        const unsigned sd = read_side(p.ws), ne = comm_off(p.ws) + ((sd ^ 1) << 13), no = comm_off(p.ws) + (sd << 13);
        C[4] = (T0 == 1 && k == 0 && w == 0 && l == 0) ? WS_EPO : OOB;
        C[5] = ne + NB_FA + T0 * 16384 + min(14 * w + (l < 14 ? l : 0), 53) * 128; C[6] = c * 1280 + g * 8; C[7] = w == 3 ? OOB : l * 16; C[8] = w * 1024 + l * 16; C[9] = w * 4096 + l * 16; C[10] = l * 8;
        put_srd(SA, 0, p.ws, 0, WS_BYTES);
        SA[4] = stg; C[10] = hfoff(ct >> 1) + (ct & 1) * 512 + l * 8;
        SA[5] = no + NB_AF + w * 32768; SA[6] = no + NB_HF; SA[7] = T0 * 20480 + ct * 32; SA[8] = T0; SA[9] = 1;
        put_srd(SB, 0, p.w2, (w * 448 * D + 64 * k) * 2, F * D * 2); put_srd(SB, 4, p.out, 0, S * D * 2);
        uni(SA); uni(SB);
        asm volatile(
    "s_barrier\n"
    "s_add_u32 m0, s40, 0\n"
    "s_mov_b32 s68, 0\n"
    "buffer_load_dwordx4 v1, s[52:55], s68 offen lds\n"
    "s_add_u32 m0, s40, 1024\n"
    "s_mov_b32 s68, 20480\n"
    "buffer_load_dwordx4 v1, s[52:55], s68 offen lds\n"
    "s_add_u32 m0, s40, 2048\n"
    "s_mov_b32 s68, 40960\n"
    "buffer_load_dwordx4 v1, s[52:55], s68 offen lds\n"
    "s_add_u32 m0, s40, 3072\n"
    "s_mov_b32 s68, 61440\n"
    "buffer_load_dwordx4 v1, s[52:55], s68 offen lds\n"
    "s_add_u32 m0, s40, 4096\n"
    "s_mov_b32 s68, 64\n"
    "buffer_load_dwordx4 v1, s[52:55], s68 offen lds\n"
    "s_add_u32 m0, s40, 5120\n"
    "s_mov_b32 s68, 20544\n"
    "buffer_load_dwordx4 v1, s[52:55], s68 offen lds\n"
    "s_add_u32 m0, s40, 6144\n"
    "s_mov_b32 s68, 41024\n"
    "buffer_load_dwordx4 v1, s[52:55], s68 offen lds\n"
    "s_add_u32 m0, s40, 7168\n"
    "s_mov_b32 s68, 61504\n"
    "buffer_load_dwordx4 v1, s[52:55], s68 offen lds\n"
    "s_add_u32 m0, s40, 8192\n"
    "s_mov_b32 s68, 81920\n"
    "buffer_load_dwordx4 v1, s[52:55], s68 offen lds\n"
    "s_add_u32 m0, s40, 9216\n"
    "s_mov_b32 s68, 102400\n"
    "buffer_load_dwordx4 v1, s[52:55], s68 offen lds\n"
    "s_add_u32 m0, s40, 10240\n"
    "s_mov_b32 s68, 122880\n"
    "buffer_load_dwordx4 v1, s[52:55], s68 offen lds\n"
    "s_add_u32 m0, s40, 11264\n"
    "s_mov_b32 s68, 143360\n"
    "buffer_load_dwordx4 v1, s[52:55], s68 offen lds\n"
    "s_add_u32 m0, s40, 12288\n"
    "s_mov_b32 s68, 81984\n"
    "buffer_load_dwordx4 v1, s[52:55], s68 offen lds\n"
    "s_add_u32 m0, s40, 13312\n"
    "s_mov_b32 s68, 102464\n"
    "buffer_load_dwordx4 v1, s[52:55], s68 offen lds\n"
    "s_add_u32 m0, s40, 14336\n"
    "s_mov_b32 s68, 122944\n"
    "buffer_load_dwordx4 v1, s[52:55], s68 offen lds\n"
    "s_add_u32 m0, s40, 15360\n"
    "s_mov_b32 s68, 143424\n"
    "buffer_load_dwordx4 v1, s[52:55], s68 offen lds\n"
    "s_add_u32 m0, s40, 16384\n"
    "s_mov_b32 s68, 163840\n"
    "buffer_load_dwordx4 v1, s[52:55], s68 offen lds\n"
    "s_add_u32 m0, s40, 17408\n"
    "s_mov_b32 s68, 184320\n"
    "buffer_load_dwordx4 v1, s[52:55], s68 offen lds\n"
    "s_add_u32 m0, s40, 18432\n"
    "s_mov_b32 s68, 204800\n"
    "buffer_load_dwordx4 v1, s[52:55], s68 offen lds\n"
    "s_add_u32 m0, s40, 19456\n"
    "s_mov_b32 s68, 225280\n"
    "buffer_load_dwordx4 v1, s[52:55], s68 offen lds\n"
    "s_add_u32 m0, s40, 20480\n"
    "s_mov_b32 s68, 163904\n"
    "buffer_load_dwordx4 v1, s[52:55], s68 offen lds\n"
    "s_add_u32 m0, s40, 21504\n"
    "s_mov_b32 s68, 184384\n"
    "buffer_load_dwordx4 v1, s[52:55], s68 offen lds\n"
    "s_add_u32 m0, s40, 22528\n"
    "s_mov_b32 s68, 204864\n"
    "buffer_load_dwordx4 v1, s[52:55], s68 offen lds\n"
    "s_add_u32 m0, s40, 23552\n"
    "s_mov_b32 s68, 225344\n"
    "buffer_load_dwordx4 v1, s[52:55], s68 offen lds\n"
    "s_add_u32 m0, s40, 24576\n"
    "s_mov_b32 s68, 245760\n"
    "buffer_load_dwordx4 v1, s[52:55], s68 offen lds\n"
    "s_add_u32 m0, s40, 25600\n"
    "s_mov_b32 s68, 266240\n"
    "buffer_load_dwordx4 v1, s[52:55], s68 offen lds\n"
    "s_add_u32 m0, s40, 26624\n"
    "s_mov_b32 s68, 286720\n"
    "buffer_load_dwordx4 v1, s[52:55], s68 offen lds\n"
    "s_add_u32 m0, s40, 27648\n"
    "s_mov_b32 s68, 307200\n"
    "buffer_load_dwordx4 v1, s[52:55], s68 offen lds\n"
    "s_add_u32 m0, s40, 28672\n"
    "s_mov_b32 s68, 245824\n"
    "buffer_load_dwordx4 v1, s[52:55], s68 offen lds\n"
    "s_add_u32 m0, s40, 29696\n"
    "s_mov_b32 s68, 266304\n"
    "buffer_load_dwordx4 v1, s[52:55], s68 offen lds\n"
    "s_add_u32 m0, s40, 30720\n"
    "s_mov_b32 s68, 286784\n"
    "buffer_load_dwordx4 v1, s[52:55], s68 offen lds\n"
    "s_add_u32 m0, s40, 31744\n"
    "s_mov_b32 s68, 307264\n"
    "buffer_load_dwordx4 v1, s[52:55], s68 offen lds\n"
    "s_add_u32 m0, s40, 32768\n"
    "s_mov_b32 s68, 327680\n"
    "buffer_load_dwordx4 v1, s[52:55], s68 offen lds\n"
    "s_add_u32 m0, s40, 33792\n"
    "s_mov_b32 s68, 348160\n"
    "buffer_load_dwordx4 v1, s[52:55], s68 offen lds\n"
    "s_add_u32 m0, s40, 34816\n"
    "s_mov_b32 s68, 368640\n"
    "buffer_load_dwordx4 v1, s[52:55], s68 offen lds\n"
    "s_add_u32 m0, s40, 35840\n"
    "s_mov_b32 s68, 389120\n"
    "buffer_load_dwordx4 v1, s[52:55], s68 offen lds\n"
    "s_waitcnt vmcnt(32)\n"
    "ds_read_b64_tr_b16 a[0:1], v2 offset:0\n"
    "ds_read_b64_tr_b16 a[2:3], v2 offset:1024\n"
    "ds_read_b64_tr_b16 a[4:5], v2 offset:2048\n"
    "ds_read_b64_tr_b16 a[6:7], v2 offset:3072\n"
    "ds_read_b64_tr_b16 a[8:9], v3 offset:0\n"
    "ds_read_b64_tr_b16 a[10:11], v3 offset:1024\n"
    "ds_read_b64_tr_b16 a[12:13], v3 offset:2048\n"
    "ds_read_b64_tr_b16 a[14:15], v3 offset:3072\n"
    "s_waitcnt lgkmcnt(0)\n"
    "s_add_u32 m0, s40, 0\n"
    "s_mov_b32 s68, 327744\n"
    "buffer_load_dwordx4 v1, s[52:55], s68 offen lds\n"
    "s_add_u32 m0, s40, 1024\n"
    "s_mov_b32 s68, 348224\n"
    "buffer_load_dwordx4 v1, s[52:55], s68 offen lds\n"
    "s_add_u32 m0, s40, 2048\n"
    "s_mov_b32 s68, 368704\n"
    "buffer_load_dwordx4 v1, s[52:55], s68 offen lds\n"
    "s_add_u32 m0, s40, 3072\n"
    "s_mov_b32 s68, 389184\n"
    "buffer_load_dwordx4 v1, s[52:55], s68 offen lds\n"
    "s_waitcnt vmcnt(32)\n"
    "ds_read_b64_tr_b16 a[112:113], v2 offset:4096\n"
    "ds_read_b64_tr_b16 a[114:115], v2 offset:5120\n"
    "ds_read_b64_tr_b16 a[116:117], v2 offset:6144\n"
    "ds_read_b64_tr_b16 a[118:119], v2 offset:7168\n"
    "ds_read_b64_tr_b16 a[120:121], v3 offset:4096\n"
    "ds_read_b64_tr_b16 a[122:123], v3 offset:5120\n"
    "ds_read_b64_tr_b16 a[124:125], v3 offset:6144\n"
    "ds_read_b64_tr_b16 a[126:127], v3 offset:7168\n"
    "s_waitcnt lgkmcnt(0)\n"
    "s_add_u32 m0, s40, 4096\n"
    "s_mov_b32 s68, 409600\n"
    "buffer_load_dwordx4 v1, s[52:55], s68 offen lds\n"
    "s_add_u32 m0, s40, 5120\n"
    "s_mov_b32 s68, 430080\n"
    "buffer_load_dwordx4 v1, s[52:55], s68 offen lds\n"
    "s_add_u32 m0, s40, 6144\n"
    "s_mov_b32 s68, 450560\n"
    "buffer_load_dwordx4 v1, s[52:55], s68 offen lds\n"
    "s_add_u32 m0, s40, 7168\n"
    "s_mov_b32 s68, 471040\n"
    "buffer_load_dwordx4 v1, s[52:55], s68 offen lds\n"
    "s_waitcnt vmcnt(32)\n"
    "ds_read_b64_tr_b16 a[16:17], v2 offset:8192\n"
    "ds_read_b64_tr_b16 a[18:19], v2 offset:9216\n"
    "ds_read_b64_tr_b16 a[20:21], v2 offset:10240\n"
    "ds_read_b64_tr_b16 a[22:23], v2 offset:11264\n"
    "ds_read_b64_tr_b16 a[24:25], v3 offset:8192\n"
    "ds_read_b64_tr_b16 a[26:27], v3 offset:9216\n"
    "ds_read_b64_tr_b16 a[28:29], v3 offset:10240\n"
    "ds_read_b64_tr_b16 a[30:31], v3 offset:11264\n"
    "s_waitcnt lgkmcnt(0)\n"
    "s_add_u32 m0, s40, 8192\n"
    "s_mov_b32 s68, 409664\n"
    "buffer_load_dwordx4 v1, s[52:55], s68 offen lds\n"
    "s_add_u32 m0, s40, 9216\n"
    "s_mov_b32 s68, 430144\n"
    "buffer_load_dwordx4 v1, s[52:55], s68 offen lds\n"
    "s_add_u32 m0, s40, 10240\n"
    "s_mov_b32 s68, 450624\n"
    "buffer_load_dwordx4 v1, s[52:55], s68 offen lds\n"
    "s_add_u32 m0, s40, 11264\n"
    "s_mov_b32 s68, 471104\n"
    "buffer_load_dwordx4 v1, s[52:55], s68 offen lds\n"
    "s_waitcnt vmcnt(32)\n"
    "ds_read_b64_tr_b16 a[128:129], v2 offset:12288\n"
    "ds_read_b64_tr_b16 a[130:131], v2 offset:13312\n"
    "ds_read_b64_tr_b16 a[132:133], v2 offset:14336\n"
    "ds_read_b64_tr_b16 a[134:135], v2 offset:15360\n"
    "ds_read_b64_tr_b16 a[136:137], v3 offset:12288\n"
    "ds_read_b64_tr_b16 a[138:139], v3 offset:13312\n"
    "ds_read_b64_tr_b16 a[140:141], v3 offset:14336\n"
    "ds_read_b64_tr_b16 a[142:143], v3 offset:15360\n"
    "s_waitcnt lgkmcnt(0)\n"
    "s_add_u32 m0, s40, 12288\n"
    "s_mov_b32 s68, 491520\n"
    "buffer_load_dwordx4 v1, s[52:55], s68 offen lds\n"
    "s_add_u32 m0, s40, 13312\n"
    "s_mov_b32 s68, 512000\n"
    "buffer_load_dwordx4 v1, s[52:55], s68 offen lds\n"
    "s_add_u32 m0, s40, 14336\n"
    "s_mov_b32 s68, 532480\n"
    "buffer_load_dwordx4 v1, s[52:55], s68 offen lds\n"
    "s_add_u32 m0, s40, 15360\n"
    "s_mov_b32 s68, 552960\n"
    "buffer_load_dwordx4 v1, s[52:55], s68 offen lds\n"
    "s_waitcnt vmcnt(32)\n"
    "ds_read_b64_tr_b16 a[32:33], v2 offset:16384\n"
    "ds_read_b64_tr_b16 a[34:35], v2 offset:17408\n"
    "ds_read_b64_tr_b16 a[36:37], v2 offset:18432\n"
    "ds_read_b64_tr_b16 a[38:39], v2 offset:19456\n"
    "ds_read_b64_tr_b16 a[40:41], v3 offset:16384\n"
    "ds_read_b64_tr_b16 a[42:43], v3 offset:17408\n"
    "ds_read_b64_tr_b16 a[44:45], v3 offset:18432\n"
    "ds_read_b64_tr_b16 a[46:47], v3 offset:19456\n"
    "s_waitcnt lgkmcnt(0)\n"
    "s_add_u32 m0, s40, 16384\n"
    "s_mov_b32 s68, 491584\n"
    "buffer_load_dwordx4 v1, s[52:55], s68 offen lds\n"
    "s_add_u32 m0, s40, 17408\n"
    "s_mov_b32 s68, 512064\n"
    "buffer_load_dwordx4 v1, s[52:55], s68 offen lds\n"
    "s_add_u32 m0, s40, 18432\n"
    "s_mov_b32 s68, 532544\n"
    "buffer_load_dwordx4 v1, s[52:55], s68 offen lds\n"
    "s_add_u32 m0, s40, 19456\n"
    "s_mov_b32 s68, 553024\n"
    "buffer_load_dwordx4 v1, s[52:55], s68 offen lds\n"
    "s_waitcnt vmcnt(32)\n"
    "ds_read_b64_tr_b16 a[144:145], v2 offset:20480\n"
    "ds_read_b64_tr_b16 a[146:147], v2 offset:21504\n"
    "ds_read_b64_tr_b16 a[148:149], v2 offset:22528\n"
    "ds_read_b64_tr_b16 a[150:151], v2 offset:23552\n"
    "ds_read_b64_tr_b16 a[152:153], v3 offset:20480\n"
    "ds_read_b64_tr_b16 a[154:155], v3 offset:21504\n"
    "ds_read_b64_tr_b16 a[156:157], v3 offset:22528\n"
    "ds_read_b64_tr_b16 a[158:159], v3 offset:23552\n"
    "s_waitcnt vmcnt(28)\n"
    "ds_read_b64_tr_b16 a[48:49], v2 offset:24576\n"
    "ds_read_b64_tr_b16 a[50:51], v2 offset:25600\n"
    "ds_read_b64_tr_b16 a[52:53], v2 offset:26624\n"
    "ds_read_b64_tr_b16 a[54:55], v2 offset:27648\n"
    "ds_read_b64_tr_b16 a[56:57], v3 offset:24576\n"
    "ds_read_b64_tr_b16 a[58:59], v3 offset:25600\n"
    "s_waitcnt lgkmcnt(7)\n"
    "ds_read_b64_tr_b16 a[60:61], v3 offset:26624\n"
    "ds_read_b64_tr_b16 a[62:63], v3 offset:27648\n"
    "s_waitcnt vmcnt(24)\n"
    "ds_read_b64_tr_b16 a[160:161], v2 offset:28672\n"
    "ds_read_b64_tr_b16 a[162:163], v2 offset:29696\n"
    "ds_read_b64_tr_b16 a[164:165], v2 offset:30720\n"
    "ds_read_b64_tr_b16 a[166:167], v2 offset:31744\n"
    "ds_read_b64_tr_b16 a[168:169], v3 offset:28672\n"
    "s_waitcnt lgkmcnt(7)\n"
    "ds_read_b64_tr_b16 a[170:171], v3 offset:29696\n"
    "ds_read_b64_tr_b16 a[172:173], v3 offset:30720\n"
    "ds_read_b64_tr_b16 a[174:175], v3 offset:31744\n"
    "s_waitcnt vmcnt(20)\n"
    "ds_read_b64_tr_b16 a[64:65], v2 offset:32768\n"
    "ds_read_b64_tr_b16 a[66:67], v2 offset:33792\n"
    "ds_read_b64_tr_b16 a[68:69], v2 offset:34816\n"
    "ds_read_b64_tr_b16 a[70:71], v2 offset:35840\n"
    "s_waitcnt lgkmcnt(7)\n"
    "ds_read_b64_tr_b16 a[72:73], v3 offset:32768\n"
    "ds_read_b64_tr_b16 a[74:75], v3 offset:33792\n"
    "ds_read_b64_tr_b16 a[76:77], v3 offset:34816\n"
    "ds_read_b64_tr_b16 a[78:79], v3 offset:35840\n"
    "s_waitcnt vmcnt(16)\n"
    "ds_read_b64_tr_b16 a[176:177], v2 offset:0\n"
    "ds_read_b64_tr_b16 a[178:179], v2 offset:1024\n"
    "ds_read_b64_tr_b16 a[180:181], v2 offset:2048\n"
    "s_waitcnt lgkmcnt(7)\n"
    "ds_read_b64_tr_b16 a[182:183], v2 offset:3072\n"
    "ds_read_b64_tr_b16 a[184:185], v3 offset:0\n"
    "ds_read_b64_tr_b16 a[186:187], v3 offset:1024\n"
    "ds_read_b64_tr_b16 a[188:189], v3 offset:2048\n"
    "ds_read_b64_tr_b16 a[190:191], v3 offset:3072\n"
    "s_waitcnt vmcnt(12)\n"
    "ds_read_b64_tr_b16 a[80:81], v2 offset:4096\n"
    "ds_read_b64_tr_b16 a[82:83], v2 offset:5120\n"
    "s_waitcnt lgkmcnt(7)\n"
    "ds_read_b64_tr_b16 a[84:85], v2 offset:6144\n"
    "ds_read_b64_tr_b16 a[86:87], v2 offset:7168\n"
    "ds_read_b64_tr_b16 a[88:89], v3 offset:4096\n"
    "ds_read_b64_tr_b16 a[90:91], v3 offset:5120\n"
    "ds_read_b64_tr_b16 a[92:93], v3 offset:6144\n"
    "ds_read_b64_tr_b16 a[94:95], v3 offset:7168\n"
    "s_waitcnt vmcnt(8)\n"
    "ds_read_b64_tr_b16 a[192:193], v2 offset:8192\n"
    "s_waitcnt lgkmcnt(7)\n"
    "ds_read_b64_tr_b16 a[194:195], v2 offset:9216\n"
    "ds_read_b64_tr_b16 a[196:197], v2 offset:10240\n"
    "ds_read_b64_tr_b16 a[198:199], v2 offset:11264\n"
    "ds_read_b64_tr_b16 a[200:201], v3 offset:8192\n"
    "ds_read_b64_tr_b16 a[202:203], v3 offset:9216\n"
    "ds_read_b64_tr_b16 a[204:205], v3 offset:10240\n"
    "ds_read_b64_tr_b16 a[206:207], v3 offset:11264\n"
    "s_waitcnt vmcnt(4)\n"
    "s_waitcnt lgkmcnt(7)\n"
    "ds_read_b64_tr_b16 a[96:97], v2 offset:12288\n"
    "ds_read_b64_tr_b16 a[98:99], v2 offset:13312\n"
    "ds_read_b64_tr_b16 a[100:101], v2 offset:14336\n"
    "ds_read_b64_tr_b16 a[102:103], v2 offset:15360\n"
    "ds_read_b64_tr_b16 a[104:105], v3 offset:12288\n"
    "ds_read_b64_tr_b16 a[106:107], v3 offset:13312\n"
    "ds_read_b64_tr_b16 a[108:109], v3 offset:14336\n"
    "s_waitcnt lgkmcnt(7)\n"
    "ds_read_b64_tr_b16 a[110:111], v3 offset:15360\n"
    "s_waitcnt vmcnt(0)\n"
    "ds_read_b64_tr_b16 a[208:209], v2 offset:16384\n"
    "ds_read_b64_tr_b16 a[210:211], v2 offset:17408\n"
    "ds_read_b64_tr_b16 a[212:213], v2 offset:18432\n"
    "ds_read_b64_tr_b16 a[214:215], v2 offset:19456\n"
    "ds_read_b64_tr_b16 a[216:217], v3 offset:16384\n"
    "ds_read_b64_tr_b16 a[218:219], v3 offset:17408\n"
    "s_waitcnt lgkmcnt(7)\n"
    "ds_read_b64_tr_b16 a[220:221], v3 offset:18432\n"
    "ds_read_b64_tr_b16 a[222:223], v3 offset:19456\n"
    "s_waitcnt lgkmcnt(0)\n"
    "s_mov_b32 s71, 0\n"
    "s_mov_b32 s74, 0\n"
    "v_mov_b32 v2, v12\n"
    "v_mov_b32 v12, 0\n"
    "s_barrier\n"
    "RG_loop_%=:\n"
    "s_add_u32 s68, s44, 0\n"
    "s_lshr_b32 s75, s68, 4\n"
    "s_lshl_b32 s75, s75, 21\n"
    "s_lshl_b32 s68, s68, 17\n"
    "s_add_u32 s75, s75, s68\n"
    "s_add_u32 s68, s44, 2\n"
    "s_lshr_b32 s76, s68, 4\n"
    "s_lshl_b32 s76, s76, 21\n"
    "s_lshl_b32 s68, s68, 17\n"
    "s_add_u32 s76, s76, s68\n"
    "s_add_u32 s68, s44, 0\n"
    "s_lshr_b32 s77, s68, 5\n"
    "s_lshl_b32 s77, s77, 21\n"
    "s_lshl_b32 s68, s68, 16\n"
    "s_add_u32 s77, s77, s68\n"
    "s_add_u32 s76, s76, s41\n"
    "s_add_u32 s69, s75, s41\n"
    "s_add_u32 s75, s75, s46\n"
    "s_add_u32 s77, s77, s42\n"
    "s_cmp_lg_u32 s74, 0\n"
    "s_cbranch_scc1 RG_main_%=\n"
    "s_add_u32 s73, s44, 2\n"
    "s_lshr_b32 s73, s73, 1\n"
    "s_mov_b32 s72, 0\n"
    "RG_pa_%=:\n"
    "buffer_load_dword v13, v5, s[36:39], 0 offen sc1\n"
    "s_waitcnt vmcnt(0)\n"
    "v_cmp_gt_u32 vcc, s73, v13\n"
    "s_cbranch_vccz RG_qa_%=\n"
    "s_nop 0\n"
    "s_ff1_i32_b64 s94, vcc\n"
    "s_lshl_b64 exec, 1, s94\n"
    "RG_ra_%=:\n"
    "s_cmp_lg_u32 s71, 0\n"
    "s_cbranch_scc1 RG_qa_%=\n"
    "s_sleep 4\n"
    "buffer_load_dword v13, v5, s[36:39], 0 offen sc1\n"
    "s_waitcnt vmcnt(0)\n"
    "v_cmp_gt_u32 vcc, s73, v13\n"
    "s_add_u32 s72, s72, 1\n"
    "s_cmp_lt_u32 s72, 0x40000\n"
    "s_cselect_b32 s71, s71, 1\n"
    "s_cbranch_vccnz RG_ra_%=\n"
    "s_mov_b64 exec, -1\n"
    "s_branch RG_pa_%=\n"
    "RG_qa_%=:\n"
    "s_mov_b64 exec, -1\n"
    "s_add_u32 s68, s69, 0\n"
    "buffer_load_dwordx4 v[48:51], v0, s[36:39], s68 offen offset:0 sc1\n"
    "buffer_load_dwordx4 v[52:55], v0, s[36:39], s68 offen offset:1024 sc1\n"
    "buffer_load_dwordx4 v[56:59], v0, s[36:39], s68 offen offset:2048 sc1\n"
    "buffer_load_dwordx4 v[60:63], v0, s[36:39], s68 offen offset:3072 sc1\n"
    "s_add_u32 s68, s69, 4096\n"
    "buffer_load_dwordx4 v[64:67], v0, s[36:39], s68 offen offset:0 sc1\n"
    "buffer_load_dwordx4 v[68:71], v0, s[36:39], s68 offen offset:1024 sc1\n"
    "buffer_load_dwordx4 v[72:75], v0, s[36:39], s68 offen offset:2048 sc1\n"
    "buffer_load_dwordx4 v[76:79], v0, s[36:39], s68 offen offset:3072 sc1\n"
    "s_add_u32 s68, s69, 16384\n"
    "buffer_load_dwordx4 v[80:83], v0, s[36:39], s68 offen offset:0 sc1\n"
    "buffer_load_dwordx4 v[84:87], v0, s[36:39], s68 offen offset:1024 sc1\n"
    "buffer_load_dwordx4 v[88:91], v0, s[36:39], s68 offen offset:2048 sc1\n"
    "buffer_load_dwordx4 v[92:95], v0, s[36:39], s68 offen offset:3072 sc1\n"
    "s_add_u32 s68, s69, 20480\n"
    "buffer_load_dwordx4 v[96:99], v7, s[36:39], s68 offen offset:0 sc1\n"
    "buffer_load_dwordx4 v[100:103], v7, s[36:39], s68 offen offset:1024 sc1\n"
    "RG_main_%=:\n"
    "s_add_u32 s73, s44, 4\n"
    "s_lshr_b32 s73, s73, 1\n"
    "v_cmp_gt_u32 vcc, s73, v13\n"
    "s_add_u32 s73, s44, 6\n"
    "s_lshr_b32 s73, s73, 1\n"
    "v_cmp_gt_u32 s[94:95], s73, v13\n"
    "v_mov_b32 v14, 0x7ffffff0\n"
    "s_nop 0\n"
    "v_cndmask_b32 v14, v14, v5, s[94:95]\n"
    "buffer_load_dword v14, v14, s[36:39], 0 offen sc1\n"
    "buffer_load_dwordx2 v[32:33], v10, s[36:39], s77 offen sc1\n"
    "s_cmp_eq_u64 vcc, 0\n"
    "s_cselect_b32 s74, 1, 0\n"
    "s_cmp_ge_u32 s44, 126\n"
    "s_cselect_b32 s74, 0, s74\n"
    "s_cmp_lg_u32 s74, 0\n"
    "s_cselect_b32 s70, 0, 0x7ffffff0\n"
    "v_or_b32 v34, s70, v0\n"
    "v_or_b32 v35, s70, v7\n"
    "s_waitcnt vmcnt(15)\n"
    "v_mfma_f32_16x16x32_bf16 v[16:19], a[0:3], v[48:51], 0\n"
    "v_mfma_f32_16x16x32_bf16 v[20:23], a[8:11], v[48:51], 0\n"
    "v_mfma_f32_16x16x32_bf16 v[24:27], a[112:115], v[48:51], 0\n"
    "v_mfma_f32_16x16x32_bf16 v[28:31], a[120:123], v[48:51], 0\n"
    "s_add_u32 s68, s76, 0\n"
    "buffer_load_dwordx4 v[48:51], v34, s[36:39], s68 offen offset:0 sc1\n"
    "s_waitcnt vmcnt(15)\n"
    "v_mfma_f32_16x16x32_bf16 v[16:19], a[4:7], v[52:55], v[16:19]\n"
    "v_mfma_f32_16x16x32_bf16 v[20:23], a[12:15], v[52:55], v[20:23]\n"
    "v_mfma_f32_16x16x32_bf16 v[24:27], a[116:119], v[52:55], v[24:27]\n"
    "v_mfma_f32_16x16x32_bf16 v[28:31], a[124:127], v[52:55], v[28:31]\n"
    "buffer_load_dwordx4 v[52:55], v34, s[36:39], s68 offen offset:1024 sc1\n"
    "s_waitcnt vmcnt(15)\n"
    "v_mfma_f32_16x16x32_bf16 v[16:19], a[16:19], v[56:59], v[16:19]\n"
    "v_mfma_f32_16x16x32_bf16 v[20:23], a[24:27], v[56:59], v[20:23]\n"
    "v_mfma_f32_16x16x32_bf16 v[24:27], a[128:131], v[56:59], v[24:27]\n"
    "v_mfma_f32_16x16x32_bf16 v[28:31], a[136:139], v[56:59], v[28:31]\n"
    "buffer_load_dwordx4 v[56:59], v34, s[36:39], s68 offen offset:2048 sc1\n"
    "s_waitcnt vmcnt(15)\n"
    "v_mfma_f32_16x16x32_bf16 v[16:19], a[20:23], v[60:63], v[16:19]\n"
    "v_mfma_f32_16x16x32_bf16 v[20:23], a[28:31], v[60:63], v[20:23]\n"
    "v_mfma_f32_16x16x32_bf16 v[24:27], a[132:135], v[60:63], v[24:27]\n"
    "v_mfma_f32_16x16x32_bf16 v[28:31], a[140:143], v[60:63], v[28:31]\n"
    "buffer_load_dwordx4 v[60:63], v34, s[36:39], s68 offen offset:3072 sc1\n"
    "s_waitcnt vmcnt(15)\n"
    "v_mfma_f32_16x16x32_bf16 v[16:19], a[32:35], v[64:67], v[16:19]\n"
    "v_mfma_f32_16x16x32_bf16 v[20:23], a[40:43], v[64:67], v[20:23]\n"
    "v_mfma_f32_16x16x32_bf16 v[24:27], a[144:147], v[64:67], v[24:27]\n"
    "v_mfma_f32_16x16x32_bf16 v[28:31], a[152:155], v[64:67], v[28:31]\n"
    "s_add_u32 s68, s76, 4096\n"
    "buffer_load_dwordx4 v[64:67], v34, s[36:39], s68 offen offset:0 sc1\n"
    "s_waitcnt vmcnt(15)\n"
    "v_mfma_f32_16x16x32_bf16 v[16:19], a[36:39], v[68:71], v[16:19]\n"
    "v_mfma_f32_16x16x32_bf16 v[20:23], a[44:47], v[68:71], v[20:23]\n"
    "v_mfma_f32_16x16x32_bf16 v[24:27], a[148:151], v[68:71], v[24:27]\n"
    "v_mfma_f32_16x16x32_bf16 v[28:31], a[156:159], v[68:71], v[28:31]\n"
    "buffer_load_dwordx4 v[68:71], v34, s[36:39], s68 offen offset:1024 sc1\n"
    "s_waitcnt vmcnt(15)\n"
    "v_mfma_f32_16x16x32_bf16 v[16:19], a[48:51], v[72:75], v[16:19]\n"
    "v_mfma_f32_16x16x32_bf16 v[20:23], a[56:59], v[72:75], v[20:23]\n"
    "v_mfma_f32_16x16x32_bf16 v[24:27], a[160:163], v[72:75], v[24:27]\n"
    "v_mfma_f32_16x16x32_bf16 v[28:31], a[168:171], v[72:75], v[28:31]\n"
    "buffer_load_dwordx4 v[72:75], v34, s[36:39], s68 offen offset:2048 sc1\n"
    "s_waitcnt vmcnt(15)\n"
    "v_mfma_f32_16x16x32_bf16 v[16:19], a[52:55], v[76:79], v[16:19]\n"
    "v_mfma_f32_16x16x32_bf16 v[20:23], a[60:63], v[76:79], v[20:23]\n"
    "v_mfma_f32_16x16x32_bf16 v[24:27], a[164:167], v[76:79], v[24:27]\n"
    "v_mfma_f32_16x16x32_bf16 v[28:31], a[172:175], v[76:79], v[28:31]\n"
    "buffer_load_dwordx4 v[76:79], v34, s[36:39], s68 offen offset:3072 sc1\n"
    "s_waitcnt vmcnt(15)\n"
    "v_mfma_f32_16x16x32_bf16 v[16:19], a[64:67], v[80:83], v[16:19]\n"
    "v_mfma_f32_16x16x32_bf16 v[20:23], a[72:75], v[80:83], v[20:23]\n"
    "v_mfma_f32_16x16x32_bf16 v[24:27], a[176:179], v[80:83], v[24:27]\n"
    "v_mfma_f32_16x16x32_bf16 v[28:31], a[184:187], v[80:83], v[28:31]\n"
    "s_add_u32 s68, s76, 16384\n"
    "buffer_load_dwordx4 v[80:83], v34, s[36:39], s68 offen offset:0 sc1\n"
    "s_waitcnt vmcnt(15)\n"
    "v_mfma_f32_16x16x32_bf16 v[16:19], a[68:71], v[84:87], v[16:19]\n"
    "v_mfma_f32_16x16x32_bf16 v[20:23], a[76:79], v[84:87], v[20:23]\n"
    "v_mfma_f32_16x16x32_bf16 v[24:27], a[180:183], v[84:87], v[24:27]\n"
    "v_mfma_f32_16x16x32_bf16 v[28:31], a[188:191], v[84:87], v[28:31]\n"
    "buffer_load_dwordx4 v[84:87], v34, s[36:39], s68 offen offset:1024 sc1\n"
    "s_waitcnt vmcnt(15)\n"
    "v_mfma_f32_16x16x32_bf16 v[16:19], a[80:83], v[88:91], v[16:19]\n"
    "v_mfma_f32_16x16x32_bf16 v[20:23], a[88:91], v[88:91], v[20:23]\n"
    "v_mfma_f32_16x16x32_bf16 v[24:27], a[192:195], v[88:91], v[24:27]\n"
    "v_mfma_f32_16x16x32_bf16 v[28:31], a[200:203], v[88:91], v[28:31]\n"
    "buffer_load_dwordx4 v[88:91], v34, s[36:39], s68 offen offset:2048 sc1\n"
    "s_waitcnt vmcnt(15)\n"
    "v_mfma_f32_16x16x32_bf16 v[16:19], a[84:87], v[92:95], v[16:19]\n"
    "v_mfma_f32_16x16x32_bf16 v[20:23], a[92:95], v[92:95], v[20:23]\n"
    "v_mfma_f32_16x16x32_bf16 v[24:27], a[196:199], v[92:95], v[24:27]\n"
    "v_mfma_f32_16x16x32_bf16 v[28:31], a[204:207], v[92:95], v[28:31]\n"
    "buffer_load_dwordx4 v[92:95], v34, s[36:39], s68 offen offset:3072 sc1\n"
    "s_waitcnt vmcnt(15)\n"
    "v_mfma_f32_16x16x32_bf16 v[16:19], a[96:99], v[96:99], v[16:19]\n"
    "v_mfma_f32_16x16x32_bf16 v[20:23], a[104:107], v[96:99], v[20:23]\n"
    "v_mfma_f32_16x16x32_bf16 v[24:27], a[208:211], v[96:99], v[24:27]\n"
    "v_mfma_f32_16x16x32_bf16 v[28:31], a[216:219], v[96:99], v[28:31]\n"
    "s_add_u32 s68, s76, 20480\n"
    "buffer_load_dwordx4 v[96:99], v35, s[36:39], s68 offen offset:0 sc1\n"
    "s_waitcnt vmcnt(15)\n"
    "v_mfma_f32_16x16x32_bf16 v[16:19], a[100:103], v[100:103], v[16:19]\n"
    "v_mfma_f32_16x16x32_bf16 v[20:23], a[108:111], v[100:103], v[20:23]\n"
    "v_mfma_f32_16x16x32_bf16 v[24:27], a[212:215], v[100:103], v[24:27]\n"
    "v_mfma_f32_16x16x32_bf16 v[28:31], a[220:223], v[100:103], v[28:31]\n"
    "buffer_load_dwordx4 v[100:103], v35, s[36:39], s68 offen offset:1024 sc1\n"
    "s_nop 0\n"
    "ds_write_b128 v8, v[16:19] offset:0\n"
    "s_nop 0\n"
    "ds_write_b128 v8, v[20:23] offset:4096\n"
    "s_nop 0\n"
    "ds_write_b128 v8, v[24:27] offset:8192\n"
    "s_nop 0\n"
    "ds_write_b128 v8, v[28:31] offset:12288\n"
    "s_waitcnt vmcnt(14)\n"
    "v_max_u32 v13, v13, v14\n"
    "s_waitcnt lgkmcnt(0)\n"
    "s_barrier\n"
    "s_cmp_eq_u32 s45, 0\n"
    "s_cbranch_scc1 RG_next_%=\n"
    "ds_read_b128 v[16:19], v9 offset:0\n"
    "ds_read_b128 v[20:23], v9 offset:1024\n"
    "ds_read_b128 v[24:27], v9 offset:2048\n"
    "ds_read_b128 v[28:31], v9 offset:3072\n"
    "v_lshlrev_b32 v36, 16, v32\n"
    "v_and_b32 v37, 0xffff0000, v32\n"
    "v_lshlrev_b32 v38, 16, v33\n"
    "v_and_b32 v39, 0xffff0000, v33\n"
    "s_waitcnt lgkmcnt(2)\n"
    "v_pk_add_f32 v[16:17], v[16:17], v[20:21]\n"
    "v_pk_add_f32 v[18:19], v[18:19], v[22:23]\n"
    "s_waitcnt lgkmcnt(0)\n"
    "v_pk_add_f32 v[24:25], v[24:25], v[28:29]\n"
    "v_pk_add_f32 v[26:27], v[26:27], v[30:31]\n"
    "v_pk_add_f32 v[16:17], v[16:17], v[24:25]\n"
    "v_pk_add_f32 v[18:19], v[18:19], v[26:27]\n"
    "v_pk_add_f32 v[16:17], v[16:17], v[36:37]\n"
    "v_pk_add_f32 v[18:19], v[18:19], v[38:39]\n"
    "v_cvt_pk_bf16_f32 v36, v16, v17\n"
    "v_cvt_pk_bf16_f32 v37, v18, v19\n"
    "buffer_store_dwordx2 v[36:37], v6, s[56:59], s43 offen\n"
    "RG_next_%=:\n"
    "v_xor_b32 v8, 0x4000, v8\n"
    "v_xor_b32 v9, 0x4000, v9\n"
    "s_add_u32 s43, s43, 40960\n"
    "s_add_u32 s44, s44, 2\n"
    "s_cmp_lt_u32 s44, 128\n"
    "s_cbranch_scc1 RG_loop_%=\n"
    "buffer_load_dwordx2 v[36:37], v4, s[36:39], 0 offen sc1\n"
    "s_waitcnt vmcnt(0)\n"
    "v_add_u32 v36, 0x7f4a7c16, v36\n"
    "v_add_u32 v37, 0x9e3779b9, v37\n"
    "buffer_store_dwordx2 v[36:37], v4, s[36:39], 0 offen sc1\n"
    "s_waitcnt vmcnt(0)\n"
    "s_endpgm\n"
            :: "{v[0:15]}"(C), "{s[36:51]}"(SA), "{s[52:67]}"(SB) : CLOB_W);
        __builtin_unreachable();
    }
    {
        ROLE_BEGIN
        const int e = xi == 3 ? slot : 24 + 2 * (xi - 1) + (slot - 8);
        const int upr = e & 1, upj = e >> 1;
        const unsigned sd = read_side(p.ws), ne = comm_off(p.ws) + ((sd ^ 1) << 13), no = comm_off(p.ws) + (sd << 13);
        const int l = lane_id(), c = l & 15, g = l >> 4;
        const int sg = upj * 4 + w;
        v16u C = {}, SA = {}, SB = {};
        const unsigned stg = w * 36864;
        strip_consts(C, l, stg, F * 2, true);
        C[4] = l == 0 ? ne + NB_FA + upr * 16384 + sg * 128 : OOB;
        C[5] = ne + NB_FH + (l < 40 ? l : 0) * 128;
        C[6] = sg < 54 ? l * 16 : OOB;
        C[9] = 20480 + g * 1280 + c * 8; C[10] = l * 8;
        put_srd(SA, 0, p.ws, 0, WS_BYTES);
        SA[4] = stg; SA[5] = no + NB_HF + w * 16384; SA[6] = no + NB_AF + (sg / 14) * 32768 + spread((sg % 14) * 1024); SA[7] = upr; SA[8] = upr * 16384; SA[9] = w * 7168;
        put_srd(SB, 0, p.w1, sg < 54 ? sg * 64 : D * F * 2, D * F * 2); put_srd(SB, 4, p.w3, sg < 54 ? sg * 64 : D * F * 2, D * F * 2);
        uni(SA); uni(SB);
        asm volatile(
    "s_barrier\n"
    "s_add_u32 m0, s40, 0\n"
    "s_mov_b32 s68, 0\n"
    "buffer_load_dwordx4 v1, s[52:55], s68 offen lds\n"
    "s_add_u32 m0, s40, 1024\n"
    "s_mov_b32 s68, 55296\n"
    "buffer_load_dwordx4 v1, s[52:55], s68 offen lds\n"
    "s_add_u32 m0, s40, 2048\n"
    "s_mov_b32 s68, 110592\n"
    "buffer_load_dwordx4 v1, s[52:55], s68 offen lds\n"
    "s_add_u32 m0, s40, 3072\n"
    "s_mov_b32 s68, 165888\n"
    "buffer_load_dwordx4 v1, s[52:55], s68 offen lds\n"
    "s_add_u32 m0, s40, 4096\n"
    "s_mov_b32 s68, 221184\n"
    "buffer_load_dwordx4 v1, s[52:55], s68 offen lds\n"
    "s_add_u32 m0, s40, 5120\n"
    "s_mov_b32 s68, 276480\n"
    "buffer_load_dwordx4 v1, s[52:55], s68 offen lds\n"
    "s_add_u32 m0, s40, 6144\n"
    "s_mov_b32 s68, 331776\n"
    "buffer_load_dwordx4 v1, s[52:55], s68 offen lds\n"
    "s_add_u32 m0, s40, 7168\n"
    "s_mov_b32 s68, 387072\n"
    "buffer_load_dwordx4 v1, s[52:55], s68 offen lds\n"
    "s_add_u32 m0, s40, 8192\n"
    "s_mov_b32 s68, 442368\n"
    "buffer_load_dwordx4 v1, s[52:55], s68 offen lds\n"
    "s_add_u32 m0, s40, 9216\n"
    "s_mov_b32 s68, 497664\n"
    "buffer_load_dwordx4 v1, s[52:55], s68 offen lds\n"
    "s_add_u32 m0, s40, 10240\n"
    "s_mov_b32 s68, 552960\n"
    "buffer_load_dwordx4 v1, s[52:55], s68 offen lds\n"
    "s_add_u32 m0, s40, 11264\n"
    "s_mov_b32 s68, 608256\n"
    "buffer_load_dwordx4 v1, s[52:55], s68 offen lds\n"
    "s_add_u32 m0, s40, 12288\n"
    "s_mov_b32 s68, 663552\n"
    "buffer_load_dwordx4 v1, s[52:55], s68 offen lds\n"
    "s_add_u32 m0, s40, 13312\n"
    "s_mov_b32 s68, 718848\n"
    "buffer_load_dwordx4 v1, s[52:55], s68 offen lds\n"
    "s_add_u32 m0, s40, 14336\n"
    "s_mov_b32 s68, 774144\n"
    "buffer_load_dwordx4 v1, s[52:55], s68 offen lds\n"
    "s_add_u32 m0, s40, 15360\n"
    "s_mov_b32 s68, 829440\n"
    "buffer_load_dwordx4 v1, s[52:55], s68 offen lds\n"
    "s_add_u32 m0, s40, 16384\n"
    "s_mov_b32 s68, 884736\n"
    "buffer_load_dwordx4 v1, s[52:55], s68 offen lds\n"
    "s_add_u32 m0, s40, 17408\n"
    "s_mov_b32 s68, 940032\n"
    "buffer_load_dwordx4 v1, s[52:55], s68 offen lds\n"
    "s_add_u32 m0, s40, 18432\n"
    "s_mov_b32 s68, 995328\n"
    "buffer_load_dwordx4 v1, s[52:55], s68 offen lds\n"
    "s_add_u32 m0, s40, 19456\n"
    "s_mov_b32 s68, 1050624\n"
    "buffer_load_dwordx4 v1, s[52:55], s68 offen lds\n"
    "s_add_u32 m0, s40, 20480\n"
    "s_mov_b32 s68, 1105920\n"
    "buffer_load_dwordx4 v1, s[52:55], s68 offen lds\n"
    "s_add_u32 m0, s40, 21504\n"
    "s_mov_b32 s68, 1161216\n"
    "buffer_load_dwordx4 v1, s[52:55], s68 offen lds\n"
    "s_add_u32 m0, s40, 22528\n"
    "s_mov_b32 s68, 1216512\n"
    "buffer_load_dwordx4 v1, s[52:55], s68 offen lds\n"
    "s_add_u32 m0, s40, 23552\n"
    "s_mov_b32 s68, 1271808\n"
    "buffer_load_dwordx4 v1, s[52:55], s68 offen lds\n"
    "s_add_u32 m0, s40, 24576\n"
    "s_mov_b32 s68, 1327104\n"
    "buffer_load_dwordx4 v1, s[52:55], s68 offen lds\n"
    "s_add_u32 m0, s40, 25600\n"
    "s_mov_b32 s68, 1382400\n"
    "buffer_load_dwordx4 v1, s[52:55], s68 offen lds\n"
    "s_add_u32 m0, s40, 26624\n"
    "s_mov_b32 s68, 1437696\n"
    "buffer_load_dwordx4 v1, s[52:55], s68 offen lds\n"
    "s_add_u32 m0, s40, 27648\n"
    "s_mov_b32 s68, 1492992\n"
    "buffer_load_dwordx4 v1, s[52:55], s68 offen lds\n"
    "s_add_u32 m0, s40, 28672\n"
    "s_mov_b32 s68, 1548288\n"
    "buffer_load_dwordx4 v1, s[52:55], s68 offen lds\n"
    "s_add_u32 m0, s40, 29696\n"
    "s_mov_b32 s68, 1603584\n"
    "buffer_load_dwordx4 v1, s[52:55], s68 offen lds\n"
    "s_add_u32 m0, s40, 30720\n"
    "s_mov_b32 s68, 1658880\n"
    "buffer_load_dwordx4 v1, s[52:55], s68 offen lds\n"
    "s_add_u32 m0, s40, 31744\n"
    "s_mov_b32 s68, 1714176\n"
    "buffer_load_dwordx4 v1, s[52:55], s68 offen lds\n"
    "s_add_u32 m0, s40, 32768\n"
    "s_mov_b32 s68, 1769472\n"
    "buffer_load_dwordx4 v1, s[52:55], s68 offen lds\n"
    "s_add_u32 m0, s40, 33792\n"
    "s_mov_b32 s68, 1824768\n"
    "buffer_load_dwordx4 v1, s[52:55], s68 offen lds\n"
    "s_add_u32 m0, s40, 34816\n"
    "s_mov_b32 s68, 1880064\n"
    "buffer_load_dwordx4 v1, s[52:55], s68 offen lds\n"
    "s_add_u32 m0, s40, 35840\n"
    "s_mov_b32 s68, 1935360\n"
    "buffer_load_dwordx4 v1, s[52:55], s68 offen lds\n"
    "s_waitcnt vmcnt(32)\n"
    "ds_read_b64_tr_b16 a[0:1], v2 offset:0\n"
    "ds_read_b64_tr_b16 a[2:3], v2 offset:1024\n"
    "ds_read_b64_tr_b16 a[4:5], v2 offset:2048\n"
    "ds_read_b64_tr_b16 a[6:7], v2 offset:3072\n"
    "ds_read_b64_tr_b16 a[8:9], v3 offset:0\n"
    "ds_read_b64_tr_b16 a[10:11], v3 offset:1024\n"
    "ds_read_b64_tr_b16 a[12:13], v3 offset:2048\n"
    "ds_read_b64_tr_b16 a[14:15], v3 offset:3072\n"
    "s_waitcnt lgkmcnt(0)\n"
    "s_add_u32 m0, s40, 0\n"
    "s_mov_b32 s68, 1990656\n"
    "buffer_load_dwordx4 v1, s[52:55], s68 offen lds\n"
    "s_add_u32 m0, s40, 1024\n"
    "s_mov_b32 s68, 2045952\n"
    "buffer_load_dwordx4 v1, s[52:55], s68 offen lds\n"
    "s_add_u32 m0, s40, 2048\n"
    "s_mov_b32 s68, 2101248\n"
    "buffer_load_dwordx4 v1, s[52:55], s68 offen lds\n"
    "s_add_u32 m0, s40, 3072\n"
    "s_mov_b32 s68, 2156544\n"
    "buffer_load_dwordx4 v1, s[52:55], s68 offen lds\n"
    "s_waitcnt vmcnt(32)\n"
    "ds_read_b64_tr_b16 a[16:17], v2 offset:4096\n"
    "ds_read_b64_tr_b16 a[18:19], v2 offset:5120\n"
    "ds_read_b64_tr_b16 a[20:21], v2 offset:6144\n"
    "ds_read_b64_tr_b16 a[22:23], v2 offset:7168\n"
    "ds_read_b64_tr_b16 a[24:25], v3 offset:4096\n"
    "ds_read_b64_tr_b16 a[26:27], v3 offset:5120\n"
    "ds_read_b64_tr_b16 a[28:29], v3 offset:6144\n"
    "ds_read_b64_tr_b16 a[30:31], v3 offset:7168\n"
    "s_waitcnt lgkmcnt(0)\n"
    "s_add_u32 m0, s40, 4096\n"
    "s_mov_b32 s68, 0\n"
    "buffer_load_dwordx4 v1, s[56:59], s68 offen lds\n"
    "s_add_u32 m0, s40, 5120\n"
    "s_mov_b32 s68, 55296\n"
    "buffer_load_dwordx4 v1, s[56:59], s68 offen lds\n"
    "s_add_u32 m0, s40, 6144\n"
    "s_mov_b32 s68, 110592\n"
    "buffer_load_dwordx4 v1, s[56:59], s68 offen lds\n"
    "s_add_u32 m0, s40, 7168\n"
    "s_mov_b32 s68, 165888\n"
    "buffer_load_dwordx4 v1, s[56:59], s68 offen lds\n"
    "s_waitcnt vmcnt(32)\n"
    "ds_read_b64_tr_b16 a[32:33], v2 offset:8192\n"
    "ds_read_b64_tr_b16 a[34:35], v2 offset:9216\n"
    "ds_read_b64_tr_b16 a[36:37], v2 offset:10240\n"
    "ds_read_b64_tr_b16 a[38:39], v2 offset:11264\n"
    "ds_read_b64_tr_b16 a[40:41], v3 offset:8192\n"
    "ds_read_b64_tr_b16 a[42:43], v3 offset:9216\n"
    "ds_read_b64_tr_b16 a[44:45], v3 offset:10240\n"
    "ds_read_b64_tr_b16 a[46:47], v3 offset:11264\n"
    "s_waitcnt lgkmcnt(0)\n"
    "s_add_u32 m0, s40, 8192\n"
    "s_mov_b32 s68, 221184\n"
    "buffer_load_dwordx4 v1, s[56:59], s68 offen lds\n"
    "s_add_u32 m0, s40, 9216\n"
    "s_mov_b32 s68, 276480\n"
    "buffer_load_dwordx4 v1, s[56:59], s68 offen lds\n"
    "s_add_u32 m0, s40, 10240\n"
    "s_mov_b32 s68, 331776\n"
    "buffer_load_dwordx4 v1, s[56:59], s68 offen lds\n"
    "s_add_u32 m0, s40, 11264\n"
    "s_mov_b32 s68, 387072\n"
    "buffer_load_dwordx4 v1, s[56:59], s68 offen lds\n"
    "s_waitcnt vmcnt(32)\n"
    "ds_read_b64_tr_b16 a[48:49], v2 offset:12288\n"
    "ds_read_b64_tr_b16 a[50:51], v2 offset:13312\n"
    "ds_read_b64_tr_b16 a[52:53], v2 offset:14336\n"
    "ds_read_b64_tr_b16 a[54:55], v2 offset:15360\n"
    "ds_read_b64_tr_b16 a[56:57], v3 offset:12288\n"
    "ds_read_b64_tr_b16 a[58:59], v3 offset:13312\n"
    "ds_read_b64_tr_b16 a[60:61], v3 offset:14336\n"
    "ds_read_b64_tr_b16 a[62:63], v3 offset:15360\n"
    "s_waitcnt lgkmcnt(0)\n"
    "s_add_u32 m0, s40, 12288\n"
    "s_mov_b32 s68, 442368\n"
    "buffer_load_dwordx4 v1, s[56:59], s68 offen lds\n"
    "s_add_u32 m0, s40, 13312\n"
    "s_mov_b32 s68, 497664\n"
    "buffer_load_dwordx4 v1, s[56:59], s68 offen lds\n"
    "s_add_u32 m0, s40, 14336\n"
    "s_mov_b32 s68, 552960\n"
    "buffer_load_dwordx4 v1, s[56:59], s68 offen lds\n"
    "s_add_u32 m0, s40, 15360\n"
    "s_mov_b32 s68, 608256\n"
    "buffer_load_dwordx4 v1, s[56:59], s68 offen lds\n"
    "s_waitcnt vmcnt(32)\n"
    "ds_read_b64_tr_b16 a[64:65], v2 offset:16384\n"
    "ds_read_b64_tr_b16 a[66:67], v2 offset:17408\n"
    "ds_read_b64_tr_b16 a[68:69], v2 offset:18432\n"
    "ds_read_b64_tr_b16 a[70:71], v2 offset:19456\n"
    "ds_read_b64_tr_b16 a[72:73], v3 offset:16384\n"
    "ds_read_b64_tr_b16 a[74:75], v3 offset:17408\n"
    "ds_read_b64_tr_b16 a[76:77], v3 offset:18432\n"
    "ds_read_b64_tr_b16 a[78:79], v3 offset:19456\n"
    "s_waitcnt lgkmcnt(0)\n"
    "s_add_u32 m0, s40, 16384\n"
    "s_mov_b32 s68, 663552\n"
    "buffer_load_dwordx4 v1, s[56:59], s68 offen lds\n"
    "s_add_u32 m0, s40, 17408\n"
    "s_mov_b32 s68, 718848\n"
    "buffer_load_dwordx4 v1, s[56:59], s68 offen lds\n"
    "s_add_u32 m0, s40, 18432\n"
    "s_mov_b32 s68, 774144\n"
    "buffer_load_dwordx4 v1, s[56:59], s68 offen lds\n"
    "s_add_u32 m0, s40, 19456\n"
    "s_mov_b32 s68, 829440\n"
    "buffer_load_dwordx4 v1, s[56:59], s68 offen lds\n"
    "s_waitcnt vmcnt(32)\n"
    "ds_read_b64_tr_b16 a[80:81], v2 offset:20480\n"
    "ds_read_b64_tr_b16 a[82:83], v2 offset:21504\n"
    "ds_read_b64_tr_b16 a[84:85], v2 offset:22528\n"
    "ds_read_b64_tr_b16 a[86:87], v2 offset:23552\n"
    "ds_read_b64_tr_b16 a[88:89], v3 offset:20480\n"
    "ds_read_b64_tr_b16 a[90:91], v3 offset:21504\n"
    "ds_read_b64_tr_b16 a[92:93], v3 offset:22528\n"
    "ds_read_b64_tr_b16 a[94:95], v3 offset:23552\n"
    "s_waitcnt lgkmcnt(0)\n"
    "s_add_u32 m0, s40, 20480\n"
    "s_mov_b32 s68, 884736\n"
    "buffer_load_dwordx4 v1, s[56:59], s68 offen lds\n"
    "s_add_u32 m0, s40, 21504\n"
    "s_mov_b32 s68, 940032\n"
    "buffer_load_dwordx4 v1, s[56:59], s68 offen lds\n"
    "s_add_u32 m0, s40, 22528\n"
    "s_mov_b32 s68, 995328\n"
    "buffer_load_dwordx4 v1, s[56:59], s68 offen lds\n"
    "s_add_u32 m0, s40, 23552\n"
    "s_mov_b32 s68, 1050624\n"
    "buffer_load_dwordx4 v1, s[56:59], s68 offen lds\n"
    "s_waitcnt vmcnt(32)\n"
    "ds_read_b64_tr_b16 a[96:97], v2 offset:24576\n"
    "ds_read_b64_tr_b16 a[98:99], v2 offset:25600\n"
    "ds_read_b64_tr_b16 a[100:101], v2 offset:26624\n"
    "ds_read_b64_tr_b16 a[102:103], v2 offset:27648\n"
    "ds_read_b64_tr_b16 a[104:105], v3 offset:24576\n"
    "ds_read_b64_tr_b16 a[106:107], v3 offset:25600\n"
    "ds_read_b64_tr_b16 a[108:109], v3 offset:26624\n"
    "ds_read_b64_tr_b16 a[110:111], v3 offset:27648\n"
    "s_waitcnt lgkmcnt(0)\n"
    "s_add_u32 m0, s40, 24576\n"
    "s_mov_b32 s68, 1105920\n"
    "buffer_load_dwordx4 v1, s[56:59], s68 offen lds\n"
    "s_add_u32 m0, s40, 25600\n"
    "s_mov_b32 s68, 1161216\n"
    "buffer_load_dwordx4 v1, s[56:59], s68 offen lds\n"
    "s_add_u32 m0, s40, 26624\n"
    "s_mov_b32 s68, 1216512\n"
    "buffer_load_dwordx4 v1, s[56:59], s68 offen lds\n"
    "s_add_u32 m0, s40, 27648\n"
    "s_mov_b32 s68, 1271808\n"
    "buffer_load_dwordx4 v1, s[56:59], s68 offen lds\n"
    "s_waitcnt vmcnt(32)\n"
    "ds_read_b64_tr_b16 a[112:113], v2 offset:28672\n"
    "ds_read_b64_tr_b16 a[114:115], v2 offset:29696\n"
    "ds_read_b64_tr_b16 a[116:117], v2 offset:30720\n"
    "ds_read_b64_tr_b16 a[118:119], v2 offset:31744\n"
    "ds_read_b64_tr_b16 a[120:121], v3 offset:28672\n"
    "ds_read_b64_tr_b16 a[122:123], v3 offset:29696\n"
    "ds_read_b64_tr_b16 a[124:125], v3 offset:30720\n"
    "ds_read_b64_tr_b16 a[126:127], v3 offset:31744\n"
    "s_waitcnt lgkmcnt(0)\n"
    "s_add_u32 m0, s40, 28672\n"
    "s_mov_b32 s68, 1327104\n"
    "buffer_load_dwordx4 v1, s[56:59], s68 offen lds\n"
    "s_add_u32 m0, s40, 29696\n"
    "s_mov_b32 s68, 1382400\n"
    "buffer_load_dwordx4 v1, s[56:59], s68 offen lds\n"
    "s_add_u32 m0, s40, 30720\n"
    "s_mov_b32 s68, 1437696\n"
    "buffer_load_dwordx4 v1, s[56:59], s68 offen lds\n"
    "s_add_u32 m0, s40, 31744\n"
    "s_mov_b32 s68, 1492992\n"
    "buffer_load_dwordx4 v1, s[56:59], s68 offen lds\n"
    "s_waitcnt vmcnt(32)\n"
    "ds_read_b64_tr_b16 a[128:129], v2 offset:32768\n"
    "ds_read_b64_tr_b16 a[130:131], v2 offset:33792\n"
    "ds_read_b64_tr_b16 a[132:133], v2 offset:34816\n"
    "ds_read_b64_tr_b16 a[134:135], v2 offset:35840\n"
    "ds_read_b64_tr_b16 a[136:137], v3 offset:32768\n"
    "ds_read_b64_tr_b16 a[138:139], v3 offset:33792\n"
    "ds_read_b64_tr_b16 a[140:141], v3 offset:34816\n"
    "ds_read_b64_tr_b16 a[142:143], v3 offset:35840\n"
    "s_waitcnt lgkmcnt(0)\n"
    "s_add_u32 m0, s40, 32768\n"
    "s_mov_b32 s68, 1548288\n"
    "buffer_load_dwordx4 v1, s[56:59], s68 offen lds\n"
    "s_add_u32 m0, s40, 33792\n"
    "s_mov_b32 s68, 1603584\n"
    "buffer_load_dwordx4 v1, s[56:59], s68 offen lds\n"
    "s_add_u32 m0, s40, 34816\n"
    "s_mov_b32 s68, 1658880\n"
    "buffer_load_dwordx4 v1, s[56:59], s68 offen lds\n"
    "s_add_u32 m0, s40, 35840\n"
    "s_mov_b32 s68, 1714176\n"
    "buffer_load_dwordx4 v1, s[56:59], s68 offen lds\n"
    "s_waitcnt vmcnt(32)\n"
    "ds_read_b64_tr_b16 a[144:145], v2 offset:0\n"
    "ds_read_b64_tr_b16 a[146:147], v2 offset:1024\n"
    "ds_read_b64_tr_b16 a[148:149], v2 offset:2048\n"
    "ds_read_b64_tr_b16 a[150:151], v2 offset:3072\n"
    "ds_read_b64_tr_b16 a[152:153], v3 offset:0\n"
    "ds_read_b64_tr_b16 a[154:155], v3 offset:1024\n"
    "ds_read_b64_tr_b16 a[156:157], v3 offset:2048\n"
    "ds_read_b64_tr_b16 a[158:159], v3 offset:3072\n"
    "s_waitcnt lgkmcnt(0)\n"
    "s_add_u32 m0, s40, 0\n"
    "s_mov_b32 s68, 1769472\n"
    "buffer_load_dwordx4 v1, s[56:59], s68 offen lds\n"
    "s_add_u32 m0, s40, 1024\n"
    "s_mov_b32 s68, 1824768\n"
    "buffer_load_dwordx4 v1, s[56:59], s68 offen lds\n"
    "s_add_u32 m0, s40, 2048\n"
    "s_mov_b32 s68, 1880064\n"
    "buffer_load_dwordx4 v1, s[56:59], s68 offen lds\n"
    "s_add_u32 m0, s40, 3072\n"
    "s_mov_b32 s68, 1935360\n"
    "buffer_load_dwordx4 v1, s[56:59], s68 offen lds\n"
    "s_waitcnt vmcnt(32)\n"
    "ds_read_b64_tr_b16 a[160:161], v2 offset:4096\n"
    "ds_read_b64_tr_b16 a[162:163], v2 offset:5120\n"
    "ds_read_b64_tr_b16 a[164:165], v2 offset:6144\n"
    "ds_read_b64_tr_b16 a[166:167], v2 offset:7168\n"
    "ds_read_b64_tr_b16 a[168:169], v3 offset:4096\n"
    "ds_read_b64_tr_b16 a[170:171], v3 offset:5120\n"
    "ds_read_b64_tr_b16 a[172:173], v3 offset:6144\n"
    "ds_read_b64_tr_b16 a[174:175], v3 offset:7168\n"
    "s_waitcnt lgkmcnt(0)\n"
    "s_add_u32 m0, s40, 4096\n"
    "s_mov_b32 s68, 1990656\n"
    "buffer_load_dwordx4 v1, s[56:59], s68 offen lds\n"
    "s_add_u32 m0, s40, 5120\n"
    "s_mov_b32 s68, 2045952\n"
    "buffer_load_dwordx4 v1, s[56:59], s68 offen lds\n"
    "s_add_u32 m0, s40, 6144\n"
    "s_mov_b32 s68, 2101248\n"
    "buffer_load_dwordx4 v1, s[56:59], s68 offen lds\n"
    "s_add_u32 m0, s40, 7168\n"
    "s_mov_b32 s68, 2156544\n"
    "buffer_load_dwordx4 v1, s[56:59], s68 offen lds\n"
    "s_waitcnt vmcnt(32)\n"
    "ds_read_b64_tr_b16 a[176:177], v2 offset:8192\n"
    "ds_read_b64_tr_b16 a[178:179], v2 offset:9216\n"
    "ds_read_b64_tr_b16 a[180:181], v2 offset:10240\n"
    "ds_read_b64_tr_b16 a[182:183], v2 offset:11264\n"
    "ds_read_b64_tr_b16 a[184:185], v3 offset:8192\n"
    "ds_read_b64_tr_b16 a[186:187], v3 offset:9216\n"
    "ds_read_b64_tr_b16 a[188:189], v3 offset:10240\n"
    "ds_read_b64_tr_b16 a[190:191], v3 offset:11264\n"
    "s_waitcnt vmcnt(28)\n"
    "ds_read_b64_tr_b16 a[192:193], v2 offset:12288\n"
    "ds_read_b64_tr_b16 a[194:195], v2 offset:13312\n"
    "ds_read_b64_tr_b16 a[196:197], v2 offset:14336\n"
    "ds_read_b64_tr_b16 a[198:199], v2 offset:15360\n"
    "ds_read_b64_tr_b16 a[200:201], v3 offset:12288\n"
    "ds_read_b64_tr_b16 a[202:203], v3 offset:13312\n"
    "s_waitcnt lgkmcnt(7)\n"
    "ds_read_b64_tr_b16 a[204:205], v3 offset:14336\n"
    "ds_read_b64_tr_b16 a[206:207], v3 offset:15360\n"
    "s_waitcnt vmcnt(24)\n"
    "ds_read_b64_tr_b16 a[208:209], v2 offset:16384\n"
    "ds_read_b64_tr_b16 a[210:211], v2 offset:17408\n"
    "ds_read_b64_tr_b16 a[212:213], v2 offset:18432\n"
    "ds_read_b64_tr_b16 a[214:215], v2 offset:19456\n"
    "ds_read_b64_tr_b16 a[216:217], v3 offset:16384\n"
    "s_waitcnt lgkmcnt(7)\n"
    "ds_read_b64_tr_b16 a[218:219], v3 offset:17408\n"
    "ds_read_b64_tr_b16 a[220:221], v3 offset:18432\n"
    "ds_read_b64_tr_b16 a[222:223], v3 offset:19456\n"
    "s_waitcnt vmcnt(20)\n"
    "ds_read_b64_tr_b16 a[224:225], v2 offset:20480\n"
    "ds_read_b64_tr_b16 a[226:227], v2 offset:21504\n"
    "ds_read_b64_tr_b16 a[228:229], v2 offset:22528\n"
    "ds_read_b64_tr_b16 a[230:231], v2 offset:23552\n"
    "s_waitcnt lgkmcnt(7)\n"
    "ds_read_b64_tr_b16 a[232:233], v3 offset:20480\n"
    "ds_read_b64_tr_b16 a[234:235], v3 offset:21504\n"
    "ds_read_b64_tr_b16 a[236:237], v3 offset:22528\n"
    "ds_read_b64_tr_b16 a[238:239], v3 offset:23552\n"
    "s_waitcnt vmcnt(16)\n"
    "ds_read_b64_tr_b16 a[240:241], v2 offset:24576\n"
    "ds_read_b64_tr_b16 a[242:243], v2 offset:25600\n"
    "ds_read_b64_tr_b16 a[244:245], v2 offset:26624\n"
    "s_waitcnt lgkmcnt(7)\n"
    "ds_read_b64_tr_b16 a[246:247], v2 offset:27648\n"
    "ds_read_b64_tr_b16 a[248:249], v3 offset:24576\n"
    "ds_read_b64_tr_b16 a[250:251], v3 offset:25600\n"
    "ds_read_b64_tr_b16 a[252:253], v3 offset:26624\n"
    "ds_read_b64_tr_b16 a[254:255], v3 offset:27648\n"
    "s_waitcnt vmcnt(12)\n"
    "ds_read_b64_tr_b16 v[192:193], v2 offset:28672\n"
    "ds_read_b64_tr_b16 v[194:195], v2 offset:29696\n"
    "s_waitcnt lgkmcnt(7)\n"
    "ds_read_b64_tr_b16 v[196:197], v2 offset:30720\n"
    "ds_read_b64_tr_b16 v[198:199], v2 offset:31744\n"
    "ds_read_b64_tr_b16 v[200:201], v3 offset:28672\n"
    "ds_read_b64_tr_b16 v[202:203], v3 offset:29696\n"
    "ds_read_b64_tr_b16 v[204:205], v3 offset:30720\n"
    "ds_read_b64_tr_b16 v[206:207], v3 offset:31744\n"
    "s_waitcnt vmcnt(8)\n"
    "ds_read_b64_tr_b16 v[208:209], v2 offset:32768\n"
    "s_waitcnt lgkmcnt(7)\n"
    "ds_read_b64_tr_b16 v[210:211], v2 offset:33792\n"
    "ds_read_b64_tr_b16 v[212:213], v2 offset:34816\n"
    "ds_read_b64_tr_b16 v[214:215], v2 offset:35840\n"
    "ds_read_b64_tr_b16 v[216:217], v3 offset:32768\n"
    "ds_read_b64_tr_b16 v[218:219], v3 offset:33792\n"
    "ds_read_b64_tr_b16 v[220:221], v3 offset:34816\n"
    "ds_read_b64_tr_b16 v[222:223], v3 offset:35840\n"
    "s_waitcnt vmcnt(4)\n"
    "s_waitcnt lgkmcnt(7)\n"
    "ds_read_b64_tr_b16 v[224:225], v2 offset:0\n"
    "ds_read_b64_tr_b16 v[226:227], v2 offset:1024\n"
    "ds_read_b64_tr_b16 v[228:229], v2 offset:2048\n"
    "ds_read_b64_tr_b16 v[230:231], v2 offset:3072\n"
    "ds_read_b64_tr_b16 v[232:233], v3 offset:0\n"
    "ds_read_b64_tr_b16 v[234:235], v3 offset:1024\n"
    "ds_read_b64_tr_b16 v[236:237], v3 offset:2048\n"
    "s_waitcnt lgkmcnt(7)\n"
    "ds_read_b64_tr_b16 v[238:239], v3 offset:3072\n"
    "s_waitcnt vmcnt(0)\n"
    "ds_read_b64_tr_b16 v[240:241], v2 offset:4096\n"
    "ds_read_b64_tr_b16 v[242:243], v2 offset:5120\n"
    "ds_read_b64_tr_b16 v[244:245], v2 offset:6144\n"
    "ds_read_b64_tr_b16 v[246:247], v2 offset:7168\n"
    "ds_read_b64_tr_b16 v[248:249], v3 offset:4096\n"
    "ds_read_b64_tr_b16 v[250:251], v3 offset:5120\n"
    "s_waitcnt lgkmcnt(7)\n"
    "ds_read_b64_tr_b16 v[252:253], v3 offset:6144\n"
    "ds_read_b64_tr_b16 v[254:255], v3 offset:7168\n"
    "s_waitcnt lgkmcnt(0)\n"
    "v_mov_b32 v80, 0x3f803f80\n"
    "v_mov_b32 v81, 0x3f803f80\n"
    "v_mov_b32 v82, 0x3f803f80\n"
    "v_mov_b32 v83, 0x3f803f80\n"
    "s_nop 1\n"
    "v_mfma_f32_16x16x32_bf16 v[16:19], a[0:3], v[80:83], 0\n"
    "v_mfma_f32_16x16x32_bf16 v[20:23], a[8:11], v[80:83], 0\n"
    "v_mfma_f32_16x16x32_bf16 v[24:27], a[160:163], v[80:83], 0\n"
    "v_mfma_f32_16x16x32_bf16 v[28:31], a[168:171], v[80:83], 0\n"
    "v_mfma_f32_16x16x32_bf16 v[16:19], a[4:7], v[80:83], v[16:19]\n"
    "v_mfma_f32_16x16x32_bf16 v[20:23], a[12:15], v[80:83], v[20:23]\n"
    "v_mfma_f32_16x16x32_bf16 v[24:27], a[164:167], v[80:83], v[24:27]\n"
    "v_mfma_f32_16x16x32_bf16 v[28:31], a[172:175], v[80:83], v[28:31]\n"
    "v_mfma_f32_16x16x32_bf16 v[16:19], a[16:19], v[80:83], v[16:19]\n"
    "v_mfma_f32_16x16x32_bf16 v[20:23], a[24:27], v[80:83], v[20:23]\n"
    "v_mfma_f32_16x16x32_bf16 v[24:27], a[176:179], v[80:83], v[24:27]\n"
    "v_mfma_f32_16x16x32_bf16 v[28:31], a[184:187], v[80:83], v[28:31]\n"
    "v_mfma_f32_16x16x32_bf16 v[16:19], a[20:23], v[80:83], v[16:19]\n"
    "v_mfma_f32_16x16x32_bf16 v[20:23], a[28:31], v[80:83], v[20:23]\n"
    "v_mfma_f32_16x16x32_bf16 v[24:27], a[180:183], v[80:83], v[24:27]\n"
    "v_mfma_f32_16x16x32_bf16 v[28:31], a[188:191], v[80:83], v[28:31]\n"
    "v_mfma_f32_16x16x32_bf16 v[16:19], a[32:35], v[80:83], v[16:19]\n"
    "v_mfma_f32_16x16x32_bf16 v[20:23], a[40:43], v[80:83], v[20:23]\n"
    "v_mfma_f32_16x16x32_bf16 v[24:27], a[192:195], v[80:83], v[24:27]\n"
    "v_mfma_f32_16x16x32_bf16 v[28:31], a[200:203], v[80:83], v[28:31]\n"
    "v_mfma_f32_16x16x32_bf16 v[16:19], a[36:39], v[80:83], v[16:19]\n"
    "v_mfma_f32_16x16x32_bf16 v[20:23], a[44:47], v[80:83], v[20:23]\n"
    "v_mfma_f32_16x16x32_bf16 v[24:27], a[196:199], v[80:83], v[24:27]\n"
    "v_mfma_f32_16x16x32_bf16 v[28:31], a[204:207], v[80:83], v[28:31]\n"
    "v_mfma_f32_16x16x32_bf16 v[16:19], a[48:51], v[80:83], v[16:19]\n"
    "v_mfma_f32_16x16x32_bf16 v[20:23], a[56:59], v[80:83], v[20:23]\n"
    "v_mfma_f32_16x16x32_bf16 v[24:27], a[208:211], v[80:83], v[24:27]\n"
    "v_mfma_f32_16x16x32_bf16 v[28:31], a[216:219], v[80:83], v[28:31]\n"
    "v_mfma_f32_16x16x32_bf16 v[16:19], a[52:55], v[80:83], v[16:19]\n"
    "v_mfma_f32_16x16x32_bf16 v[20:23], a[60:63], v[80:83], v[20:23]\n"
    "v_mfma_f32_16x16x32_bf16 v[24:27], a[212:215], v[80:83], v[24:27]\n"
    "v_mfma_f32_16x16x32_bf16 v[28:31], a[220:223], v[80:83], v[28:31]\n"
    "v_mfma_f32_16x16x32_bf16 v[16:19], a[64:67], v[80:83], v[16:19]\n"
    "v_mfma_f32_16x16x32_bf16 v[20:23], a[72:75], v[80:83], v[20:23]\n"
    "v_mfma_f32_16x16x32_bf16 v[24:27], a[224:227], v[80:83], v[24:27]\n"
    "v_mfma_f32_16x16x32_bf16 v[28:31], a[232:235], v[80:83], v[28:31]\n"
    "v_mfma_f32_16x16x32_bf16 v[16:19], a[68:71], v[80:83], v[16:19]\n"
    "v_mfma_f32_16x16x32_bf16 v[20:23], a[76:79], v[80:83], v[20:23]\n"
    "v_mfma_f32_16x16x32_bf16 v[24:27], a[228:231], v[80:83], v[24:27]\n"
    "v_mfma_f32_16x16x32_bf16 v[28:31], a[236:239], v[80:83], v[28:31]\n"
    "v_mfma_f32_16x16x32_bf16 v[16:19], a[80:83], v[80:83], v[16:19]\n"
    "v_mfma_f32_16x16x32_bf16 v[20:23], a[88:91], v[80:83], v[20:23]\n"
    "v_mfma_f32_16x16x32_bf16 v[24:27], a[240:243], v[80:83], v[24:27]\n"
    "v_mfma_f32_16x16x32_bf16 v[28:31], a[248:251], v[80:83], v[28:31]\n"
    "v_mfma_f32_16x16x32_bf16 v[16:19], a[84:87], v[80:83], v[16:19]\n"
    "v_mfma_f32_16x16x32_bf16 v[20:23], a[92:95], v[80:83], v[20:23]\n"
    "v_mfma_f32_16x16x32_bf16 v[24:27], a[244:247], v[80:83], v[24:27]\n"
    "v_mfma_f32_16x16x32_bf16 v[28:31], a[252:255], v[80:83], v[28:31]\n"
    "v_mfma_f32_16x16x32_bf16 v[16:19], a[96:99], v[80:83], v[16:19]\n"
    "v_mfma_f32_16x16x32_bf16 v[20:23], a[104:107], v[80:83], v[20:23]\n"
    "v_mfma_f32_16x16x32_bf16 v[24:27], v[192:195], v[80:83], v[24:27]\n"
    "v_mfma_f32_16x16x32_bf16 v[28:31], v[200:203], v[80:83], v[28:31]\n"
    "v_mfma_f32_16x16x32_bf16 v[16:19], a[100:103], v[80:83], v[16:19]\n"
    "v_mfma_f32_16x16x32_bf16 v[20:23], a[108:111], v[80:83], v[20:23]\n"
    "v_mfma_f32_16x16x32_bf16 v[24:27], v[196:199], v[80:83], v[24:27]\n"
    "v_mfma_f32_16x16x32_bf16 v[28:31], v[204:207], v[80:83], v[28:31]\n"
    "v_mfma_f32_16x16x32_bf16 v[16:19], a[112:115], v[80:83], v[16:19]\n"
    "v_mfma_f32_16x16x32_bf16 v[20:23], a[120:123], v[80:83], v[20:23]\n"
    "v_mfma_f32_16x16x32_bf16 v[24:27], v[208:211], v[80:83], v[24:27]\n"
    "v_mfma_f32_16x16x32_bf16 v[28:31], v[216:219], v[80:83], v[28:31]\n"
    "v_mfma_f32_16x16x32_bf16 v[16:19], a[116:119], v[80:83], v[16:19]\n"
    "v_mfma_f32_16x16x32_bf16 v[20:23], a[124:127], v[80:83], v[20:23]\n"
    "v_mfma_f32_16x16x32_bf16 v[24:27], v[212:215], v[80:83], v[24:27]\n"
    "v_mfma_f32_16x16x32_bf16 v[28:31], v[220:223], v[80:83], v[28:31]\n"
    "v_mfma_f32_16x16x32_bf16 v[16:19], a[128:131], v[80:83], v[16:19]\n"
    "v_mfma_f32_16x16x32_bf16 v[20:23], a[136:139], v[80:83], v[20:23]\n"
    "v_mfma_f32_16x16x32_bf16 v[24:27], v[224:227], v[80:83], v[24:27]\n"
    "v_mfma_f32_16x16x32_bf16 v[28:31], v[232:235], v[80:83], v[28:31]\n"
    "v_mfma_f32_16x16x32_bf16 v[16:19], a[132:135], v[80:83], v[16:19]\n"
    "v_mfma_f32_16x16x32_bf16 v[20:23], a[140:143], v[80:83], v[20:23]\n"
    "v_mfma_f32_16x16x32_bf16 v[24:27], v[228:231], v[80:83], v[24:27]\n"
    "v_mfma_f32_16x16x32_bf16 v[28:31], v[236:239], v[80:83], v[28:31]\n"
    "v_mfma_f32_16x16x32_bf16 v[16:19], a[144:147], v[80:83], v[16:19]\n"
    "v_mfma_f32_16x16x32_bf16 v[20:23], a[152:155], v[80:83], v[20:23]\n"
    "v_mfma_f32_16x16x32_bf16 v[24:27], v[240:243], v[80:83], v[24:27]\n"
    "v_mfma_f32_16x16x32_bf16 v[28:31], v[248:251], v[80:83], v[28:31]\n"
    "v_mfma_f32_16x16x32_bf16 v[16:19], a[148:151], v[80:83], v[16:19]\n"
    "v_mfma_f32_16x16x32_bf16 v[20:23], a[156:159], v[80:83], v[20:23]\n"
    "v_mfma_f32_16x16x32_bf16 v[24:27], v[244:247], v[80:83], v[24:27]\n"
    "v_mfma_f32_16x16x32_bf16 v[28:31], v[252:255], v[80:83], v[28:31]\n"
    "s_mov_b32 s71, 0\n"
    "s_mov_b32 s74, 0\n"
    "v_mov_b32 v14, v10\n"
    "v_mov_b32 v12, 0\n"
    "s_barrier\n"
    "v_mov_b32 v108, 0\n"
    "v_mov_b32 v109, 0\n"
    "v_mov_b32 v110, 0\n"
    "RU_loop_%=:\n"
    "s_cmp_lg_u32 s74, 0\n"
    "s_cbranch_scc1 RU_main_%=\n"
    "s_add_u32 s73, s43, 1\n"
    "s_mov_b32 s72, 0\n"
    "RU_pa_%=:\n"
    "buffer_load_dword v110, v5, s[36:39], s44 offen sc1\n"
    "s_waitcnt vmcnt(0)\n"
    "v_cmp_gt_u32 vcc, s73, v110\n"
    "s_cbranch_vccz RU_qa_%=\n"
    "s_nop 0\n"
    "s_ff1_i32_b64 s94, vcc\n"
    "s_lshl_b64 exec, 1, s94\n"
    "RU_ra_%=:\n"
    "s_cmp_lg_u32 s71, 0\n"
    "s_cbranch_scc1 RU_qa_%=\n"
    "s_sleep 4\n"
    "buffer_load_dword v110, v5, s[36:39], s44 offen sc1\n"
    "s_waitcnt vmcnt(0)\n"
    "v_cmp_gt_u32 vcc, s73, v110\n"
    "s_add_u32 s72, s72, 1\n"
    "s_cmp_lt_u32 s72, 0x40000\n"
    "s_cselect_b32 s71, s71, 1\n"
    "s_cbranch_vccnz RU_ra_%=\n"
    "s_mov_b64 exec, -1\n"
    "s_branch RU_pa_%=\n"
    "RU_qa_%=:\n"
    "s_mov_b64 exec, -1\n"
    "s_add_u32 s68, s43, 0\n"
    "s_lshr_b32 s76, s68, 5\n"
    "s_lshl_b32 s76, s76, 21\n"
    "s_lshl_b32 s68, s68, 16\n"
    "s_add_u32 s76, s76, s68\n"
    "s_add_u32 s76, s76, s41\n"
    "s_add_u32 s68, s76, 0\n"
    "s_add_u32 m0, s45, 0\n"
    "s_nop 0\n"
    "buffer_load_dwordx4 v0, s[36:39], s68 offen offset:0 sc1 lds\n"
    "buffer_load_dwordx4 v0, s[36:39], s68 offen offset:1024 sc1 lds\n"
    "buffer_load_dwordx4 v0, s[36:39], s68 offen offset:2048 sc1 lds\n"
    "buffer_load_dwordx4 v0, s[36:39], s68 offen offset:3072 sc1 lds\n"
    "s_add_u32 s68, s76, 4096\n"
    "s_add_u32 m0, s45, 4096\n"
    "s_nop 0\n"
    "buffer_load_dwordx4 v0, s[36:39], s68 offen offset:0 sc1 lds\n"
    "buffer_load_dwordx4 v0, s[36:39], s68 offen offset:1024 sc1 lds\n"
    "buffer_load_dwordx4 v0, s[36:39], s68 offen offset:2048 sc1 lds\n"
    "s_add_u32 s75, s44, 32768\n"
    "s_cmp_ge_u32 s75, 49152\n"
    "s_cselect_b32 s69, 49152, 0\n"
    "s_sub_u32 s75, s75, s69\n"
    "buffer_load_dword v108, v5, s[36:39], s75 offen sc1\n"
    "s_waitcnt vmcnt(0)\n"
    "RU_main_%=:\n"
    "s_barrier\n"
    "ds_read2st64_b64 v[48:51], v14 offset0:0 offset1:1\n"
    "ds_read2st64_b64 v[52:55], v14 offset0:2 offset1:3\n"
    "ds_read2st64_b64 v[56:59], v14 offset0:4 offset1:5\n"
    "s_add_u32 s73, s43, 3\n"
    "v_cmp_gt_u32 vcc, s73, v108\n"
    "s_xor_b32 s45, s45, 0x8000\n"
    "s_add_u32 s68, s43, 2\n"
    "s_lshr_b32 s76, s68, 5\n"
    "s_lshl_b32 s76, s76, 21\n"
    "s_lshl_b32 s68, s68, 16\n"
    "s_add_u32 s76, s76, s68\n"
    "s_add_u32 s76, s76, s41\n"
    "s_cmp_eq_u64 vcc, 0\n"
    "s_cselect_b32 s74, 1, 0\n"
    "s_cmp_ge_u32 s43, 126\n"
    "s_cselect_b32 s74, 0, s74\n"
    "s_waitcnt lgkmcnt(2)\n"
    "v_mfma_f32_16x16x32_bf16 v[32:35], a[0:3], v[48:51], 0\n"
    "ds_read2st64_b64 v[60:63], v14 offset0:6 offset1:7\n"
    "v_mfma_f32_16x16x32_bf16 v[36:39], a[8:11], v[48:51], 0\n"
    "ds_read2_b64 v[112:115], v9 offset0:0 offset1:16\n"
    "v_mfma_f32_16x16x32_bf16 v[40:43], a[160:163], v[48:51], 0\n"
    "s_cmp_eq_u32 s74, 0\n"
    "s_cbranch_scc1 RU_g612_%=\n"
    "s_add_u32 s68, s76, 0\n"
    "s_add_u32 m0, s45, 0\n"
    "s_nop 0\n"
    "buffer_load_dwordx4 v0, s[36:39], s68 offen offset:0 sc1 lds\n"
    "buffer_load_dwordx4 v0, s[36:39], s68 offen offset:1024 sc1 lds\n"
    "RU_g612_%=:\n"
    "v_mfma_f32_16x16x32_bf16 v[44:47], a[168:171], v[48:51], 0\n"
    "s_waitcnt lgkmcnt(3)\n"
    "v_mfma_f32_16x16x32_bf16 v[32:35], a[4:7], v[52:55], v[32:35]\n"
    "ds_read2st64_b64 v[64:67], v14 offset0:8 offset1:9\n"
    "v_mfma_f32_16x16x32_bf16 v[36:39], a[12:15], v[52:55], v[36:39]\n"
    "ds_read2_b64 v[116:119], v9 offset0:32 offset1:48\n"
    "s_add_u32 s44, s44, 32768\n"
    "s_cmp_ge_u32 s44, 49152\n"
    "s_cselect_b32 s69, 49152, 0\n"
    "s_sub_u32 s44, s44, s69\n"
    "v_mfma_f32_16x16x32_bf16 v[40:43], a[164:167], v[52:55], v[40:43]\n"
    "v_mfma_f32_16x16x32_bf16 v[44:47], a[172:175], v[52:55], v[44:47]\n"
    "s_waitcnt lgkmcnt(4)\n"
    "v_mfma_f32_16x16x32_bf16 v[32:35], a[16:19], v[56:59], v[32:35]\n"
    "ds_read2st64_b64 v[68:71], v14 offset0:10 offset1:11\n"
    "v_mfma_f32_16x16x32_bf16 v[36:39], a[24:27], v[56:59], v[36:39]\n"
    "ds_read2_b64 v[120:123], v9 offset0:64 offset1:80\n"
    "v_mfma_f32_16x16x32_bf16 v[40:43], a[176:179], v[56:59], v[40:43]\n"
    "s_cmp_eq_u32 s74, 0\n"
    "s_cbranch_scc1 RU_g638_%=\n"
    "buffer_load_dwordx4 v0, s[36:39], s68 offen offset:2048 sc1 lds\n"
    "buffer_load_dwordx4 v0, s[36:39], s68 offen offset:3072 sc1 lds\n"
    "RU_g638_%=:\n"
    "v_mfma_f32_16x16x32_bf16 v[44:47], a[184:187], v[56:59], v[44:47]\n"
    "s_waitcnt lgkmcnt(5)\n"
    "v_mfma_f32_16x16x32_bf16 v[32:35], a[20:23], v[60:63], v[32:35]\n"
    "ds_read2st64_b64 v[72:75], v14 offset0:12 offset1:13\n"
    "v_mfma_f32_16x16x32_bf16 v[36:39], a[28:31], v[60:63], v[36:39]\n"
    "ds_read2_b64 v[124:127], v9 offset0:96 offset1:112\n"
    "s_add_u32 s75, s44, 32768\n"
    "s_cmp_ge_u32 s75, 49152\n"
    "s_cselect_b32 s69, 49152, 0\n"
    "s_sub_u32 s75, s75, s69\n"
    "v_mfma_f32_16x16x32_bf16 v[40:43], a[180:183], v[60:63], v[40:43]\n"
    "v_mfma_f32_16x16x32_bf16 v[44:47], a[188:191], v[60:63], v[44:47]\n"
    "s_waitcnt lgkmcnt(5)\n"
    "v_mfma_f32_16x16x32_bf16 v[32:35], a[32:35], v[64:67], v[32:35]\n"
    "ds_read2st64_b64 v[76:79], v14 offset0:14 offset1:15\n"
    "v_mfma_f32_16x16x32_bf16 v[36:39], a[40:43], v[64:67], v[36:39]\n"
    "ds_read2_b64 v[128:131], v9 offset0:128 offset1:144\n"
    "v_mfma_f32_16x16x32_bf16 v[40:43], a[192:195], v[64:67], v[40:43]\n"
    "s_cmp_eq_u32 s74, 0\n"
    "s_cbranch_scc1 RU_g661_%=\n"
    "s_add_u32 s68, s76, 4096\n"
    "s_add_u32 m0, s45, 4096\n"
    "s_nop 0\n"
    "buffer_load_dwordx4 v0, s[36:39], s68 offen offset:0 sc1 lds\n"
    "buffer_load_dwordx4 v0, s[36:39], s68 offen offset:1024 sc1 lds\n"
    "buffer_load_dwordx4 v0, s[36:39], s68 offen offset:2048 sc1 lds\n"
    "RU_g661_%=:\n"
    "v_mfma_f32_16x16x32_bf16 v[44:47], a[200:203], v[64:67], v[44:47]\n"
    "s_waitcnt lgkmcnt(5)\n"
    "v_mfma_f32_16x16x32_bf16 v[32:35], a[36:39], v[68:71], v[32:35]\n"
    "ds_read2st64_b64 v[48:51], v14 offset0:16 offset1:17\n"
    "v_mfma_f32_16x16x32_bf16 v[36:39], a[44:47], v[68:71], v[36:39]\n"
    "s_add_u32 s73, s43, 5\n"
    "v_cmp_gt_u32 vcc, s73, v109\n"
    "v_mfma_f32_16x16x32_bf16 v[40:43], a[196:199], v[68:71], v[40:43]\n"
    "v_mfma_f32_16x16x32_bf16 v[44:47], a[204:207], v[68:71], v[44:47]\n"
    "s_waitcnt lgkmcnt(4)\n"
    "v_mfma_f32_16x16x32_bf16 v[32:35], a[48:51], v[72:75], v[32:35]\n"
    "ds_read2st64_b64 v[52:55], v14 offset0:18 offset1:19\n"
    "v_mfma_f32_16x16x32_bf16 v[36:39], a[56:59], v[72:75], v[36:39]\n"
    "v_mfma_f32_16x16x32_bf16 v[40:43], a[208:211], v[72:75], v[40:43]\n"
    "s_cbranch_vccz RU_nola_%=\n"
    "buffer_load_dword v109, v5, s[36:39], s75 offen sc1\n"
    "RU_nola_%=:\n"
    "v_mfma_f32_16x16x32_bf16 v[44:47], a[216:219], v[72:75], v[44:47]\n"
    "s_waitcnt lgkmcnt(3)\n"
    "v_mfma_f32_16x16x32_bf16 v[32:35], a[52:55], v[76:79], v[32:35]\n"
    "ds_read2st64_b64 v[56:59], v14 offset0:20 offset1:21\n"
    "v_mfma_f32_16x16x32_bf16 v[36:39], a[60:63], v[76:79], v[36:39]\n"
    "s_waitcnt lgkmcnt(3)\n"
    "v_add_f32 v112, v112, v114\n"
    "v_add_f32 v113, v113, v115\n"
    "v_mfma_f32_16x16x32_bf16 v[40:43], a[212:215], v[76:79], v[40:43]\n"
    "v_add_f32 v116, v116, v118\n"
    "v_add_f32 v117, v117, v119\n"
    "v_mfma_f32_16x16x32_bf16 v[44:47], a[220:223], v[76:79], v[44:47]\n"
    "v_add_f32 v120, v120, v122\n"
    "v_add_f32 v121, v121, v123\n"
    "s_waitcnt lgkmcnt(2)\n"
    "v_mfma_f32_16x16x32_bf16 v[32:35], a[64:67], v[48:51], v[32:35]\n"
    "ds_read2st64_b64 v[60:63], v14 offset0:22 offset1:23\n"
    "v_add_f32 v124, v124, v126\n"
    "v_mfma_f32_16x16x32_bf16 v[36:39], a[72:75], v[48:51], v[36:39]\n"
    "v_add_f32 v125, v125, v127\n"
    "v_add_f32 v128, v128, v130\n"
    "v_mfma_f32_16x16x32_bf16 v[40:43], a[224:227], v[48:51], v[40:43]\n"
    "v_add_f32 v129, v129, v131\n"
    "v_add_f32 v112, v112, v116\n"
    "v_mfma_f32_16x16x32_bf16 v[44:47], a[232:235], v[48:51], v[44:47]\n"
    "v_add_f32 v113, v113, v117\n"
    "v_add_f32 v120, v120, v124\n"
    "s_waitcnt lgkmcnt(2)\n"
    "v_mfma_f32_16x16x32_bf16 v[32:35], a[68:71], v[52:55], v[32:35]\n"
    "ds_read2st64_b64 v[64:67], v14 offset0:24 offset1:25\n"
    "v_add_f32 v121, v121, v125\n"
    "v_mfma_f32_16x16x32_bf16 v[36:39], a[76:79], v[52:55], v[36:39]\n"
    "v_add_f32 v112, v112, v120\n"
    "v_add_f32 v113, v113, v121\n"
    "v_mfma_f32_16x16x32_bf16 v[40:43], a[228:231], v[52:55], v[40:43]\n"
    "v_add_f32 v112, v112, v128\n"
    "v_add_f32 v113, v113, v129\n"
    "v_mfma_f32_16x16x32_bf16 v[44:47], a[236:239], v[52:55], v[44:47]\n"
    "v_mov_b32 v114, v112\n"
    "v_mov_b32 v115, v113\n"
    "s_waitcnt lgkmcnt(2)\n"
    "v_mfma_f32_16x16x32_bf16 v[32:35], a[80:83], v[56:59], v[32:35]\n"
    "ds_read2st64_b64 v[68:71], v14 offset0:26 offset1:27\n"
    "v_mfma_f32_16x16x32_bf16 v[36:39], a[88:91], v[56:59], v[36:39]\n"
    "v_permlane16_swap_b32 v112, v114\n"
    "v_mfma_f32_16x16x32_bf16 v[40:43], a[240:243], v[56:59], v[40:43]\n"
    "v_permlane16_swap_b32 v113, v115\n"
    "v_mfma_f32_16x16x32_bf16 v[44:47], a[248:251], v[56:59], v[44:47]\n"
    "v_add_f32 v112, v112, v114\n"
    "s_waitcnt lgkmcnt(2)\n"
    "v_mfma_f32_16x16x32_bf16 v[32:35], a[84:87], v[60:63], v[32:35]\n"
    "ds_read2st64_b64 v[72:75], v14 offset0:28 offset1:29\n"
    "v_add_f32 v113, v113, v115\n"
    "v_mfma_f32_16x16x32_bf16 v[36:39], a[92:95], v[60:63], v[36:39]\n"
    "v_mov_b32 v114, v112\n"
    "v_mov_b32 v115, v113\n"
    "v_mfma_f32_16x16x32_bf16 v[40:43], a[244:247], v[60:63], v[40:43]\n"
    "v_mfma_f32_16x16x32_bf16 v[44:47], a[252:255], v[60:63], v[44:47]\n"
    "v_permlane32_swap_b32 v112, v114\n"
    "s_waitcnt lgkmcnt(2)\n"
    "v_mfma_f32_16x16x32_bf16 v[32:35], a[96:99], v[64:67], v[32:35]\n"
    "ds_read2st64_b64 v[76:79], v14 offset0:30 offset1:31\n"
    "v_permlane32_swap_b32 v113, v115\n"
    "v_mfma_f32_16x16x32_bf16 v[36:39], a[104:107], v[64:67], v[36:39]\n"
    "v_add_f32 v112, v112, v114\n"
    "v_mfma_f32_16x16x32_bf16 v[40:43], v[192:195], v[64:67], v[40:43]\n"
    "v_add_f32 v113, v113, v115\n"
    "v_mul_f32 v112, 0x3acccccd, v112\n"
    "v_mfma_f32_16x16x32_bf16 v[44:47], v[200:203], v[64:67], v[44:47]\n"
    "v_mul_f32 v113, 0x3acccccd, v113\n"
    "v_fma_f32 v113, -v112, v112, v113\n"
    "s_waitcnt lgkmcnt(2)\n"
    "v_mfma_f32_16x16x32_bf16 v[32:35], a[100:103], v[68:71], v[32:35]\n"
    "ds_read2st64_b64 v[48:51], v14 offset0:32 offset1:33\n"
    "v_max_f32 v113, 0, v113\n"
    "v_mfma_f32_16x16x32_bf16 v[36:39], a[108:111], v[68:71], v[36:39]\n"
    "v_add_f32 v113, 0x3727c5ac, v113\n"
    "v_rsq_f32 v113, v113\n"
    "v_mfma_f32_16x16x32_bf16 v[40:43], v[196:199], v[68:71], v[40:43]\n"
    "v_mfma_f32_16x16x32_bf16 v[44:47], v[204:207], v[68:71], v[44:47]\n"
    "s_waitcnt lgkmcnt(2)\n"
    "v_mfma_f32_16x16x32_bf16 v[32:35], a[112:115], v[72:75], v[32:35]\n"
    "ds_read2st64_b64 v[52:55], v14 offset0:34 offset1:35\n"
    "v_mfma_f32_16x16x32_bf16 v[36:39], a[120:123], v[72:75], v[36:39]\n"
    "v_mfma_f32_16x16x32_bf16 v[40:43], v[208:211], v[72:75], v[40:43]\n"
    "v_mfma_f32_16x16x32_bf16 v[44:47], v[216:219], v[72:75], v[44:47]\n"
    "s_waitcnt lgkmcnt(2)\n"
    "v_mfma_f32_16x16x32_bf16 v[32:35], a[116:119], v[76:79], v[32:35]\n"
    "ds_read2st64_b64 v[56:59], v14 offset0:36 offset1:37\n"
    "s_add_u32 s70, s43, 0\n"
    "v_mfma_f32_16x16x32_bf16 v[36:39], a[124:127], v[76:79], v[36:39]\n"
    "s_lshr_b32 s77, s70, 4\n"
    "v_mfma_f32_16x16x32_bf16 v[40:43], v[212:215], v[76:79], v[40:43]\n"
    "s_lshl_b32 s77, s77, 21\n"
    "v_mfma_f32_16x16x32_bf16 v[44:47], v[220:223], v[76:79], v[44:47]\n"
    "s_lshl_b32 s70, s70, 17\n"
    "s_waitcnt lgkmcnt(2)\n"
    "v_mfma_f32_16x16x32_bf16 v[32:35], a[128:131], v[48:51], v[32:35]\n"
    "ds_read2st64_b64 v[60:63], v14 offset0:38 offset1:39\n"
    "s_add_u32 s77, s77, s70\n"
    "v_mfma_f32_16x16x32_bf16 v[36:39], a[136:139], v[48:51], v[36:39]\n"
    "s_add_u32 s77, s77, s42\n"
    "v_mfma_f32_16x16x32_bf16 v[40:43], v[224:227], v[48:51], v[40:43]\n"
    "v_mfma_f32_16x16x32_bf16 v[44:47], v[232:235], v[48:51], v[44:47]\n"
    "s_waitcnt lgkmcnt(2)\n"
    "v_mfma_f32_16x16x32_bf16 v[32:35], a[132:135], v[52:55], v[32:35]\n"
    "v_mfma_f32_16x16x32_bf16 v[36:39], a[140:143], v[52:55], v[36:39]\n"
    "v_mfma_f32_16x16x32_bf16 v[40:43], v[228:231], v[52:55], v[40:43]\n"
    "v_mfma_f32_16x16x32_bf16 v[44:47], v[236:239], v[52:55], v[44:47]\n"
    "s_waitcnt lgkmcnt(1)\n"
    "v_mfma_f32_16x16x32_bf16 v[32:35], a[144:147], v[56:59], v[32:35]\n"
    "v_mfma_f32_16x16x32_bf16 v[36:39], a[152:155], v[56:59], v[36:39]\n"
    "v_mfma_f32_16x16x32_bf16 v[40:43], v[240:243], v[56:59], v[40:43]\n"
    "v_mfma_f32_16x16x32_bf16 v[44:47], v[248:251], v[56:59], v[44:47]\n"
    "s_waitcnt lgkmcnt(0)\n"
    "v_mfma_f32_16x16x32_bf16 v[32:35], a[148:151], v[60:63], v[32:35]\n"
    "v_mfma_f32_16x16x32_bf16 v[36:39], a[156:159], v[60:63], v[36:39]\n"
    "v_mfma_f32_16x16x32_bf16 v[40:43], v[244:247], v[60:63], v[40:43]\n"
    "v_mfma_f32_16x16x32_bf16 v[44:47], v[252:255], v[60:63], v[44:47]\n"
    "s_nop 1\n"
    "v_fma_f32 v32, -v112, v16, v32\n"
    "v_fma_f32 v33, -v112, v17, v33\n"
    "v_fma_f32 v34, -v112, v18, v34\n"
    "v_fma_f32 v35, -v112, v19, v35\n"
    "v_fma_f32 v36, -v112, v20, v36\n"
    "v_fma_f32 v37, -v112, v21, v37\n"
    "v_fma_f32 v38, -v112, v22, v38\n"
    "v_fma_f32 v39, -v112, v23, v39\n"
    "v_fma_f32 v40, -v112, v24, v40\n"
    "v_fma_f32 v41, -v112, v25, v41\n"
    "v_fma_f32 v42, -v112, v26, v42\n"
    "v_fma_f32 v43, -v112, v27, v43\n"
    "v_fma_f32 v44, -v112, v28, v44\n"
    "v_fma_f32 v45, -v112, v29, v45\n"
    "v_fma_f32 v46, -v112, v30, v46\n"
    "v_fma_f32 v47, -v112, v31, v47\n"
    "v_pk_mul_f32 v[32:33], v[32:33], v[112:113] op_sel:[0,1] op_sel_hi:[1,1]\n"
    "v_pk_mul_f32 v[34:35], v[34:35], v[112:113] op_sel:[0,1] op_sel_hi:[1,1]\n"
    "v_pk_mul_f32 v[36:37], v[36:37], v[112:113] op_sel:[0,1] op_sel_hi:[1,1]\n"
    "v_pk_mul_f32 v[38:39], v[38:39], v[112:113] op_sel:[0,1] op_sel_hi:[1,1]\n"
    "v_pk_mul_f32 v[40:41], v[40:41], v[112:113] op_sel:[0,1] op_sel_hi:[1,1]\n"
    "v_pk_mul_f32 v[42:43], v[42:43], v[112:113] op_sel:[0,1] op_sel_hi:[1,1]\n"
    "v_pk_mul_f32 v[44:45], v[44:45], v[112:113] op_sel:[0,1] op_sel_hi:[1,1]\n"
    "v_pk_mul_f32 v[46:47], v[46:47], v[112:113] op_sel:[0,1] op_sel_hi:[1,1]\n"
    "v_mul_f32 v114, 0xbfb8aa3b, v32\n"
    "v_mul_f32 v115, 0xbfb8aa3b, v33\n"
    "v_mul_f32 v116, 0xbfb8aa3b, v34\n"
    "v_mul_f32 v117, 0xbfb8aa3b, v35\n"
    "v_mul_f32 v118, 0xbfb8aa3b, v36\n"
    "v_mul_f32 v119, 0xbfb8aa3b, v37\n"
    "v_mul_f32 v120, 0xbfb8aa3b, v38\n"
    "v_mul_f32 v121, 0xbfb8aa3b, v39\n"
    "v_exp_f32 v114, v114\n"
    "v_exp_f32 v115, v115\n"
    "v_exp_f32 v116, v116\n"
    "v_exp_f32 v117, v117\n"
    "v_exp_f32 v118, v118\n"
    "v_exp_f32 v119, v119\n"
    "v_exp_f32 v120, v120\n"
    "v_exp_f32 v121, v121\n"
    "v_pk_mul_f32 v[32:33], v[32:33], v[40:41]\n"
    "v_pk_mul_f32 v[34:35], v[34:35], v[42:43]\n"
    "v_pk_mul_f32 v[36:37], v[36:37], v[44:45]\n"
    "v_pk_mul_f32 v[38:39], v[38:39], v[46:47]\n"
    "v_pk_add_f32 v[114:115], v[114:115], 1.0 op_sel_hi:[1,0]\n"
    "v_pk_add_f32 v[116:117], v[116:117], 1.0 op_sel_hi:[1,0]\n"
    "v_pk_add_f32 v[118:119], v[118:119], 1.0 op_sel_hi:[1,0]\n"
    "v_pk_add_f32 v[120:121], v[120:121], 1.0 op_sel_hi:[1,0]\n"
    "v_rcp_f32 v114, v114\n"
    "v_rcp_f32 v115, v115\n"
    "v_rcp_f32 v116, v116\n"
    "v_rcp_f32 v117, v117\n"
    "v_rcp_f32 v118, v118\n"
    "v_rcp_f32 v119, v119\n"
    "v_rcp_f32 v120, v120\n"
    "v_rcp_f32 v121, v121\n"
    "v_pk_mul_f32 v[32:33], v[32:33], v[114:115]\n"
    "v_pk_mul_f32 v[34:35], v[34:35], v[116:117]\n"
    "v_pk_mul_f32 v[36:37], v[36:37], v[118:119]\n"
    "v_pk_mul_f32 v[38:39], v[38:39], v[120:121]\n"
    "v_cvt_pk_bf16_f32 v124, v32, v33\n"
    "v_cvt_pk_bf16_f32 v125, v34, v35\n"
    "v_cvt_pk_bf16_f32 v126, v36, v37\n"
    "v_cvt_pk_bf16_f32 v127, v38, v39\n"
    "s_waitcnt vmcnt(0)\n"
    "buffer_store_dword v12, v4, s[36:39], 0 offen sc1\n"
    "buffer_store_dwordx4 v[124:127], v6, s[36:39], s77 offen sc1\n"
    "s_lshr_b32 s68, s43, 1\n"
    "s_add_u32 s68, s68, 1\n"
    "v_mov_b32 v12, s68\n"
    "v_xor_b32 v9, 0x8000, v9\n"
    "v_xor_b32 v14, 0x8000, v14\n"
    "v_mov_b32 v13, v110\n"
    "v_mov_b32 v110, v108\n"
    "v_mov_b32 v108, v109\n"
    "v_mov_b32 v109, v13\n"
    "s_cmp_lg_u32 s74, 0\n"
    "s_cbranch_scc1 RU_skip_%=\n"
    "s_waitcnt vmcnt(0)\n"
    "buffer_store_dword v12, v4, s[36:39], 0 offen sc1\n"
    "RU_skip_%=:\n"
    "s_add_u32 s43, s43, 2\n"
    "s_cmp_lt_u32 s43, 128\n"
    "s_cbranch_scc1 RU_loop_%=\n"
    "s_waitcnt vmcnt(0)\n"
    "s_endpgm\n"
            :: "{v[0:15]}"(C), "{s[36:51]}"(SA), "{s[52:67]}"(SB) : CLOB_W);
        __builtin_unreachable();
    }
}

extern "C" void kernel_launch(void* const* d_in, const int* in_sizes, int n_in, void* d_out, int out_size, void* d_ws, size_t ws_size, hipStream_t stream) {
    Params p;
    p.x = (const u16*)d_in[0]; p.mem0 = (const u16*)d_in[1];
    p.Wq = (const u16*)d_in[2]; p.Wk = (const u16*)d_in[3]; p.Wv = (const u16*)d_in[4]; p.Wo = (const u16*)d_in[5];
    p.w1 = (const u16*)d_in[8]; p.w3 = (const u16*)d_in[9]; p.w2 = (const u16*)d_in[10];
    p.out = (u16*)d_out; p.ws = (char*)d_ws;
    p.k1 = K1C; p.k2 = K2C; p.c1 = C1C; p.pad = 0;
    hipFuncSetAttribute((const void*)k_main, hipFuncAttributeMaxDynamicSharedMemorySize, L_END);
    k_main<<<256, 256, L_END, stream>>>(p);
}
```
